```python
import torch

def _softmax(x, axis=-1):
    return torch.softmax(x, dim=axis)
def _gelu(x):
    return torch.nn.functional.gelu(x, approximate="tanh")
def _silu(x):
    return torch.nn.functional.silu(x)
def _sigmoid(x):
    return torch.sigmoid(x)
def _layernorm(x, axis=-1, eps=1e-5):
    m = x.mean(dim=axis, keepdim=True)
    v = x.var(dim=axis, keepdim=True, unbiased=False)
    return (x - m) / torch.sqrt(v + eps)
def _scatter_add(tgt, idx, src, axis=0):
    return tgt.clone().index_add_(axis, idx, src)
def _segment_sum(src, seg, n):
    out = torch.zeros((n,) + src.shape[1:], dtype=src.dtype)
    return out.index_add_(0, seg, src)
def _pad(x, pw):
    flat = tuple(v for pair in reversed(pw) for v in pair)
    return torch.nn.functional.pad(x, flat)

kernel_name = 'standalone__gqa_softmax__near_ridge'

def setup_inputs():

    _w = (lambda a: (lambda t: t.to(torch.bfloat16) if t.is_floating_point() else t)(torch.as_tensor(a)))
    B, Hq, Hkv, Sq, Sk, D = 32, 32, 16, 384, 512, 128
    return {
        "q": _w((lambda *s: torch.randn(*s, dtype=torch.bfloat16) * 0.8)(B, Hq, Sq, D)),
        "k": _w((lambda *s: torch.randn(*s, dtype=torch.bfloat16) * 0.8)(B, Hkv, Sk, D)),
        "v": _w((lambda *s: torch.randn(*s, dtype=torch.bfloat16) * 0.05)(B, Hkv, Sk, D)),
    }

def reference(q, k, v):
    B, Hq, Sq, D = q.shape
    Hkv = k.shape[1]
    rep = Hq // Hkv
    k = (lambda x, n, axis: torch.repeat_interleave(x, n, dim=axis))(k, rep, axis=1)
    v = (lambda x, n, axis: torch.repeat_interleave(x, n, dim=axis))(v, rep, axis=1)
    scale = D ** -0.5
    scores = torch.einsum("bhqd,bhkd->bhqk", q, k) * scale
    p = _softmax(scores, axis=-1)
    return torch.einsum("bhqk,bhkd->bhqd", p, v)
```

```cpp
#include <hip/hip_runtime.h>
#include <hip/hip_bf16.h>
using bf16 = __hip_bfloat16;
constexpr int   D = 128, NW = 8, QBLK = 32, KVBLK = 64;
constexpr float SCALE = 0.088388347648318440f;
constexpr float THR = 8.f;
constexpr int SDEPTH = 2;
constexpr int LDQ = D, LDK = D, LDO = D;
constexpr size_t SHM_V = KVBLK * D * 2, SHM_K = KVBLK * D * 2, SHM_ATTN = 2 * SHM_V + 2 * SHM_K + NW * 64 * 4;
using bf16x8 = __attribute__((ext_vector_type(8))) short;
using s16x4  = __attribute__((ext_vector_type(4))) short;
using f32x16 = __attribute__((ext_vector_type(16))) float;
using u32x4  = __attribute__((ext_vector_type(4))) unsigned;
#define KSWZ(row, colB) ((row) * 256 + ((colB) ^ (((row) & 7) << 4)))
#define SBAR() __builtin_amdgcn_sched_barrier(0)
__device__ __forceinline__ int crow(int r, int hi) { return (r & 3) + 8 * (r >> 2) + 4 * hi; }
__device__ __forceinline__ unsigned cvtpk(float lo, float hi) {
  unsigned r; asm volatile("v_cvt_pk_bf16_f32 %0, %1, %2" : "=v"(r) : "v"(lo), "v"(hi)); return r;
}
__device__ __forceinline__ bf16x8 ld8(const bf16* p) { return *reinterpret_cast<const bf16x8*>(p); }
__device__ __forceinline__ void partialSM(f32x16& p0, f32x16& p1, float& m_reg, float& mn, float& alpha) {
  constexpr float C = SCALE * 1.4426950408889634f;
  float pmax = p0[0]; for (int r = 1; r < 16; ++r) pmax = fmaxf(pmax, p0[r]); for (int r = 0; r < 16; ++r) pmax = fmaxf(pmax, p1[r]);
  { auto rr = __builtin_amdgcn_permlane32_swap(__float_as_uint(pmax), __float_as_uint(pmax), false, false);
    pmax = fmaxf(__uint_as_float(rr[0]), __uint_as_float(rr[1])); }
  if (__builtin_expect(__all(pmax - m_reg <= THR / SCALE), 1)) { mn = m_reg; alpha = 1.f; }
  else { mn = fmaxf(m_reg, pmax); alpha = __builtin_amdgcn_exp2f((m_reg - mn) * C); m_reg = mn; }
  float mnC = -mn * C;
  for (int r = 0; r < 16; ++r) p0[r] = fmaf(p0[r], C, mnC); for (int r = 0; r < 16; ++r) p1[r] = fmaf(p1[r], C, mnC);
  for (int r = 0; r < 16; ++r) p0[r] = __builtin_amdgcn_exp2f(p0[r]);
}
__device__ __forceinline__ void finishSM(f32x16& p0, f32x16& p1, float alpha, float& l_reg, bf16x8& pa0, bf16x8& pa1, bf16x8& pa2, bf16x8& pa3) {
  for (int r = 0; r < 16; ++r) p1[r] = __builtin_amdgcn_exp2f(p1[r]);
  float ps = 0; for (int r = 0; r < 16; ++r) ps += p0[r]; for (int r = 0; r < 16; ++r) ps += p1[r];
  { auto rr = __builtin_amdgcn_permlane32_swap(__float_as_uint(ps), __float_as_uint(ps), false, false);
    ps = __uint_as_float(rr[0]) + __uint_as_float(rr[1]); }
  l_reg = l_reg * alpha + ps;
#define PK4(P, BASE, OUT) do { unsigned a0 = cvtpk(P[BASE + 0], P[BASE + 1]), a1 = cvtpk(P[BASE + 2], P[BASE + 3]);   \
    unsigned b0 = cvtpk(P[BASE + 4], P[BASE + 5]), b1 = cvtpk(P[BASE + 6], P[BASE + 7]);                              \
    auto r0 = __builtin_amdgcn_permlane32_swap(a0, b0, false, false); auto r1 = __builtin_amdgcn_permlane32_swap(a1, b1, false, false); \
    u32x4 w = {r0[0], r1[0], r0[1], r1[1]}; OUT = *reinterpret_cast<bf16x8*>(&w); } while (0)
  PK4(p0, 0, pa0); PK4(p0, 8, pa1); PK4(p1, 0, pa2); PK4(p1, 8, pa3);
#undef PK4
}
__device__ __forceinline__ void qkt(f32x16& p0, f32x16& p1, const bf16* Ks, const bf16x8* qr, int r32, int hi) {
  p0 = f32x16{}; p1 = f32x16{};
  for (int d0 = 0; d0 < 8; ++d0) { int cb = (d0 * 16 + hi * 8) * 2;
    bf16x8 b0 = *reinterpret_cast<const bf16x8*>((const char*)Ks + KSWZ(r32, cb));
    bf16x8 b1 = *reinterpret_cast<const bf16x8*>((const char*)Ks + KSWZ(32 + r32, cb));
    p0 = __builtin_amdgcn_mfma_f32_32x32x16_bf16(b0, qr[d0], p0, 0, 0, 0);
    p1 = __builtin_amdgcn_mfma_f32_32x32x16_bf16(b1, qr[d0], p1, 0, 0, 0); }
}
__device__ __forceinline__ int v_st(int k, int c) { const int kk = (k & ~0xC) | ((k & 4) << 1) | ((k & 8) >> 1); return ((kk >> 3) * 4 + (c >> 5)) * 512 + ((kk & 7) * 32 + (c & 31)) * 2; }
__device__ __forceinline__ int v_rd_base(int lane) { return ((lane & 3) << 3) | (((lane >> 2) & 3) << 6) | (((lane >> 4) & 1) << 5) | (((lane >> 5) & 1) << 8); }
constexpr int v_rd_off(int d0, int ks, int half) { return d0 * 512 + ks * 4096 + half * 2048; }
template <int OFF> __device__ __forceinline__ s16x4 tr_read(int vb) {
  s16x4 r; asm volatile("ds_read_b64_tr_b16 %0, %1 offset:%2" : "=&v"(r) : "v"(vb), "i"(OFF) : "memory"); return r;
}
template <int D0> __device__ __forceinline__ void pv_one(f32x16& od, int vb, bf16x8 pa0, bf16x8 pa1, bf16x8 pa2, bf16x8 pa3) {
  const s16x4 l0 = tr_read<v_rd_off(D0, 0, 0)>(vb), h0 = tr_read<v_rd_off(D0, 0, 1)>(vb), l1 = tr_read<v_rd_off(D0, 1, 0)>(vb), h1 = tr_read<v_rd_off(D0, 1, 1)>(vb);
  const s16x4 l2 = tr_read<v_rd_off(D0, 2, 0)>(vb), h2 = tr_read<v_rd_off(D0, 2, 1)>(vb), l3 = tr_read<v_rd_off(D0, 3, 0)>(vb), h3 = tr_read<v_rd_off(D0, 3, 1)>(vb);
  asm volatile("s_waitcnt lgkmcnt(0)" ::: "memory"); SBAR();
#define PK(L, H) (bf16x8){L[0], L[1], L[2], L[3], H[0], H[1], H[2], H[3]}
  od = __builtin_amdgcn_mfma_f32_32x32x16_bf16(pa0, PK(l0, h0), od, 0, 0, 0);
  od = __builtin_amdgcn_mfma_f32_32x32x16_bf16(pa1, PK(l1, h1), od, 0, 0, 0);
  od = __builtin_amdgcn_mfma_f32_32x32x16_bf16(pa2, PK(l2, h2), od, 0, 0, 0);
  od = __builtin_amdgcn_mfma_f32_32x32x16_bf16(pa3, PK(l3, h3), od, 0, 0, 0);
#undef PK
}
__device__ __forceinline__ void pv_d0(f32x16* o, int vb, bf16x8 pa0, bf16x8 pa1, bf16x8 pa2, bf16x8 pa3) {
  pv_one<0>(o[0], vb, pa0, pa1, pa2, pa3); pv_one<1>(o[1], vb, pa0, pa1, pa2, pa3); pv_one<2>(o[2], vb, pa0, pa1, pa2, pa3); pv_one<3>(o[3], vb, pa0, pa1, pa2, pa3);
}
__device__ __forceinline__ void attn_dense_body(const bf16* __restrict__ Qb, const bf16* __restrict__ Kh, const bf16* __restrict__ Vh,
                                                bf16* __restrict__ Ob, int seq, char* lds) {
  const int tid = threadIdx.x, wid = tid >> 6, lane = tid & 63, r32 = lane & 31, hi = lane >> 5;
  bf16* V_lds = (bf16*)lds; bf16* K_lds = (bf16*)(lds + 2 * SHM_V);
  float* ws = (float*)(lds + 2 * SHM_V + 2 * SHM_K) + wid * 64; float* li_l = ws; float* al_l = ws + 32;
  float m_reg = -1e30f, l_reg = 0; f32x16 o[4] = {}; bf16x8 qr[8];
  const bf16* Qw = Qb + (long)(wid * QBLK + r32) * LDQ + hi * 8;
#pragma unroll
  for (int d0 = 0; d0 < 8; ++d0) qr[d0] = ld8(Qw + d0 * 16);
  const int sr = tid >> 4, sc = (tid & 15) * 8, vst0 = v_st(sr, sc), vst1 = v_st(32 + sr, sc);
  const int vb0 = (int)(uintptr_t)V_lds + v_rd_base(lane);
  struct { bf16x8 vs0, vs1, ks0, ks1; } sr_[SDEPTH];
#define SLOAD(i, k0) do { sr_[i].vs0 = ld8(&Vh[(long)((k0) + sr) * LDK + sc]); sr_[i].vs1 = ld8(&Vh[(long)((k0) + 32 + sr) * LDK + sc]); \
    sr_[i].ks0 = ld8(&Kh[(long)((k0) + sr) * LDK + sc]); sr_[i].ks1 = ld8(&Kh[(long)((k0) + 32 + sr) * LDK + sc]); } while (0)
#define SWRITE(b, i) do { *(bf16x8*)((char*)V_lds + (b) * SHM_V + vst0) = (sr_[i].vs0);          \
    *(bf16x8*)((char*)V_lds + (b) * SHM_V + vst1) = (sr_[i].vs1); int kc = sc * 2;               \
    *(bf16x8*)((char*)K_lds + (b) * SHM_K + KSWZ(sr, kc)) = (sr_[i].ks0);                       \
    *(bf16x8*)((char*)K_lds + (b) * SHM_K + KSWZ(32 + sr, kc)) = (sr_[i].ks1); } while (0)
#define SWAIT() do { asm volatile("s_waitcnt vmcnt(4)" ::: "memory"); } while (0)
#define RESC(a) do { if (__any((a) < 1.f)) { if (hi == 0) al_l[r32] = (a); asm volatile("s_waitcnt lgkmcnt(0)" ::: "memory"); \
    for (int d = 0; d < 4; ++d) for (int r = 0; r < 16; ++r) o[d][r] *= al_l[crow(r, hi)]; } } while (0)
  f32x16 pA0, pA1, pB0, pB1; float mnA, mnB, alA, alB; bf16x8 pa0, pa1, pa2, pa3; const int NT = seq / KVBLK;
  constexpr int SE = 0, SO = SDEPTH - 1;
  SLOAD(SE, 0); asm volatile("s_waitcnt vmcnt(0)" ::: "memory"); SWRITE(0, SE); __syncthreads();
  qkt(pA0, pA1, K_lds, qr, r32, hi); partialSM(pA0, pA1, m_reg, mnA, alA);
  SLOAD(SO, KVBLK); if (2 < NT) SLOAD(SE, 2 * KVBLK);
  SWAIT(); SWRITE(1, SO); __syncthreads();
  for (int j = 1; j + 1 < NT; j += 2) {
    SBAR(); qkt(pB0, pB1, (bf16*)((char*)K_lds + SHM_K), qr, r32, hi);
    finishSM(pA0, pA1, alA, l_reg, pa0, pa1, pa2, pa3); SBAR();
    SLOAD(SO, (j + SDEPTH) * KVBLK); SBAR();
    pv_d0(o, vb0, pa0, pa1, pa2, pa3); partialSM(pB0, pB1, m_reg, mnB, alB);
    __syncthreads(); SWAIT(); SWRITE(0, SE);
    RESC(alB); __syncthreads();
    SBAR(); qkt(pA0, pA1, K_lds, qr, r32, hi);
    finishSM(pB0, pB1, alB, l_reg, pa0, pa1, pa2, pa3); SBAR();
    if (j + 3 < NT) SLOAD(SE, (j + 1 + SDEPTH) * KVBLK); SBAR();
    pv_d0(o, vb0 + (int)SHM_V, pa0, pa1, pa2, pa3); partialSM(pA0, pA1, m_reg, mnA, alA);
    __syncthreads(); SWAIT(); SWRITE(1, SO);
    RESC(alA); __syncthreads();
  }
  SBAR(); qkt(pB0, pB1, (bf16*)((char*)K_lds + SHM_K), qr, r32, hi);
  finishSM(pA0, pA1, alA, l_reg, pa0, pa1, pa2, pa3); SBAR();
  pv_d0(o, vb0, pa0, pa1, pa2, pa3); partialSM(pB0, pB1, m_reg, mnB, alB);
  __syncthreads(); RESC(alB);
  finishSM(pB0, pB1, alB, l_reg, pa0, pa1, pa2, pa3); SBAR();
  pv_d0(o, vb0 + (int)SHM_V, pa0, pa1, pa2, pa3);
  if (hi == 0) li_l[r32] = l_reg; asm volatile("s_waitcnt lgkmcnt(0)" ::: "memory");
  float rli[16];
#pragma unroll
  for (int r = 0; r < 16; ++r) rli[r] = __builtin_amdgcn_rcpf(li_l[crow(r, hi)]);
  bf16* Ow = Ob + (long)(wid * QBLK) * LDO;
#pragma unroll
  for (int r = 0; r < 16; ++r) { int orow = crow(r, hi);
    for (int d0 = 0; d0 < 4; ++d0) Ow[(long)orow * LDO + d0 * 32 + r32] = __float2bfloat16(o[d0][r] * rli[r]); }
}
__global__ __launch_bounds__(64 * NW, 1)
void attn_fwd_dense(const bf16* __restrict__ Q, const bf16* __restrict__ K, const bf16* __restrict__ V, bf16* __restrict__ O) {
  extern __shared__ __attribute__((aligned(16))) char shm[];
  int id = blockIdx.x; int x = id & 7, j = id >> 3; int lin = x * 192 + j;
  long head = lin / 3, qb = lin % 3;
  const long q0 = (head * 768 + qb * 256) * D, k0 = head * 512 * (long)D;
  attn_dense_body(Q + q0, K + k0, V + k0, O + q0, 512, shm);
}
extern "C" void kernel_launch(void* const* d_in, const int* in_sizes, int n_in, void* d_out, int out_size, void* d_ws, size_t ws_size, hipStream_t stream) {
  hipFuncSetAttribute((const void*)attn_fwd_dense, hipFuncAttributeMaxDynamicSharedMemorySize, (int)SHM_ATTN);
  hipLaunchKernelGGL(attn_fwd_dense, dim3(1536), dim3(64 * NW), SHM_ATTN, stream,
                     (const bf16*)d_in[0], (const bf16*)d_in[1], (const bf16*)d_in[2], (bf16*)d_out);
}
```

```cpp
#include <hip/hip_runtime.h>
#include <hip/hip_bf16.h>

typedef __bf16   bf16x8 __attribute__((ext_vector_type(8)));
typedef __bf16   bf16x2 __attribute__((ext_vector_type(2)));
typedef float    f32x4  __attribute__((ext_vector_type(4)));
typedef float    f32x2  __attribute__((ext_vector_type(2)));
typedef unsigned u32x4  __attribute__((ext_vector_type(4)));
typedef unsigned u32x2  __attribute__((ext_vector_type(2)));
#define DI __device__ __forceinline__

DI unsigned cvtpk(float lo, float hi) { f32x2 v = {lo, hi}; bf16x2 b = __builtin_convertvector(v, bf16x2); return __builtin_bit_cast(unsigned, b); }
DI f32x4 qk16(const char* kp, const bf16x8 (&qf)[4]) {
  f32x4 s = {0.f, 0.f, 0.f, 0.f};
#pragma unroll
  for (int ks = 0; ks < 4; ++ks) s = __builtin_amdgcn_mfma_f32_16x16x32_bf16(*(const bf16x8*)(kp + 64 * ks), qf[ks], s, 0, 0, 0);
  return s;
}
DI void slow_rows(const char* Q, const char* K, const char* V, char* O, char* stage, unsigned lane) {
  const float C = 0.12751743082f;
  const unsigned i = lane & 15u, g = lane >> 4;
  bf16x8 qf[4];
#pragma unroll
  for (int ks = 0; ks < 4; ++ks) qf[ks] = *(const bf16x8*)(Q + i * 256u + 64 * ks + 16u * g);
  const char* kl = K + i * 256u + 16u * g;
  float m = -__builtin_inff();
#pragma unroll 2
  for (int kb = 0; kb < 32; ++kb) { const f32x4 s = qk16(kl + kb * 4096, qf); m = fmaxf(fmaxf(m, fmaxf(s[0], s[1])), fmaxf(s[2], s[3])); }
  m = fmaxf(m, __shfl_xor(m, 16)); m = fmaxf(m, __shfl_xor(m, 32));
  f32x4 o[8];
#pragma unroll
  for (int d = 0; d < 8; ++d) o[d] = f32x4{0.f, 0.f, 0.f, 0.f};
  float l = 0.f;
  const unsigned vsrc = ((lane >> 2) & 3u) * 256u + g * 64u + (lane & 3u) * 16u;
  const unsigned vrd = (unsigned)(size_t)stage + g * 1024u + i * 16u;
#pragma unroll 1
  for (int t = 0; t < 16; ++t) {
#pragma unroll
    for (int pc = 0; pc < 8; ++pc) *(u32x4*)(stage + pc * 1024 + lane * 16u) = *(const u32x4*)(V + t * 8192 + pc * 1024 + vsrc);
    const f32x4 s0 = qk16(kl + t * 8192, qf), s1 = qk16(kl + t * 8192 + 4096, qf);
    float p[8];
#pragma unroll
    for (int r = 0; r < 4; ++r) { p[r] = __builtin_amdgcn_exp2f((s0[r] - m) * C); p[4 + r] = __builtin_amdgcn_exp2f((s1[r] - m) * C); }
#pragma unroll
    for (int r = 0; r < 8; ++r) l += p[r];
    const u32x4 pw = {cvtpk(p[0], p[1]), cvtpk(p[2], p[3]), cvtpk(p[4], p[5]), cvtpk(p[6], p[7])};
#pragma unroll
    for (int d = 0; d < 8; ++d) {
      u32x2 lo, hi;
      asm volatile("s_waitcnt lgkmcnt(0)\n\tds_read_b64_tr_b16 %0, %2 offset:%3\n\tds_read_b64_tr_b16 %1, %2 offset:%4\n\ts_waitcnt lgkmcnt(0)"
                   : "=&v"(lo), "=&v"(hi) : "v"(vrd), "i"((d >> 1) * 256 + (d & 1) * 8), "i"(4096 + (d >> 1) * 256 + (d & 1) * 8) : "memory");
      const u32x4 vw = {lo[0], lo[1], hi[0], hi[1]};
      o[d] = __builtin_amdgcn_mfma_f32_16x16x32_bf16(__builtin_bit_cast(bf16x8, vw), __builtin_bit_cast(bf16x8, pw), o[d], 0, 0, 0);
    }
  }
  l += __shfl_xor(l, 16); l += __shfl_xor(l, 32);
  const float rl = 1.0f / l;
#pragma unroll
  for (int j = 0; j < 4; ++j) {
    const f32x4 a = o[2 * j] * rl, b = o[2 * j + 1] * rl;
    const u32x4 w = {cvtpk(a[0], a[1]), cvtpk(a[2], a[3]), cvtpk(b[0], b[1]), cvtpk(b[2], b[3])};
    *(u32x4*)(O + i * 256u + 64 * j + 16u * g) = w;
  }
}

__global__ __launch_bounds__(256, 1) void gqa_attn_fwd(const char* __restrict__ Q, const char* __restrict__ K, const char* __restrict__ V, char* __restrict__ O
) {
  extern __shared__ __attribute__((aligned(16))) char lds[];
  const unsigned wv = __builtin_amdgcn_readfirstlane(threadIdx.x >> 6), wg = blockIdx.x;
  const unsigned ldsb = __builtin_amdgcn_readfirstlane((unsigned)(size_t)lds);
  unsigned bad;
  asm volatile(
"s_load_dwordx8 s[40:47], %[ka], 0x0\n"
"v_mbcnt_lo_u32_b32 v8, -1, 0\n"
"s_and_b32 s76, %[wg], 7\n"
"v_mbcnt_hi_u32_b32 v8, -1, v8\n"
"s_lshl_b32 s76, s76, 6\n"
"s_lshr_b32 s81, %[wg], 3\n"
"s_lshr_b32 s74, s76, 4\n"
"s_lshr_b32 s74, 0x30524130, s74\n"
"s_and_b32 s74, s74, 7\n"
"s_lshl_b32 s74, s74, 5\n"
"s_add_u32 s60, s81, s74\n"
"s_mov_b32 s80, 32\n"
"s_lshl_b32 s78, %[wv], 14\n"
"v_bfe_u32 v0, v8, 2, 2\n"
"v_lshrrev_b32 v1, 4, v8\n"
"v_and_b32 v2, 3, v8\n"
"v_lshlrev_b32 v3, 8, v0\n"
"v_lshl_add_u32 v3, v1, 6, v3\n"
"v_lshl_add_u32 v219, v2, 4, v3\n"
"v_xor_b32 v220, 32, v219\n"
"s_mul_i32 s74, s76, 3\n"
"s_add_u32 s74, s74, s60\n"
"s_lshl_b32 s74, s74, 16\n"
"s_add_u32 s61, s74, s78\n"
"s_add_u32 s74, s60, %[wv]\n"
"s_and_b32 s74, s74, 3\n"
"s_lshl_b32 s99, s74, 12\n"
"s_lshl_b32 s74, %[wv], 13\n"
"s_add_u32 s66, %[lds], s74\n"
"s_add_u32 s66, s66, 131072\n"
"s_getpc_b64 s[96:97]\n"
".Lattn_pc:\n"
"s_lshl_b32 s72, s81, 2\n"
"s_add_u32 s72, s72, %[wv]\n"
"s_mul_i32 s72, s72, 544\n"
"s_min_u32 s72, s72, .Lattn_end-.Lattn_pc-1024\n"
"s_add_u32 s96, s96, s72\n"
"s_addc_u32 s97, s97, 0\n"
"global_load_dword a214, v219, s[96:97]\n"
"s_lshl_b32 s77, %[wv], 12\n"
"s_mul_i32 s74, s60, 43691\n"
"s_lshr_b32 s74, s74, 17\n"
"s_add_u32 s74, s74, s76\n"
"s_lshl_b32 s74, s74, 17\n"
"s_add_u32 s79, s74, s77\n"
"s_mov_b32 s38, 0x76543210\n"
"s_lshr_b32 s74, s76, 6\n"
"s_mul_i32 s74, s74, 0x11111111\n"
"s_add_u32 s38, s38, s74\n"
"s_add_u32 s64, %[lds], s77\n"
"s_xor_b32 s70, s99, 4096\n"
"s_xor_b32 s71, s99, 8192\n"
"s_xor_b32 s37, s99, 12288\n"
"s_mov_b32 s54, s79\n"
"s_add_u32 s65, s64, 65536\n"
"v_and_b32 v4, 2, v0\n"
"v_xor_b32 v4, v4, v1\n"
"v_lshlrev_b32 v4, 4, v4\n"
"v_lshl_add_u32 v4, v2, 6, v4\n"
"v_lshl_add_u32 v4, v0, 10, v4\n"
"v_add_u32 v216, %[lds], v4\n"
"v_add_u32 v218, s66, v4\n"
"v_add_u32 v6, s65, v4\n"
"v_add_u32 v7, s64, v4\n"
"v_and_b32 v5, 15, v8\n"
"v_lshlrev_b32 v4, 4, v5\n"
"v_lshl_add_u32 v4, v1, 10, v4\n"
"v_add_u32 v4, %[lds], v4\n"
"v_add_u32 v217, 65536, v4\n"
"v_and_b32 v4, 7, v5\n"
"v_xor_b32 v4, v4, v1\n"
"v_lshlrev_b32 v4, 4, v4\n"
"v_lshl_add_u32 v4, v5, 7, v4\n"
"v_add_u32 v224, s64, v4\n"
"v_xor_b32 v4, 64, v4\n"
"v_add_u32 v225, s64, v4\n"
"v_lshlrev_b32 v4, 4, v8\n"
"v_add_u32 v226, s64, v4\n"
"v_lshrrev_b32 v4, 3, v8\n"
"v_xor_b32 v5, v4, v8\n"
"v_and_b32 v5, 7, v5\n"
"v_lshlrev_b32 v5, 4, v5\n"
"v_lshl_add_u32 v223, v4, 8, v5\n"
"v_mov_b32 v209, 0x3f800000\n"
"v_mov_b32 v211, 0x3f800000\n"
"v_mov_b32 v4, 0x3f803f80\n"
"v_cmp_gt_u32 vcc, 2, v2\n"
"s_nop 3\n"
"v_cndmask_b32 v4, 0, v4, vcc\n"
"v_cmp_eq_u32 vcc, 1, v2\n"
"v_mov_b32 v5, 0x44444444\n"
"v_mov_b32 v3, 0xeeeeeeee\n"
"s_nop 1\n"
"v_cndmask_b32 v215, v5, v3, vcc\n"
"v_accvgpr_write_b32 a192, 0\n"
"v_accvgpr_write_b32 a193, 0\n"
"v_accvgpr_write_b32 a194, 0\n"
"v_accvgpr_write_b32 a195, 0\n"
"v_accvgpr_write_b32 a196, 0\n"
"v_accvgpr_write_b32 a197, 0\n"
"v_accvgpr_write_b32 a198, 0\n"
"v_accvgpr_write_b32 a199, 0\n"
"v_accvgpr_write_b32 a200, 0\n"
"v_accvgpr_write_b32 a201, 0\n"
"v_accvgpr_write_b32 a202, 0\n"
"v_accvgpr_write_b32 a203, 0\n"
"v_accvgpr_write_b32 a204, 0\n"
"v_accvgpr_write_b32 a205, 0\n"
"v_accvgpr_write_b32 a206, 0\n"
"v_accvgpr_write_b32 a207, 0\n"
"v_accvgpr_write_b32 a208, v4\n"
"v_accvgpr_write_b32 a209, v4\n"
"v_accvgpr_write_b32 a210, v4\n"
"v_accvgpr_write_b32 a211, v4\n"
"s_mov_b32 s68, 0x3e0293ee\n"
"s_mov_b32 s69, 0x3e0293ee\n"
"s_mov_b32 s67, 0xffff0000\n"
"s_mov_b32 s58, 0\n"
"s_mov_b32 s59, 0x20000\n"
"s_mov_b32 s72, 3\n"
"s_branch .Lattn_tabe\n"
".Lattn_ret:\n"
"s_setpc_b64 s[86:87]\n"
".Lattn_tab:\n"
"s_branch .Lattn_pad0\n"
"s_branch .Lattn_pad7\n"
"s_branch .Lattn_pad8\n"
"s_branch .Lattn_pad9\n"
"s_branch .Lattn_pad10\n"
"s_branch .Lattn_pad11\n"
"s_branch .Lattn_pad1\n"
"s_branch .Lattn_pad2\n"
"s_branch .Lattn_pad3\n"
"s_branch .Lattn_pad4\n"
"s_branch .Lattn_pad5\n"
"s_branch .Lattn_pad6\n"
"s_branch .Lattn_pad12\n"
"s_branch .Lattn_pad13\n"
"s_branch .Lattn_pad14\n"
"s_branch .Lattn_pad15\n"
"s_branch .Lattn_ret\n"
"s_branch .Lattn_ret\n"
"s_branch .Lattn_ret\n"
"s_branch .Lattn_ret\n"
"s_branch .Lattn_ret\n"
"s_branch .Lattn_ret\n"
"s_branch .Lattn_ret\n"
"s_branch .Lattn_ret\n"
"s_branch .Lattn_ret\n"
"s_branch .Lattn_ret\n"
"s_branch .Lattn_ret\n"
"s_branch .Lattn_ret\n"
"s_branch .Lattn_ret\n"
"s_branch .Lattn_ret\n"
"s_branch .Lattn_ret\n"
"s_branch .Lattn_ret\n"
".Lattn_tabe:\n"
"s_getpc_b64 s[84:85]\n"
".Lattn_hpc:\n"
"s_lshl_b32 s86, %[wv], 2\n"
"s_add_u32 s84, s84, s86\n"
"s_sub_u32 s84, s84, .Lattn_hpc-.Lattn_tab\n"
"s_waitcnt lgkmcnt(0)\n"
"s_getreg_b32 s82, hwreg(HW_REG_XCC_ID, 0, 1)\n"
"s_cmp_lg_u32 s82, 0\n"
"s_cbranch_scc1 .Lattn_go\n"
"v_mov_b32 v9, s61\n"
"v_mov_b32 v10, s79\n"
"global_load_dword v11, v9, s[40:41]\n"
"global_load_dword v12, v10, s[42:43]\n"
"global_load_dword v13, v10, s[44:45]\n"
"s_waitcnt vmcnt(0)\n"
"s_memrealtime s[88:89]\n"
"s_mov_b64 vcc, -1\n"
"s_swappc_b64 s[86:87], s[84:85]\n"
"s_add_u32 s84, s84, 16\n"
"s_swappc_b64 s[86:87], s[84:85]\n"
"s_add_u32 s84, s84, 16\n"
"s_swappc_b64 s[86:87], s[84:85]\n"
"s_add_u32 s84, s84, 16\n"
"s_swappc_b64 s[86:87], s[84:85]\n"
"s_add_u32 s84, s84, 16\n"
"s_mov_b64 vcc, 0\n"
".Lattn_dl:\n"
"s_sleep 2\n"
"s_memrealtime s[86:87]\n"
"s_waitcnt lgkmcnt(0)\n"
"s_sub_u32 s86, s86, s88\n"
"s_cmp_lt_u32 s86, 180\n"
"s_cbranch_scc1 .Lattn_dl\n"
".Lattn_go:\n"
"s_add_u32 m0, s66, 0\n"
"s_mov_b32 s55, s61\n"
"s_add_u32 s35, s55, s99\n"
"s_add_u32 s52, s40, s35\n"
"s_addc_u32 s53, s41, 0\n"
"global_load_lds_dwordx4 v219, s[52:53] offset:0\n"
"global_load_lds_dwordx4 v219, s[52:53] offset:1024\n"
"global_load_lds_dwordx4 v220, s[52:53] offset:2048\n"
"global_load_lds_dwordx4 v220, s[52:53] offset:3072\n"
"s_add_u32 m0, s64, 0\n"
"s_bfe_u32 s48, s38, 0x30000\n"
"s_lshl_b32 s48, s48, 14\n"
"s_add_u32 s36, s79, s48\n"
"s_add_u32 s48, s42, s36\n"
"s_addc_u32 s49, s43, 0\n"
"global_load_lds_dwordx4 v219, s[48:49] offset:0\n"
"global_load_lds_dwordx4 v219, s[48:49] offset:1024\n"
"global_load_lds_dwordx4 v220, s[48:49] offset:2048\n"
"global_load_lds_dwordx4 v220, s[48:49] offset:3072\n"
"s_add_u32 m0, s66, 4096\n"
"s_add_u32 s35, s55, s70\n"
"s_add_u32 s52, s40, s35\n"
"s_addc_u32 s53, s41, 0\n"
"global_load_lds_dwordx4 v219, s[52:53] offset:0\n"
"global_load_lds_dwordx4 v219, s[52:53] offset:1024\n"
"global_load_lds_dwordx4 v220, s[52:53] offset:2048\n"
"global_load_lds_dwordx4 v220, s[52:53] offset:3072\n"
"s_mov_b32 s56, s46\n"
"s_mov_b32 s57, s47\n"
"s_add_u32 s74, s60, 32\n"
"s_sub_u32 s60, s60, 160\n"
"s_cselect_b32 s60, s74, s60\n"
"s_lshr_b32 s80, s80, 1\n"
"s_cselect_b32 s73, -1, 0\n"
"s_mul_i32 s74, s76, 3\n"
"s_add_u32 s74, s74, s60\n"
"s_lshl_b32 s74, s74, 16\n"
"s_add_u32 s62, s74, s78\n"
"s_mul_i32 s74, s60, 43691\n"
"s_lshr_b32 s74, s74, 17\n"
"s_add_u32 s74, s74, s76\n"
"s_lshl_b32 s74, s74, 17\n"
"s_add_u32 s63, s74, s77\n"
"s_mov_b64 vcc, -1\n"
"s_swappc_b64 s[86:87], s[84:85]\n"
"s_add_u32 s84, s84, 16\n"
"s_swappc_b64 s[86:87], s[84:85]\n"
"s_add_u32 s84, s84, 16\n"
"s_mov_b64 vcc, 0\n"
"s_waitcnt vmcnt(8)\n"
"ds_read_b128 v[64:67], v218 offset:0\n"
"ds_read_b128 v[68:71], v218 offset:256\n"
"ds_read_b128 v[72:75], v218 offset:512\n"
"ds_read_b128 v[76:79], v218 offset:768\n"
"s_add_u32 m0, s65, 49152\n"
"s_add_u32 s35, s55, s71\n"
"s_add_u32 s52, s40, s35\n"
"s_addc_u32 s53, s41, 0\n"
"global_load_lds_dwordx4 v219, s[52:53] offset:0\n"
"global_load_lds_dwordx4 v219, s[52:53] offset:1024\n"
"global_load_lds_dwordx4 v220, s[52:53] offset:2048\n"
"global_load_lds_dwordx4 v220, s[52:53] offset:3072\n"
"s_waitcnt lgkmcnt(3)\n"
"v_lshlrev_b32 v228, 16, v64\n"
"v_and_b32 v229, s67, v64\n"
"v_lshlrev_b32 v230, 16, v65\n"
"v_and_b32 v231, s67, v65\n"
"v_lshlrev_b32 v232, 16, v66\n"
"v_and_b32 v233, s67, v66\n"
"v_lshlrev_b32 v234, 16, v67\n"
"v_and_b32 v235, s67, v67\n"
"v_pk_mul_f32 v[228:229], v[228:229], s[68:69]\n"
"v_pk_mul_f32 v[230:231], v[230:231], s[68:69]\n"
"v_pk_mul_f32 v[232:233], v[232:233], s[68:69]\n"
"v_pk_mul_f32 v[234:235], v[234:235], s[68:69]\n"
"v_cvt_pk_bf16_f32 v228, v228, v229\n"
"v_cvt_pk_bf16_f32 v230, v230, v231\n"
"v_cvt_pk_bf16_f32 v232, v232, v233\n"
"v_cvt_pk_bf16_f32 v234, v234, v235\n"
"v_accvgpr_write_b32 a0, v228\n"
"v_accvgpr_write_b32 a1, v230\n"
"v_accvgpr_write_b32 a2, v232\n"
"v_accvgpr_write_b32 a3, v234\n"
"s_waitcnt vmcnt(8)\n"
"s_barrier\n"
"ds_read_b128 a[64:67], v216 offset:0\n"
"ds_read_b128 a[80:83], v216 offset:4096\n"
"ds_read_b128 a[96:99], v216 offset:8192\n"
"ds_read_b128 a[112:115], v216 offset:12288\n"
"ds_read_b128 a[68:71], v216 offset:256\n"
"ds_read_b128 a[84:87], v216 offset:4352\n"
"ds_read_b128 a[100:103], v216 offset:8448\n"
"ds_read_b128 a[116:119], v216 offset:12544\n"
"ds_read_b128 a[72:75], v216 offset:512\n"
"ds_read_b128 a[88:91], v216 offset:4608\n"
"ds_read_b128 a[104:107], v216 offset:8704\n"
"ds_read_b128 a[120:123], v216 offset:12800\n"
"ds_read_b128 a[76:79], v216 offset:768\n"
"ds_read_b128 a[92:95], v216 offset:4864\n"
"ds_read_b128 a[108:111], v216 offset:8960\n"
"ds_read_b128 a[124:127], v216 offset:13056\n"
"s_add_u32 m0, s64, 49152\n"
"s_add_u32 s35, s55, s37\n"
"s_add_u32 s52, s40, s35\n"
"s_addc_u32 s53, s41, 0\n"
"global_load_lds_dwordx4 v219, s[52:53] offset:0\n"
"global_load_lds_dwordx4 v219, s[52:53] offset:1024\n"
"global_load_lds_dwordx4 v220, s[52:53] offset:2048\n"
"global_load_lds_dwordx4 v220, s[52:53] offset:3072\n"
"s_waitcnt lgkmcnt(15)\n"
"v_mfma_f32_16x16x32_bf16 v[32:35], a[64:67], a[0:3], -4.0\n"
"v_lshlrev_b32 v228, 16, v68\n"
"v_and_b32 v229, s67, v68\n"
"s_waitcnt lgkmcnt(14)\n"
"v_mfma_f32_16x16x32_bf16 v[36:39], a[80:83], a[0:3], -4.0\n"
"v_lshlrev_b32 v230, 16, v69\n"
"v_and_b32 v231, s67, v69\n"
"s_waitcnt lgkmcnt(13)\n"
"v_mfma_f32_16x16x32_bf16 v[40:43], a[96:99], a[0:3], -4.0\n"
"v_lshlrev_b32 v232, 16, v70\n"
"v_and_b32 v233, s67, v70\n"
"s_waitcnt lgkmcnt(11)\n"
"v_mfma_f32_16x16x32_bf16 v[44:47], a[112:115], a[0:3], -4.0\n"
"v_lshlrev_b32 v234, 16, v71\n"
"v_and_b32 v235, s67, v71\n"
"v_mul_f32 v228, s68, v228\n"
"v_mul_f32 v229, s68, v229\n"
"v_mul_f32 v230, s68, v230\n"
"v_mul_f32 v231, s68, v231\n"
"v_pk_mul_f32 v[232:233], v[232:233], s[68:69]\n"
"v_pk_mul_f32 v[234:235], v[234:235], s[68:69]\n"
"v_cvt_pk_bf16_f32 v228, v228, v229\n"
"v_cvt_pk_bf16_f32 v230, v230, v231\n"
"v_cvt_pk_bf16_f32 v232, v232, v233\n"
"v_cvt_pk_bf16_f32 v234, v234, v235\n"
"v_accvgpr_write_b32 a4, v228\n"
"v_accvgpr_write_b32 a5, v230\n"
"v_accvgpr_write_b32 a6, v232\n"
"v_accvgpr_write_b32 a7, v234\n"
"s_nop 1\n"
"v_mfma_f32_16x16x32_bf16 v[32:35], a[68:71], a[4:7], v[32:35]\n"
"v_lshlrev_b32 v228, 16, v72\n"
"v_and_b32 v229, s67, v72\n"
"s_waitcnt lgkmcnt(0)\n"
"v_mfma_f32_16x16x32_bf16 v[36:39], a[84:87], a[4:7], v[36:39]\n"
"v_lshlrev_b32 v230, 16, v73\n"
"v_and_b32 v231, s67, v73\n"
"v_mfma_f32_16x16x32_bf16 v[40:43], a[100:103], a[4:7], v[40:43]\n"
"v_lshlrev_b32 v232, 16, v74\n"
"v_and_b32 v233, s67, v74\n"
"v_mfma_f32_16x16x32_bf16 v[44:47], a[116:119], a[4:7], v[44:47]\n"
"v_lshlrev_b32 v234, 16, v75\n"
"v_and_b32 v235, s67, v75\n"
"v_mul_f32 v228, s68, v228\n"
"v_mul_f32 v229, s68, v229\n"
"v_mul_f32 v230, s68, v230\n"
"v_mul_f32 v231, s68, v231\n"
"v_pk_mul_f32 v[232:233], v[232:233], s[68:69]\n"
"v_pk_mul_f32 v[234:235], v[234:235], s[68:69]\n"
"v_cvt_pk_bf16_f32 v228, v228, v229\n"
"v_cvt_pk_bf16_f32 v230, v230, v231\n"
"v_cvt_pk_bf16_f32 v232, v232, v233\n"
"v_cvt_pk_bf16_f32 v234, v234, v235\n"
"v_accvgpr_write_b32 a8, v228\n"
"v_accvgpr_write_b32 a9, v230\n"
"v_accvgpr_write_b32 a10, v232\n"
"v_accvgpr_write_b32 a11, v234\n"
"s_waitcnt vmcnt(8)\n"
"ds_read_b128 v[16:19], v218 offset:4096\n"
"ds_read_b128 v[20:23], v218 offset:4352\n"
"ds_read_b128 v[24:27], v218 offset:4608\n"
"ds_read_b128 v[28:31], v218 offset:4864\n"
"s_add_u32 m0, s64, 16384\n"
"s_bfe_u32 s48, s38, 0x30004\n"
"s_lshl_b32 s48, s48, 14\n"
"s_add_u32 s36, s79, s48\n"
"s_add_u32 s48, s42, s36\n"
"s_addc_u32 s49, s43, 0\n"
"global_load_lds_dwordx4 v219, s[48:49] offset:0\n"
"global_load_lds_dwordx4 v219, s[48:49] offset:1024\n"
"global_load_lds_dwordx4 v220, s[48:49] offset:2048\n"
"global_load_lds_dwordx4 v220, s[48:49] offset:3072\n"
"v_mfma_f32_16x16x32_bf16 v[32:35], a[72:75], a[8:11], v[32:35]\n"
"v_lshlrev_b32 v228, 16, v76\n"
"v_and_b32 v229, s67, v76\n"
"v_mfma_f32_16x16x32_bf16 v[36:39], a[88:91], a[8:11], v[36:39]\n"
"v_lshlrev_b32 v230, 16, v77\n"
"v_and_b32 v231, s67, v77\n"
"v_mfma_f32_16x16x32_bf16 v[40:43], a[104:107], a[8:11], v[40:43]\n"
"v_lshlrev_b32 v232, 16, v78\n"
"v_and_b32 v233, s67, v78\n"
"v_mfma_f32_16x16x32_bf16 v[44:47], a[120:123], a[8:11], v[44:47]\n"
"v_lshlrev_b32 v234, 16, v79\n"
"v_and_b32 v235, s67, v79\n"
"v_mul_f32 v228, s68, v228\n"
"v_mul_f32 v229, s68, v229\n"
"v_mul_f32 v230, s68, v230\n"
"v_mul_f32 v231, s68, v231\n"
"v_pk_mul_f32 v[232:233], v[232:233], s[68:69]\n"
"v_pk_mul_f32 v[234:235], v[234:235], s[68:69]\n"
"v_cvt_pk_bf16_f32 v228, v228, v229\n"
"v_cvt_pk_bf16_f32 v230, v230, v231\n"
"v_cvt_pk_bf16_f32 v232, v232, v233\n"
"v_cvt_pk_bf16_f32 v234, v234, v235\n"
"v_accvgpr_write_b32 a12, v228\n"
"v_accvgpr_write_b32 a13, v230\n"
"v_accvgpr_write_b32 a14, v232\n"
"v_accvgpr_write_b32 a15, v234\n"
"s_nop 1\n"
"v_mfma_f32_16x16x32_bf16 v[32:35], a[76:79], a[12:15], v[32:35]\n"
"s_waitcnt lgkmcnt(0)\n"
"v_lshlrev_b32 v228, 16, v16\n"
"v_and_b32 v229, s67, v16\n"
"v_mfma_f32_16x16x32_bf16 v[36:39], a[92:95], a[12:15], v[36:39]\n"
"v_lshlrev_b32 v230, 16, v17\n"
"v_and_b32 v231, s67, v17\n"
"v_mfma_f32_16x16x32_bf16 v[40:43], a[108:111], a[12:15], v[40:43]\n"
"v_lshlrev_b32 v232, 16, v18\n"
"v_and_b32 v233, s67, v18\n"
"v_mfma_f32_16x16x32_bf16 v[44:47], a[124:127], a[12:15], v[44:47]\n"
"v_lshlrev_b32 v234, 16, v19\n"
"v_and_b32 v235, s67, v19\n"
"v_mul_f32 v228, s68, v228\n"
"v_mul_f32 v229, s68, v229\n"
"v_mul_f32 v230, s68, v230\n"
"v_mul_f32 v231, s68, v231\n"
"v_pk_mul_f32 v[232:233], v[232:233], s[68:69]\n"
"v_pk_mul_f32 v[234:235], v[234:235], s[68:69]\n"
"v_cvt_pk_bf16_f32 v228, v228, v229\n"
"v_cvt_pk_bf16_f32 v230, v230, v231\n"
"v_cvt_pk_bf16_f32 v232, v232, v233\n"
"v_cvt_pk_bf16_f32 v234, v234, v235\n"
"v_accvgpr_write_b32 a16, v228\n"
"v_accvgpr_write_b32 a17, v230\n"
"v_accvgpr_write_b32 a18, v232\n"
"v_accvgpr_write_b32 a19, v234\n"
"s_nop 1\n"
"v_mfma_f32_16x16x32_bf16 v[48:51], a[64:67], a[16:19], -4.0\n"
"v_lshlrev_b32 v228, 16, v20\n"
"v_and_b32 v229, s67, v20\n"
"v_mfma_f32_16x16x32_bf16 v[52:55], a[80:83], a[16:19], -4.0\n"
"v_lshlrev_b32 v230, 16, v21\n"
"v_and_b32 v231, s67, v21\n"
"v_mfma_f32_16x16x32_bf16 v[56:59], a[96:99], a[16:19], -4.0\n"
"v_lshlrev_b32 v232, 16, v22\n"
"v_and_b32 v233, s67, v22\n"
"v_mfma_f32_16x16x32_bf16 v[60:63], a[112:115], a[16:19], -4.0\n"
"v_lshlrev_b32 v234, 16, v23\n"
"v_and_b32 v235, s67, v23\n"
"v_exp_f32 v32, v32\n"
"v_exp_f32 v33, v33\n"
"s_nop 0\n"
"v_cvt_pk_bf16_f32 v128, v32, v33\n"
"v_exp_f32 v34, v34\n"
"v_exp_f32 v35, v35\n"
"s_nop 0\n"
"v_cvt_pk_bf16_f32 v129, v34, v35\n"
"v_exp_f32 v36, v36\n"
"v_exp_f32 v37, v37\n"
"v_pk_mul_f32 v[228:229], v[228:229], s[68:69]\n"
"v_pk_mul_f32 v[230:231], v[230:231], s[68:69]\n"
"v_pk_mul_f32 v[232:233], v[232:233], s[68:69]\n"
"v_pk_mul_f32 v[234:235], v[234:235], s[68:69]\n"
"v_cvt_pk_bf16_f32 v228, v228, v229\n"
"v_cvt_pk_bf16_f32 v230, v230, v231\n"
"v_cvt_pk_bf16_f32 v232, v232, v233\n"
"v_cvt_pk_bf16_f32 v234, v234, v235\n"
"v_accvgpr_write_b32 a20, v228\n"
"v_accvgpr_write_b32 a21, v230\n"
"v_accvgpr_write_b32 a22, v232\n"
"v_accvgpr_write_b32 a23, v234\n"
"s_nop 1\n"
"v_mfma_f32_16x16x32_bf16 v[48:51], a[68:71], a[20:23], v[48:51]\n"
"v_lshlrev_b32 v228, 16, v24\n"
"v_and_b32 v229, s67, v24\n"
"v_mfma_f32_16x16x32_bf16 v[52:55], a[84:87], a[20:23], v[52:55]\n"
"v_lshlrev_b32 v230, 16, v25\n"
"v_and_b32 v231, s67, v25\n"
"v_mfma_f32_16x16x32_bf16 v[56:59], a[100:103], a[20:23], v[56:59]\n"
"v_lshlrev_b32 v232, 16, v26\n"
"v_and_b32 v233, s67, v26\n"
"v_mfma_f32_16x16x32_bf16 v[60:63], a[116:119], a[20:23], v[60:63]\n"
"v_lshlrev_b32 v234, 16, v27\n"
"v_and_b32 v235, s67, v27\n"
"v_cvt_pk_bf16_f32 v130, v36, v37\n"
"v_exp_f32 v38, v38\n"
"v_exp_f32 v39, v39\n"
"s_nop 0\n"
"v_cvt_pk_bf16_f32 v131, v38, v39\n"
"v_exp_f32 v40, v40\n"
"v_exp_f32 v41, v41\n"
"s_nop 0\n"
"v_cvt_pk_bf16_f32 v132, v40, v41\n"
"v_exp_f32 v42, v42\n"
"v_exp_f32 v43, v43\n"
"v_pk_mul_f32 v[228:229], v[228:229], s[68:69]\n"
"v_pk_mul_f32 v[230:231], v[230:231], s[68:69]\n"
"v_pk_mul_f32 v[232:233], v[232:233], s[68:69]\n"
"v_pk_mul_f32 v[234:235], v[234:235], s[68:69]\n"
"v_cvt_pk_bf16_f32 v228, v228, v229\n"
"v_cvt_pk_bf16_f32 v230, v230, v231\n"
"v_cvt_pk_bf16_f32 v232, v232, v233\n"
"v_cvt_pk_bf16_f32 v234, v234, v235\n"
"v_accvgpr_write_b32 a24, v228\n"
"v_accvgpr_write_b32 a25, v230\n"
"v_accvgpr_write_b32 a26, v232\n"
"v_accvgpr_write_b32 a27, v234\n"
"s_waitcnt vmcnt(8)\n"
"ds_read_b128 v[64:67], v6 offset:49152\n"
"ds_read_b128 v[68:71], v6 offset:49408\n"
"ds_read_b128 v[72:75], v6 offset:49664\n"
"ds_read_b128 v[76:79], v6 offset:49920\n"
"s_add_u32 m0, s65, 0\n"
"s_bfe_u32 s50, s38, 0x30000\n"
"s_lshl_b32 s50, s50, 14\n"
"s_add_u32 s39, s54, s50\n"
"s_add_u32 s50, s44, s39\n"
"s_addc_u32 s51, s45, 0\n"
"global_load_lds_dwordx4 v219, s[50:51] offset:0\n"
"global_load_lds_dwordx4 v219, s[50:51] offset:1024\n"
"global_load_lds_dwordx4 v219, s[50:51] offset:2048\n"
"global_load_lds_dwordx4 v219, s[50:51] offset:3072\n"
"v_mfma_f32_16x16x32_bf16 v[48:51], a[72:75], a[24:27], v[48:51]\n"
"v_lshlrev_b32 v228, 16, v28\n"
"v_and_b32 v229, s67, v28\n"
"v_mfma_f32_16x16x32_bf16 v[52:55], a[88:91], a[24:27], v[52:55]\n"
"v_lshlrev_b32 v230, 16, v29\n"
"v_and_b32 v231, s67, v29\n"
"v_mfma_f32_16x16x32_bf16 v[56:59], a[104:107], a[24:27], v[56:59]\n"
"v_lshlrev_b32 v232, 16, v30\n"
"v_and_b32 v233, s67, v30\n"
"v_mfma_f32_16x16x32_bf16 v[60:63], a[120:123], a[24:27], v[60:63]\n"
"v_lshlrev_b32 v234, 16, v31\n"
"v_and_b32 v235, s67, v31\n"
"v_cvt_pk_bf16_f32 v133, v42, v43\n"
"v_exp_f32 v44, v44\n"
"v_exp_f32 v45, v45\n"
"s_nop 0\n"
"v_cvt_pk_bf16_f32 v134, v44, v45\n"
"v_exp_f32 v46, v46\n"
"v_exp_f32 v47, v47\n"
"s_nop 0\n"
"v_cvt_pk_bf16_f32 v135, v46, v47\n"
"v_pk_mul_f32 v[228:229], v[228:229], s[68:69]\n"
"v_pk_mul_f32 v[230:231], v[230:231], s[68:69]\n"
"v_pk_mul_f32 v[232:233], v[232:233], s[68:69]\n"
"v_pk_mul_f32 v[234:235], v[234:235], s[68:69]\n"
"v_cvt_pk_bf16_f32 v228, v228, v229\n"
"v_cvt_pk_bf16_f32 v230, v230, v231\n"
"v_cvt_pk_bf16_f32 v232, v232, v233\n"
"v_cvt_pk_bf16_f32 v234, v234, v235\n"
"v_accvgpr_write_b32 a28, v228\n"
"v_accvgpr_write_b32 a29, v230\n"
"v_accvgpr_write_b32 a30, v232\n"
"v_accvgpr_write_b32 a31, v234\n"
"s_nop 1\n"
"v_mfma_f32_16x16x32_bf16 v[48:51], a[76:79], a[28:31], v[48:51]\n"
"s_waitcnt lgkmcnt(0)\n"
"v_lshlrev_b32 v228, 16, v64\n"
"v_and_b32 v229, s67, v64\n"
"v_mfma_f32_16x16x32_bf16 v[52:55], a[92:95], a[28:31], v[52:55]\n"
"v_lshlrev_b32 v230, 16, v65\n"
"v_and_b32 v231, s67, v65\n"
"v_mfma_f32_16x16x32_bf16 v[56:59], a[108:111], a[28:31], v[56:59]\n"
"v_lshlrev_b32 v232, 16, v66\n"
"v_and_b32 v233, s67, v66\n"
"v_mfma_f32_16x16x32_bf16 v[60:63], a[124:127], a[28:31], v[60:63]\n"
"v_lshlrev_b32 v234, 16, v67\n"
"v_and_b32 v235, s67, v67\n"
"v_mul_f32 v228, s68, v228\n"
"v_mul_f32 v229, s68, v229\n"
"v_mul_f32 v230, s68, v230\n"
"v_mul_f32 v231, s68, v231\n"
"v_pk_mul_f32 v[232:233], v[232:233], s[68:69]\n"
"v_pk_mul_f32 v[234:235], v[234:235], s[68:69]\n"
"v_cvt_pk_bf16_f32 v228, v228, v229\n"
"v_cvt_pk_bf16_f32 v230, v230, v231\n"
"v_cvt_pk_bf16_f32 v232, v232, v233\n"
"v_cvt_pk_bf16_f32 v234, v234, v235\n"
"v_accvgpr_write_b32 a32, v228\n"
"v_accvgpr_write_b32 a33, v230\n"
"v_accvgpr_write_b32 a34, v232\n"
"v_accvgpr_write_b32 a35, v234\n"
"s_nop 1\n"
"v_mfma_f32_16x16x32_bf16 v[32:35], a[64:67], a[32:35], -4.0\n"
"v_lshlrev_b32 v228, 16, v68\n"
"v_and_b32 v229, s67, v68\n"
"v_mfma_f32_16x16x32_bf16 v[36:39], a[80:83], a[32:35], -4.0\n"
"v_lshlrev_b32 v230, 16, v69\n"
"v_and_b32 v231, s67, v69\n"
"v_mfma_f32_16x16x32_bf16 v[40:43], a[96:99], a[32:35], -4.0\n"
"v_lshlrev_b32 v232, 16, v70\n"
"v_and_b32 v233, s67, v70\n"
"v_mfma_f32_16x16x32_bf16 v[44:47], a[112:115], a[32:35], -4.0\n"
"v_lshlrev_b32 v234, 16, v71\n"
"v_and_b32 v235, s67, v71\n"
"v_exp_f32 v48, v48\n"
"v_exp_f32 v49, v49\n"
"s_nop 0\n"
"v_cvt_pk_bf16_f32 v136, v48, v49\n"
"v_exp_f32 v50, v50\n"
"v_exp_f32 v51, v51\n"
"s_nop 0\n"
"v_cvt_pk_bf16_f32 v137, v50, v51\n"
"v_exp_f32 v52, v52\n"
"v_exp_f32 v53, v53\n"
"v_pk_mul_f32 v[228:229], v[228:229], s[68:69]\n"
"v_pk_mul_f32 v[230:231], v[230:231], s[68:69]\n"
"v_pk_mul_f32 v[232:233], v[232:233], s[68:69]\n"
"v_pk_mul_f32 v[234:235], v[234:235], s[68:69]\n"
"v_cvt_pk_bf16_f32 v228, v228, v229\n"
"v_cvt_pk_bf16_f32 v230, v230, v231\n"
"v_cvt_pk_bf16_f32 v232, v232, v233\n"
"v_cvt_pk_bf16_f32 v234, v234, v235\n"
"v_accvgpr_write_b32 a36, v228\n"
"v_accvgpr_write_b32 a37, v230\n"
"v_accvgpr_write_b32 a38, v232\n"
"v_accvgpr_write_b32 a39, v234\n"
"s_nop 1\n"
"v_mfma_f32_16x16x32_bf16 v[32:35], a[68:71], a[36:39], v[32:35]\n"
"v_lshlrev_b32 v228, 16, v72\n"
"v_and_b32 v229, s67, v72\n"
"v_mfma_f32_16x16x32_bf16 v[36:39], a[84:87], a[36:39], v[36:39]\n"
"v_lshlrev_b32 v230, 16, v73\n"
"v_and_b32 v231, s67, v73\n"
"v_mfma_f32_16x16x32_bf16 v[40:43], a[100:103], a[36:39], v[40:43]\n"
"v_lshlrev_b32 v232, 16, v74\n"
"v_and_b32 v233, s67, v74\n"
"v_mfma_f32_16x16x32_bf16 v[44:47], a[116:119], a[36:39], v[44:47]\n"
"v_lshlrev_b32 v234, 16, v75\n"
"v_and_b32 v235, s67, v75\n"
"v_cvt_pk_bf16_f32 v138, v52, v53\n"
"v_exp_f32 v54, v54\n"
"v_exp_f32 v55, v55\n"
"s_nop 0\n"
"v_cvt_pk_bf16_f32 v139, v54, v55\n"
"v_exp_f32 v56, v56\n"
"v_exp_f32 v57, v57\n"
"s_nop 0\n"
"v_cvt_pk_bf16_f32 v140, v56, v57\n"
"v_exp_f32 v58, v58\n"
"v_exp_f32 v59, v59\n"
"v_pk_mul_f32 v[228:229], v[228:229], s[68:69]\n"
"v_pk_mul_f32 v[230:231], v[230:231], s[68:69]\n"
"v_pk_mul_f32 v[232:233], v[232:233], s[68:69]\n"
"v_pk_mul_f32 v[234:235], v[234:235], s[68:69]\n"
"v_cvt_pk_bf16_f32 v228, v228, v229\n"
"v_cvt_pk_bf16_f32 v230, v230, v231\n"
"v_cvt_pk_bf16_f32 v232, v232, v233\n"
"v_cvt_pk_bf16_f32 v234, v234, v235\n"
"v_accvgpr_write_b32 a40, v228\n"
"v_accvgpr_write_b32 a41, v230\n"
"v_accvgpr_write_b32 a42, v232\n"
"v_accvgpr_write_b32 a43, v234\n"
"s_waitcnt vmcnt(8)\n"
"ds_read_b128 v[16:19], v7 offset:49152\n"
"ds_read_b128 v[20:23], v7 offset:49408\n"
"ds_read_b128 v[24:27], v7 offset:49664\n"
"ds_read_b128 v[28:31], v7 offset:49920\n"
"s_add_u32 m0, s64, 32768\n"
"s_bfe_u32 s48, s38, 0x30008\n"
"s_lshl_b32 s48, s48, 14\n"
"s_add_u32 s36, s79, s48\n"
"s_add_u32 s48, s42, s36\n"
"s_addc_u32 s49, s43, 0\n"
"global_load_lds_dwordx4 v219, s[48:49] offset:0\n"
"global_load_lds_dwordx4 v219, s[48:49] offset:1024\n"
"global_load_lds_dwordx4 v220, s[48:49] offset:2048\n"
"global_load_lds_dwordx4 v220, s[48:49] offset:3072\n"
"v_mfma_f32_16x16x32_bf16 v[32:35], a[72:75], a[40:43], v[32:35]\n"
"v_lshlrev_b32 v228, 16, v76\n"
"v_and_b32 v229, s67, v76\n"
"v_mfma_f32_16x16x32_bf16 v[36:39], a[88:91], a[40:43], v[36:39]\n"
"v_lshlrev_b32 v230, 16, v77\n"
"v_and_b32 v231, s67, v77\n"
"v_mfma_f32_16x16x32_bf16 v[40:43], a[104:107], a[40:43], v[40:43]\n"
"v_lshlrev_b32 v232, 16, v78\n"
"v_and_b32 v233, s67, v78\n"
"v_mfma_f32_16x16x32_bf16 v[44:47], a[120:123], a[40:43], v[44:47]\n"
"v_lshlrev_b32 v234, 16, v79\n"
"v_and_b32 v235, s67, v79\n"
"v_cvt_pk_bf16_f32 v141, v58, v59\n"
"v_exp_f32 v60, v60\n"
"v_exp_f32 v61, v61\n"
"s_nop 0\n"
"v_cvt_pk_bf16_f32 v142, v60, v61\n"
"v_exp_f32 v62, v62\n"
"v_exp_f32 v63, v63\n"
"s_nop 0\n"
"v_cvt_pk_bf16_f32 v143, v62, v63\n"
"v_pk_mul_f32 v[228:229], v[228:229], s[68:69]\n"
"v_pk_mul_f32 v[230:231], v[230:231], s[68:69]\n"
"v_pk_mul_f32 v[232:233], v[232:233], s[68:69]\n"
"v_pk_mul_f32 v[234:235], v[234:235], s[68:69]\n"
"v_cvt_pk_bf16_f32 v228, v228, v229\n"
"v_cvt_pk_bf16_f32 v230, v230, v231\n"
"v_cvt_pk_bf16_f32 v232, v232, v233\n"
"v_cvt_pk_bf16_f32 v234, v234, v235\n"
"v_accvgpr_write_b32 a44, v228\n"
"v_accvgpr_write_b32 a45, v230\n"
"v_accvgpr_write_b32 a46, v232\n"
"v_accvgpr_write_b32 a47, v234\n"
"s_nop 1\n"
"v_mfma_f32_16x16x32_bf16 v[32:35], a[76:79], a[44:47], v[32:35]\n"
"s_waitcnt lgkmcnt(0)\n"
"v_lshlrev_b32 v228, 16, v16\n"
"v_and_b32 v229, s67, v16\n"
"v_mfma_f32_16x16x32_bf16 v[36:39], a[92:95], a[44:47], v[36:39]\n"
"v_lshlrev_b32 v230, 16, v17\n"
"v_and_b32 v231, s67, v17\n"
"v_mfma_f32_16x16x32_bf16 v[40:43], a[108:111], a[44:47], v[40:43]\n"
".Lattn_pad0:\n"
"s_cbranch_vccnz .Lattn_ret\n"
"v_lshlrev_b32 v232, 16, v18\n"
"v_and_b32 v233, s67, v18\n"
"v_mfma_f32_16x16x32_bf16 v[44:47], a[124:127], a[44:47], v[44:47]\n"
"v_lshlrev_b32 v234, 16, v19\n"
"v_and_b32 v235, s67, v19\n"
"v_mul_f32 v228, s68, v228\n"
"v_mul_f32 v229, s68, v229\n"
"v_mul_f32 v230, s68, v230\n"
"v_mul_f32 v231, s68, v231\n"
"v_pk_mul_f32 v[232:233], v[232:233], s[68:69]\n"
"v_pk_mul_f32 v[234:235], v[234:235], s[68:69]\n"
"v_cvt_pk_bf16_f32 v228, v228, v229\n"
"v_cvt_pk_bf16_f32 v230, v230, v231\n"
"v_cvt_pk_bf16_f32 v232, v232, v233\n"
"v_cvt_pk_bf16_f32 v234, v234, v235\n"
"v_accvgpr_write_b32 a48, v228\n"
"v_accvgpr_write_b32 a49, v230\n"
"v_accvgpr_write_b32 a50, v232\n"
"v_accvgpr_write_b32 a51, v234\n"
"s_add_u32 m0, s65, 16384\n"
"s_bfe_u32 s50, s38, 0x30004\n"
"s_lshl_b32 s50, s50, 14\n"
"s_add_u32 s39, s54, s50\n"
"s_add_u32 s50, s44, s39\n"
"s_addc_u32 s51, s45, 0\n"
"global_load_lds_dwordx4 v219, s[50:51] offset:0\n"
"global_load_lds_dwordx4 v219, s[50:51] offset:1024\n"
"global_load_lds_dwordx4 v219, s[50:51] offset:2048\n"
"global_load_lds_dwordx4 v219, s[50:51] offset:3072\n"
"v_mfma_f32_16x16x32_bf16 v[48:51], a[64:67], a[48:51], -4.0\n"
"v_lshlrev_b32 v228, 16, v20\n"
"v_and_b32 v229, s67, v20\n"
"v_mfma_f32_16x16x32_bf16 v[52:55], a[80:83], a[48:51], -4.0\n"
"v_lshlrev_b32 v230, 16, v21\n"
"v_and_b32 v231, s67, v21\n"
"v_mfma_f32_16x16x32_bf16 v[56:59], a[96:99], a[48:51], -4.0\n"
"v_lshlrev_b32 v232, 16, v22\n"
"v_and_b32 v233, s67, v22\n"
"v_mfma_f32_16x16x32_bf16 v[252:255], a[112:115], a[48:51], -4.0\n"
"v_lshlrev_b32 v234, 16, v23\n"
"v_and_b32 v235, s67, v23\n"
"v_exp_f32 v32, v32\n"
"v_exp_f32 v33, v33\n"
"s_nop 0\n"
"v_cvt_pk_bf16_f32 v144, v32, v33\n"
"v_exp_f32 v34, v34\n"
"v_exp_f32 v35, v35\n"
"s_nop 0\n"
"v_cvt_pk_bf16_f32 v145, v34, v35\n"
"v_exp_f32 v36, v36\n"
"v_exp_f32 v37, v37\n"
"v_pk_mul_f32 v[228:229], v[228:229], s[68:69]\n"
"v_pk_mul_f32 v[230:231], v[230:231], s[68:69]\n"
"v_pk_mul_f32 v[232:233], v[232:233], s[68:69]\n"
"v_pk_mul_f32 v[234:235], v[234:235], s[68:69]\n"
"v_cvt_pk_bf16_f32 v228, v228, v229\n"
"v_cvt_pk_bf16_f32 v230, v230, v231\n"
"v_cvt_pk_bf16_f32 v232, v232, v233\n"
"v_cvt_pk_bf16_f32 v234, v234, v235\n"
"v_accvgpr_write_b32 a52, v228\n"
"v_accvgpr_write_b32 a53, v230\n"
"v_accvgpr_write_b32 a54, v232\n"
"v_accvgpr_write_b32 a55, v234\n"
"s_nop 1\n"
"v_mfma_f32_16x16x32_bf16 v[48:51], a[68:71], a[52:55], v[48:51]\n"
"v_lshlrev_b32 v228, 16, v24\n"
"v_and_b32 v229, s67, v24\n"
"v_mfma_f32_16x16x32_bf16 v[52:55], a[84:87], a[52:55], v[52:55]\n"
"v_lshlrev_b32 v230, 16, v25\n"
"v_and_b32 v231, s67, v25\n"
"v_mfma_f32_16x16x32_bf16 v[56:59], a[100:103], a[52:55], v[56:59]\n"
"v_lshlrev_b32 v232, 16, v26\n"
"v_and_b32 v233, s67, v26\n"
"v_mfma_f32_16x16x32_bf16 v[252:255], a[116:119], a[52:55], v[252:255]\n"
"v_lshlrev_b32 v234, 16, v27\n"
"v_and_b32 v235, s67, v27\n"
"v_cvt_pk_bf16_f32 v146, v36, v37\n"
"v_exp_f32 v38, v38\n"
"v_exp_f32 v39, v39\n"
"s_nop 0\n"
"v_cvt_pk_bf16_f32 v147, v38, v39\n"
"v_exp_f32 v40, v40\n"
"v_exp_f32 v41, v41\n"
"s_nop 0\n"
"v_cvt_pk_bf16_f32 v148, v40, v41\n"
"v_exp_f32 v42, v42\n"
"v_exp_f32 v43, v43\n"
"v_pk_mul_f32 v[228:229], v[228:229], s[68:69]\n"
"v_pk_mul_f32 v[230:231], v[230:231], s[68:69]\n"
"v_pk_mul_f32 v[232:233], v[232:233], s[68:69]\n"
"v_pk_mul_f32 v[234:235], v[234:235], s[68:69]\n"
"v_cvt_pk_bf16_f32 v228, v228, v229\n"
"v_cvt_pk_bf16_f32 v230, v230, v231\n"
"v_cvt_pk_bf16_f32 v232, v232, v233\n"
"v_cvt_pk_bf16_f32 v234, v234, v235\n"
"v_accvgpr_write_b32 a56, v228\n"
"v_accvgpr_write_b32 a57, v230\n"
"v_accvgpr_write_b32 a58, v232\n"
"v_accvgpr_write_b32 a59, v234\n"
"s_add_u32 m0, s66, 0\n"
"s_mov_b32 s55, s62\n"
"s_add_u32 s35, s55, s99\n"
"s_add_u32 s52, s40, s35\n"
"s_addc_u32 s53, s41, 0\n"
"global_load_lds_dwordx4 v219, s[52:53] offset:0\n"
"global_load_lds_dwordx4 v219, s[52:53] offset:1024\n"
"global_load_lds_dwordx4 v220, s[52:53] offset:2048\n"
"global_load_lds_dwordx4 v220, s[52:53] offset:3072\n"
"v_mfma_f32_16x16x32_bf16 v[48:51], a[72:75], a[56:59], v[48:51]\n"
"v_lshlrev_b32 v228, 16, v28\n"
"v_and_b32 v229, s67, v28\n"
"v_mfma_f32_16x16x32_bf16 v[52:55], a[88:91], a[56:59], v[52:55]\n"
"v_lshlrev_b32 v230, 16, v29\n"
"v_and_b32 v231, s67, v29\n"
"v_mfma_f32_16x16x32_bf16 v[56:59], a[104:107], a[56:59], v[56:59]\n"
"v_lshlrev_b32 v232, 16, v30\n"
"v_and_b32 v233, s67, v30\n"
"v_mfma_f32_16x16x32_bf16 v[252:255], a[120:123], a[56:59], v[252:255]\n"
"v_lshlrev_b32 v234, 16, v31\n"
"v_and_b32 v235, s67, v31\n"
"v_cvt_pk_bf16_f32 v149, v42, v43\n"
"v_exp_f32 v44, v44\n"
"v_exp_f32 v45, v45\n"
"s_nop 0\n"
"v_cvt_pk_bf16_f32 v150, v44, v45\n"
"v_exp_f32 v46, v46\n"
"v_exp_f32 v47, v47\n"
"s_nop 0\n"
"v_cvt_pk_bf16_f32 v151, v46, v47\n"
"v_pk_mul_f32 v[228:229], v[228:229], s[68:69]\n"
"v_pk_mul_f32 v[230:231], v[230:231], s[68:69]\n"
"v_pk_mul_f32 v[232:233], v[232:233], s[68:69]\n"
"v_pk_mul_f32 v[234:235], v[234:235], s[68:69]\n"
"v_cvt_pk_bf16_f32 v228, v228, v229\n"
"v_cvt_pk_bf16_f32 v230, v230, v231\n"
"v_cvt_pk_bf16_f32 v232, v232, v233\n"
"v_cvt_pk_bf16_f32 v234, v234, v235\n"
"v_accvgpr_write_b32 a60, v228\n"
"v_accvgpr_write_b32 a61, v230\n"
"v_accvgpr_write_b32 a62, v232\n"
"v_accvgpr_write_b32 a63, v234\n"
"s_mov_b64 vcc, -1\n"
"s_swappc_b64 s[86:87], s[84:85]\n"
"s_add_u32 s84, s84, 16\n"
"s_swappc_b64 s[86:87], s[84:85]\n"
"s_add_u32 s84, s84, 16\n"
"s_mov_b64 vcc, 0\n"
"s_waitcnt vmcnt(12)\n"
"s_barrier\n"
"s_nop 0\n"
"v_mfma_f32_16x16x32_bf16 v[48:51], a[76:79], a[60:63], v[48:51]\n"
"ds_read_b128 a[128:131], v216 offset:16384\n"
"ds_read_b128 a[132:135], v216 offset:16640\n"
"v_mfma_f32_16x16x32_bf16 v[52:55], a[92:95], a[60:63], v[52:55]\n"
"ds_read_b128 a[136:139], v216 offset:16896\n"
"ds_read_b128 a[140:143], v216 offset:17152\n"
"v_mfma_f32_16x16x32_bf16 v[56:59], a[108:111], a[60:63], v[56:59]\n"
"ds_read_b64_tr_b16 a[160:161], v217 offset:0\n"
"ds_read_b64_tr_b16 a[162:163], v217 offset:4096\n"
"v_mfma_f32_16x16x32_bf16 v[252:255], a[124:127], a[60:63], v[252:255]\n"
"ds_read_b64_tr_b16 a[164:165], v217 offset:8192\n"
"ds_read_b64_tr_b16 a[166:167], v217 offset:12288\n"
"ds_read_b64_tr_b16 a[168:169], v217 offset:8\n"
"ds_read_b64_tr_b16 a[170:171], v217 offset:4104\n"
"ds_read_b64_tr_b16 a[172:173], v217 offset:8200\n"
"ds_read_b64_tr_b16 a[174:175], v217 offset:12296\n"
"s_add_u32 m0, s65, 32768\n"
"s_bfe_u32 s50, s38, 0x30008\n"
"s_lshl_b32 s50, s50, 14\n"
"s_add_u32 s39, s54, s50\n"
"s_add_u32 s50, s44, s39\n"
"s_addc_u32 s51, s45, 0\n"
"global_load_lds_dwordx4 v219, s[50:51] offset:0\n"
"global_load_lds_dwordx4 v219, s[50:51] offset:1024\n"
"global_load_lds_dwordx4 v219, s[50:51] offset:2048\n"
"v_exp_f32 v48, v48\n"
"v_exp_f32 v49, v49\n"
"s_nop 0\n"
"v_cvt_pk_bf16_f32 v152, v48, v49\n"
"v_exp_f32 v50, v50\n"
"v_exp_f32 v51, v51\n"
"s_nop 0\n"
"v_cvt_pk_bf16_f32 v153, v50, v51\n"
"v_exp_f32 v52, v52\n"
"v_exp_f32 v53, v53\n"
"s_nop 0\n"
"v_cvt_pk_bf16_f32 v154, v52, v53\n"
"v_exp_f32 v54, v54\n"
"v_exp_f32 v55, v55\n"
"s_nop 0\n"
"v_cvt_pk_bf16_f32 v155, v54, v55\n"
"v_exp_f32 v56, v56\n"
"v_exp_f32 v57, v57\n"
"s_nop 0\n"
"v_cvt_pk_bf16_f32 v156, v56, v57\n"
"v_exp_f32 v58, v58\n"
"v_exp_f32 v59, v59\n"
"s_nop 0\n"
"v_cvt_pk_bf16_f32 v157, v58, v59\n"
"s_waitcnt lgkmcnt(7)\n"
"v_mfma_f32_16x16x32_bf16 v[192:195], a[128:131], a[0:3], -4.0\n"
"v_exp_f32 v252, v252\n"
"v_mfma_f32_16x16x32_bf16 v[192:195], a[132:135], a[4:7], v[192:195]\n"
"ds_read_b128 a[144:147], v216 offset:20480\n"
"v_mfma_f32_16x16x32_bf16 v[192:195], a[136:139], a[8:11], v[192:195]\n"
"v_exp_f32 v253, v253\n"
"v_mfma_f32_16x16x32_bf16 v[192:195], a[140:143], a[12:15], v[192:195]\n"
"s_add_u32 s35, s55, s70\n"
"ds_read_b128 a[148:151], v216 offset:20736\n"
"v_cvt_pk_bf16_f32 v158, v252, v253\n"
"v_mfma_f32_16x16x32_bf16 v[196:199], a[140:143], a[28:31], -4.0\n"
"s_add_u32 s52, s40, s35\n"
"v_exp_f32 v254, v254\n"
"v_mfma_f32_16x16x32_bf16 v[196:199], a[136:139], a[24:27], v[196:199]\n"
"s_addc_u32 s53, s41, 0\n"
"ds_read_b128 a[152:155], v216 offset:20992\n"
"v_mfma_f32_16x16x32_bf16 v[196:199], a[132:135], a[20:23], v[196:199]\n"
"s_add_u32 m0, s66, 4096\n"
"v_exp_f32 v255, v255\n"
"v_mfma_f32_16x16x32_bf16 v[196:199], a[128:131], a[16:19], v[196:199]\n"
"global_load_lds_dwordx4 v219, s[52:53] offset:0\n"
"v_cvt_pk_bf16_f32 v159, v254, v255\n"
"v_mfma_f32_16x16x32_bf16 v[248:251], a[128:131], a[32:35], -4.0\n"
"v_exp_f32 v192, v192\n"
"v_mfma_f32_16x16x32_bf16 v[248:251], a[132:135], a[36:39], v[248:251]\n"
"ds_read_b128 a[156:159], v216 offset:21248\n"
"v_mfma_f32_16x16x32_bf16 v[248:251], a[136:139], a[40:43], v[248:251]\n"
"v_exp_f32 v193, v193\n"
"v_mfma_f32_16x16x32_bf16 v[248:251], a[140:143], a[44:47], v[248:251]\n"
"ds_read_b64_tr_b16 a[176:177], v217 offset:256\n"
"v_cvt_pk_bf16_f32 v160, v192, v193\n"
"v_mfma_f32_16x16x32_bf16 v[252:255], a[140:143], a[60:63], -4.0\n"
"v_exp_f32 v194, v194\n"
"v_mfma_f32_16x16x32_bf16 v[252:255], a[136:139], a[56:59], v[252:255]\n"
"ds_read_b64_tr_b16 a[178:179], v217 offset:4352\n"
"v_mfma_f32_16x16x32_bf16 v[252:255], a[132:135], a[52:55], v[252:255]\n"
"s_add_u32 m0, s65, 32768\n"
"v_exp_f32 v195, v195\n"
"v_mfma_f32_16x16x32_bf16 v[252:255], a[128:131], a[48:51], v[252:255]\n"
"global_load_lds_dwordx4 v219, s[50:51] offset:3072\n"
"v_cvt_pk_bf16_f32 v161, v194, v195\n"
"v_smfmac_f32_16x16x64_bf16 a[192:195], a[208:211], v[128:135], v215\n"
"v_smfmac_f32_16x16x64_bf16 a[196:199], a[208:211], v[136:143], v215\n"
"ds_read_b64_tr_b16 a[180:181], v217 offset:8448\n"
"v_smfmac_f32_16x16x64_bf16 a[200:203], a[208:211], v[144:151], v215\n"
"v_smfmac_f32_16x16x64_bf16 a[204:207], a[208:211], v[152:159], v215\n"
"s_waitcnt lgkmcnt(6)\n"
"ds_read_b64_tr_b16 a[182:183], v217 offset:12544\n"
"v_mfma_f32_16x16x32_bf16 v[0:3], a[160:163], v[128:131], 0\n"
"v_exp_f32 v196, v196\n"
"v_mfma_f32_16x16x32_bf16 v[0:3], a[164:167], v[132:135], v[0:3]\n"
"ds_read_b64_tr_b16 a[184:185], v217 offset:264\n"
"v_mfma_f32_16x16x32_bf16 v[4:7], a[172:175], v[132:135], 0\n"
"v_exp_f32 v197, v197\n"
"v_mfma_f32_16x16x32_bf16 v[4:7], a[168:171], v[128:131], v[4:7]\n"
"v_cvt_pk_bf16_f32 v168, v196, v197\n"
"v_mfma_f32_16x16x32_bf16 v[12:15], a[168:171], v[136:139], 0\n"
"v_exp_f32 v198, v198\n"
"v_mfma_f32_16x16x32_bf16 v[12:15], a[172:175], v[140:143], v[12:15]\n"
"s_bfe_u32 s48, s38, 0x3000c\n"
"ds_read_b64_tr_b16 a[186:187], v217 offset:4360\n"
"v_mfma_f32_16x16x32_bf16 v[8:11], a[164:167], v[140:143], 0\n"
"s_lshl_b32 s48, s48, 14\n"
"v_exp_f32 v199, v199\n"
"v_mfma_f32_16x16x32_bf16 v[8:11], a[160:163], v[136:139], v[8:11]\n"
"s_add_u32 s36, s79, s48\n"
"ds_read_b64_tr_b16 a[188:189], v217 offset:8456\n"
"v_cvt_pk_bf16_f32 v169, v198, v199\n"
"v_mfma_f32_16x16x32_bf16 v[16:19], a[160:163], v[144:147], 0\n"
"s_add_u32 s48, s42, s36\n"
"v_exp_f32 v248, v248\n"
"v_mfma_f32_16x16x32_bf16 v[16:19], a[164:167], v[148:151], v[16:19]\n"
"s_addc_u32 s49, s43, 0\n"
"ds_read_b64_tr_b16 a[190:191], v217 offset:12552\n"
"v_mfma_f32_16x16x32_bf16 v[20:23], a[172:175], v[148:151], 0\n"
"s_add_u32 m0, s64, 49152\n"
"v_exp_f32 v249, v249\n"
"v_mfma_f32_16x16x32_bf16 v[20:23], a[168:171], v[144:147], v[20:23]\n"
"global_load_lds_dwordx4 v219, s[48:49] offset:0\n"
"v_cvt_pk_bf16_f32 v176, v248, v249\n"
"v_mfma_f32_16x16x32_bf16 v[28:31], a[168:171], v[152:155], 0\n"
"v_exp_f32 v250, v250\n"
"v_mfma_f32_16x16x32_bf16 v[28:31], a[172:175], v[156:159], v[28:31]\n"
"v_mfma_f32_16x16x32_bf16 v[24:27], a[164:167], v[156:159], 0\n"
"v_exp_f32 v251, v251\n"
"v_mfma_f32_16x16x32_bf16 v[24:27], a[160:163], v[152:155], v[24:27]\n"
"v_cvt_pk_bf16_f32 v177, v250, v251\n"
"v_mfma_f32_16x16x32_bf16 v[192:195], a[144:147], a[0:3], -4.0\n"
"s_waitcnt lgkmcnt(5)\n"
"v_exp_f32 v252, v252\n"
"v_mfma_f32_16x16x32_bf16 v[192:195], a[148:151], a[4:7], v[192:195]\n"
"ds_read_b128 a[128:131], v216 offset:24576\n"
"v_mfma_f32_16x16x32_bf16 v[192:195], a[152:155], a[8:11], v[192:195]\n"
"v_exp_f32 v253, v253\n"
"v_mfma_f32_16x16x32_bf16 v[192:195], a[156:159], a[12:15], v[192:195]\n"
"ds_read_b128 a[132:135], v216 offset:24832\n"
"v_cvt_pk_bf16_f32 v184, v252, v253\n"
"v_mfma_f32_16x16x32_bf16 v[196:199], a[156:159], a[28:31], -4.0\n"
"v_exp_f32 v254, v254\n"
"v_mfma_f32_16x16x32_bf16 v[196:199], a[152:155], a[24:27], v[196:199]\n"
"ds_read_b128 a[136:139], v216 offset:25088\n"
"v_mfma_f32_16x16x32_bf16 v[196:199], a[148:151], a[20:23], v[196:199]\n"
"s_add_u32 m0, s64, 49152\n"
"v_exp_f32 v255, v255\n"
"v_mfma_f32_16x16x32_bf16 v[196:199], a[144:147], a[16:19], v[196:199]\n"
"global_load_lds_dwordx4 v219, s[48:49] offset:1024\n"
"v_cvt_pk_bf16_f32 v185, v254, v255\n"
"v_mfma_f32_16x16x32_bf16 v[248:251], a[144:147], a[32:35], -4.0\n"
"v_exp_f32 v192, v192\n"
"v_mfma_f32_16x16x32_bf16 v[248:251], a[148:151], a[36:39], v[248:251]\n"
"ds_read_b128 a[140:143], v216 offset:25344\n"
"v_mfma_f32_16x16x32_bf16 v[248:251], a[152:155], a[40:43], v[248:251]\n"
"v_exp_f32 v193, v193\n"
"v_mfma_f32_16x16x32_bf16 v[248:251], a[156:159], a[44:47], v[248:251]\n"
"ds_read_b64_tr_b16 a[160:161], v217 offset:512\n"
"v_cvt_pk_bf16_f32 v162, v192, v193\n"
"v_mfma_f32_16x16x32_bf16 v[252:255], a[156:159], a[60:63], -4.0\n"
"v_exp_f32 v194, v194\n"
"v_mfma_f32_16x16x32_bf16 v[252:255], a[152:155], a[56:59], v[252:255]\n"
"ds_read_b64_tr_b16 a[162:163], v217 offset:4608\n"
"v_mfma_f32_16x16x32_bf16 v[252:255], a[148:151], a[52:55], v[252:255]\n"
"s_add_u32 m0, s64, 49152\n"
"v_exp_f32 v195, v195\n"
"v_mfma_f32_16x16x32_bf16 v[252:255], a[144:147], a[48:51], v[252:255]\n"
"global_load_lds_dwordx4 v220, s[48:49] offset:2048\n"
"v_cvt_pk_bf16_f32 v163, v194, v195\n"
"v_mfma_f32_16x16x32_bf16 v[32:35], a[176:179], v[128:131], 0\n"
"s_waitcnt lgkmcnt(6)\n"
"v_exp_f32 v196, v196\n"
"v_mfma_f32_16x16x32_bf16 v[32:35], a[180:183], v[132:135], v[32:35]\n"
"ds_read_b64_tr_b16 a[164:165], v217 offset:8704\n"
"v_mfma_f32_16x16x32_bf16 v[36:39], a[188:191], v[132:135], 0\n"
"v_exp_f32 v197, v197\n"
"v_mfma_f32_16x16x32_bf16 v[36:39], a[184:187], v[128:131], v[36:39]\n"
"ds_read_b64_tr_b16 a[166:167], v217 offset:12800\n"
"v_cvt_pk_bf16_f32 v170, v196, v197\n"
"v_mfma_f32_16x16x32_bf16 v[44:47], a[184:187], v[136:139], 0\n"
"v_exp_f32 v198, v198\n"
"v_mfma_f32_16x16x32_bf16 v[44:47], a[188:191], v[140:143], v[44:47]\n"
"ds_read_b64_tr_b16 a[168:169], v217 offset:520\n"
"v_mfma_f32_16x16x32_bf16 v[40:43], a[180:183], v[140:143], 0\n"
"s_add_u32 m0, s66, 4096\n"
"v_exp_f32 v199, v199\n"
"v_mfma_f32_16x16x32_bf16 v[40:43], a[176:179], v[136:139], v[40:43]\n"
"global_load_lds_dwordx4 v219, s[52:53] offset:1024\n"
"v_cvt_pk_bf16_f32 v171, v198, v199\n"
"v_mfma_f32_16x16x32_bf16 v[48:51], a[176:179], v[144:147], 0\n"
"v_exp_f32 v248, v248\n"
"v_mfma_f32_16x16x32_bf16 v[48:51], a[180:183], v[148:151], v[48:51]\n"
"ds_read_b64_tr_b16 a[170:171], v217 offset:4616\n"
"v_mfma_f32_16x16x32_bf16 v[52:55], a[188:191], v[148:151], 0\n"
"v_exp_f32 v249, v249\n"
"v_mfma_f32_16x16x32_bf16 v[52:55], a[184:187], v[144:147], v[52:55]\n"
"ds_read_b64_tr_b16 a[172:173], v217 offset:8712\n"
"v_cvt_pk_bf16_f32 v178, v248, v249\n"
"v_mfma_f32_16x16x32_bf16 v[60:63], a[184:187], v[152:155], 0\n"
"v_exp_f32 v250, v250\n"
"v_mfma_f32_16x16x32_bf16 v[60:63], a[188:191], v[156:159], v[60:63]\n"
"ds_read_b64_tr_b16 a[174:175], v217 offset:12808\n"
"v_mfma_f32_16x16x32_bf16 v[56:59], a[180:183], v[156:159], 0\n"
"s_add_u32 m0, s64, 49152\n"
"v_exp_f32 v251, v251\n"
"v_mfma_f32_16x16x32_bf16 v[56:59], a[176:179], v[152:155], v[56:59]\n"
"s_waitcnt lgkmcnt(6)\n"
"global_load_lds_dwordx4 v220, s[48:49] offset:3072\n"
"v_cvt_pk_bf16_f32 v179, v250, v251\n"
"v_mfma_f32_16x16x32_bf16 v[192:195], a[128:131], a[0:3], -4.0\n"
"v_exp_f32 v252, v252\n"
"v_mfma_f32_16x16x32_bf16 v[192:195], a[132:135], a[4:7], v[192:195]\n"
"ds_read_b128 a[144:147], v216 offset:28672\n"
"v_mfma_f32_16x16x32_bf16 v[192:195], a[136:139], a[8:11], v[192:195]\n"
"v_exp_f32 v253, v253\n"
"v_mfma_f32_16x16x32_bf16 v[192:195], a[140:143], a[12:15], v[192:195]\n"
"ds_read_b128 a[148:151], v216 offset:28928\n"
"v_cvt_pk_bf16_f32 v186, v252, v253\n"
"v_mfma_f32_16x16x32_bf16 v[196:199], a[140:143], a[28:31], -4.0\n"
"v_exp_f32 v254, v254\n"
"v_mfma_f32_16x16x32_bf16 v[196:199], a[136:139], a[24:27], v[196:199]\n"
"ds_read_b128 a[152:155], v216 offset:29184\n"
"v_mfma_f32_16x16x32_bf16 v[196:199], a[132:135], a[20:23], v[196:199]\n"
"v_exp_f32 v255, v255\n"
"v_mfma_f32_16x16x32_bf16 v[196:199], a[128:131], a[16:19], v[196:199]\n"
"v_cvt_pk_bf16_f32 v187, v254, v255\n"
"v_mfma_f32_16x16x32_bf16 v[248:251], a[128:131], a[32:35], -4.0\n"
"v_exp_f32 v192, v192\n"
"v_mfma_f32_16x16x32_bf16 v[248:251], a[132:135], a[36:39], v[248:251]\n"
"ds_read_b128 a[156:159], v216 offset:29440\n"
"v_mfma_f32_16x16x32_bf16 v[248:251], a[136:139], a[40:43], v[248:251]\n"
"v_exp_f32 v193, v193\n"
"v_mfma_f32_16x16x32_bf16 v[248:251], a[140:143], a[44:47], v[248:251]\n"
"ds_read_b64_tr_b16 a[176:177], v217 offset:768\n"
"v_cvt_pk_bf16_f32 v164, v192, v193\n"
"v_mfma_f32_16x16x32_bf16 v[252:255], a[140:143], a[60:63], -4.0\n"
"v_exp_f32 v194, v194\n"
"v_mfma_f32_16x16x32_bf16 v[252:255], a[136:139], a[56:59], v[252:255]\n"
"ds_read_b64_tr_b16 a[178:179], v217 offset:4864\n"
"v_mfma_f32_16x16x32_bf16 v[252:255], a[132:135], a[52:55], v[252:255]\n"
"v_exp_f32 v195, v195\n"
"v_mfma_f32_16x16x32_bf16 v[252:255], a[128:131], a[48:51], v[252:255]\n"
"v_cvt_pk_bf16_f32 v165, v194, v195\n"
"v_mfma_f32_16x16x32_bf16 v[64:67], a[160:163], v[128:131], 0\n"
"s_waitcnt lgkmcnt(6)\n"
"v_exp_f32 v196, v196\n"
"v_mfma_f32_16x16x32_bf16 v[64:67], a[164:167], v[132:135], v[64:67]\n"
"ds_read_b64_tr_b16 a[180:181], v217 offset:8960\n"
"v_mfma_f32_16x16x32_bf16 v[68:71], a[172:175], v[132:135], 0\n"
"v_exp_f32 v197, v197\n"
"v_mfma_f32_16x16x32_bf16 v[68:71], a[168:171], v[128:131], v[68:71]\n"
"ds_read_b64_tr_b16 a[182:183], v217 offset:13056\n"
"v_cvt_pk_bf16_f32 v172, v196, v197\n"
"v_mfma_f32_16x16x32_bf16 v[76:79], a[168:171], v[136:139], 0\n"
"v_exp_f32 v198, v198\n"
"v_mfma_f32_16x16x32_bf16 v[76:79], a[172:175], v[140:143], v[76:79]\n"
"ds_read_b64_tr_b16 a[184:185], v217 offset:776\n"
"v_mfma_f32_16x16x32_bf16 v[72:75], a[164:167], v[140:143], 0\n"
"s_add_u32 m0, s66, 4096\n"
"v_exp_f32 v199, v199\n"
"v_mfma_f32_16x16x32_bf16 v[72:75], a[160:163], v[136:139], v[72:75]\n"
"global_load_lds_dwordx4 v220, s[52:53] offset:2048\n"
"v_cvt_pk_bf16_f32 v173, v198, v199\n"
"v_mfma_f32_16x16x32_bf16 v[80:83], a[160:163], v[144:147], 0\n"
"v_exp_f32 v248, v248\n"
"v_mfma_f32_16x16x32_bf16 v[80:83], a[164:167], v[148:151], v[80:83]\n"
"ds_read_b64_tr_b16 a[186:187], v217 offset:4872\n"
"v_mfma_f32_16x16x32_bf16 v[84:87], a[172:175], v[148:151], 0\n"
"v_exp_f32 v249, v249\n"
"v_mfma_f32_16x16x32_bf16 v[84:87], a[168:171], v[144:147], v[84:87]\n"
"ds_read_b64_tr_b16 a[188:189], v217 offset:8968\n"
"v_cvt_pk_bf16_f32 v180, v248, v249\n"
"v_mfma_f32_16x16x32_bf16 v[92:95], a[168:171], v[152:155], 0\n"
"v_exp_f32 v250, v250\n"
"v_mfma_f32_16x16x32_bf16 v[92:95], a[172:175], v[156:159], v[92:95]\n"
"ds_read_b64_tr_b16 a[190:191], v217 offset:13064\n"
"v_mfma_f32_16x16x32_bf16 v[88:91], a[164:167], v[156:159], 0\n"
"v_exp_f32 v251, v251\n"
"v_mfma_f32_16x16x32_bf16 v[88:91], a[160:163], v[152:155], v[88:91]\n"
"s_waitcnt vmcnt(15) lgkmcnt(6)\n"
"v_cvt_pk_bf16_f32 v181, v250, v251\n"
"v_mfma_f32_16x16x32_bf16 v[192:195], a[144:147], a[0:3], -4.0\n"
"s_barrier\n"
"v_exp_f32 v252, v252\n"
"v_mfma_f32_16x16x32_bf16 v[192:195], a[148:151], a[4:7], v[192:195]\n"
"s_bfe_u32 s50, s38, 0x3000c\n"
"ds_read_b128 a[128:131], v216 offset:32768\n"
"v_mfma_f32_16x16x32_bf16 v[192:195], a[152:155], a[8:11], v[192:195]\n"
"s_lshl_b32 s50, s50, 14\n"
"v_exp_f32 v253, v253\n"
"v_mfma_f32_16x16x32_bf16 v[192:195], a[156:159], a[12:15], v[192:195]\n"
"s_add_u32 s39, s54, s50\n"
"ds_read_b128 a[132:135], v216 offset:33024\n"
"v_cvt_pk_bf16_f32 v188, v252, v253\n"
"v_mfma_f32_16x16x32_bf16 v[196:199], a[156:159], a[28:31], -4.0\n"
"s_add_u32 s50, s44, s39\n"
"v_exp_f32 v254, v254\n"
"v_mfma_f32_16x16x32_bf16 v[196:199], a[152:155], a[24:27], v[196:199]\n"
"s_addc_u32 s51, s45, 0\n"
"ds_read_b128 a[136:139], v216 offset:33280\n"
"v_mfma_f32_16x16x32_bf16 v[196:199], a[148:151], a[20:23], v[196:199]\n"
"s_add_u32 m0, s65, 49152\n"
"v_exp_f32 v255, v255\n"
"v_mfma_f32_16x16x32_bf16 v[196:199], a[144:147], a[16:19], v[196:199]\n"
"global_load_lds_dwordx4 v219, s[50:51] offset:0\n"
"v_cvt_pk_bf16_f32 v189, v254, v255\n"
"v_mfma_f32_16x16x32_bf16 v[248:251], a[144:147], a[32:35], -4.0\n"
"v_exp_f32 v192, v192\n"
"v_mfma_f32_16x16x32_bf16 v[248:251], a[148:151], a[36:39], v[248:251]\n"
"ds_read_b128 a[140:143], v216 offset:33536\n"
"v_mfma_f32_16x16x32_bf16 v[248:251], a[152:155], a[40:43], v[248:251]\n"
"v_exp_f32 v193, v193\n"
"v_mfma_f32_16x16x32_bf16 v[248:251], a[156:159], a[44:47], v[248:251]\n"
"ds_read_b64_tr_b16 a[160:161], v217 offset:16384\n"
"v_cvt_pk_bf16_f32 v166, v192, v193\n"
"v_mfma_f32_16x16x32_bf16 v[252:255], a[156:159], a[60:63], -4.0\n"
"v_exp_f32 v194, v194\n"
"v_mfma_f32_16x16x32_bf16 v[252:255], a[152:155], a[56:59], v[252:255]\n"
"ds_read_b64_tr_b16 a[162:163], v217 offset:20480\n"
"v_mfma_f32_16x16x32_bf16 v[252:255], a[148:151], a[52:55], v[252:255]\n"
"v_exp_f32 v195, v195\n"
"v_mfma_f32_16x16x32_bf16 v[252:255], a[144:147], a[48:51], v[252:255]\n"
"v_cvt_pk_bf16_f32 v167, v194, v195\n"
"v_mfma_f32_16x16x32_bf16 v[96:99], a[176:179], v[128:131], 0\n"
"s_waitcnt lgkmcnt(6)\n"
"v_exp_f32 v196, v196\n"
"v_mfma_f32_16x16x32_bf16 v[96:99], a[180:183], v[132:135], v[96:99]\n"
"ds_read_b64_tr_b16 a[164:165], v217 offset:24576\n"
"v_mfma_f32_16x16x32_bf16 v[100:103], a[188:191], v[132:135], 0\n"
"v_exp_f32 v197, v197\n"
"v_mfma_f32_16x16x32_bf16 v[100:103], a[184:187], v[128:131], v[100:103]\n"
"ds_read_b64_tr_b16 a[166:167], v217 offset:28672\n"
"v_cvt_pk_bf16_f32 v174, v196, v197\n"
"v_mfma_f32_16x16x32_bf16 v[108:111], a[184:187], v[136:139], 0\n"
"v_exp_f32 v198, v198\n"
"v_mfma_f32_16x16x32_bf16 v[108:111], a[188:191], v[140:143], v[108:111]\n"
"ds_read_b64_tr_b16 a[168:169], v217 offset:16392\n"
"v_mfma_f32_16x16x32_bf16 v[104:107], a[180:183], v[140:143], 0\n"
"s_add_u32 m0, s65, 49152\n"
"v_exp_f32 v199, v199\n"
"v_mfma_f32_16x16x32_bf16 v[104:107], a[176:179], v[136:139], v[104:107]\n"
"global_load_lds_dwordx4 v219, s[50:51] offset:1024\n"
"v_cvt_pk_bf16_f32 v175, v198, v199\n"
"v_mfma_f32_16x16x32_bf16 v[112:115], a[176:179], v[144:147], 0\n"
"v_exp_f32 v248, v248\n"
"v_mfma_f32_16x16x32_bf16 v[112:115], a[180:183], v[148:151], v[112:115]\n"
"ds_read_b64_tr_b16 a[170:171], v217 offset:20488\n"
"v_mfma_f32_16x16x32_bf16 v[116:119], a[188:191], v[148:151], 0\n"
"v_exp_f32 v249, v249\n"
"v_mfma_f32_16x16x32_bf16 v[116:119], a[184:187], v[144:147], v[116:119]\n"
"ds_read_b64_tr_b16 a[172:173], v217 offset:24584\n"
"v_cvt_pk_bf16_f32 v182, v248, v249\n"
"v_mfma_f32_16x16x32_bf16 v[124:127], a[184:187], v[152:155], 0\n"
"v_exp_f32 v250, v250\n"
"v_mfma_f32_16x16x32_bf16 v[124:127], a[188:191], v[156:159], v[124:127]\n"
"ds_read_b64_tr_b16 a[174:175], v217 offset:28680\n"
"v_mfma_f32_16x16x32_bf16 v[120:123], a[180:183], v[156:159], 0\n"
"s_add_u32 m0, s65, 49152\n"
"v_exp_f32 v251, v251\n"
"v_mfma_f32_16x16x32_bf16 v[120:123], a[176:179], v[152:155], v[120:123]\n"
"s_waitcnt lgkmcnt(6)\n"
"global_load_lds_dwordx4 v219, s[50:51] offset:2048\n"
"v_cvt_pk_bf16_f32 v183, v250, v251\n"
"s_branch .Lattn_A2\n"
".p2align 12\n"
".Lattn_B7:\n"
"v_mfma_f32_16x16x32_bf16 v[192:195], a[128:131], a[64:67], -4.0\n"
"s_waitcnt lgkmcnt(7)\n"
"v_exp_f32 v252, v252\n"
"v_mfma_f32_16x16x32_bf16 v[192:195], a[132:135], a[68:71], v[192:195]\n"
".Lattn_pad1:\n"
"s_cbranch_vccnz .Lattn_ret\n"
"ds_read_b128 a[144:147], v216 offset:36864\n"
"v_cvt_pk_bf16_f32 v234, v234, v235\n"
"v_mfma_f32_16x16x32_bf16 v[192:195], a[136:139], a[72:75], v[192:195]\n"
"v_exp_f32 v253, v253\n"
"v_mfma_f32_16x16x32_bf16 v[192:195], a[140:143], a[76:79], v[192:195]\n"
"ds_read_b128 a[148:151], v216 offset:37120\n"
"v_cvt_pk_bf16_f32 v190, v252, v253\n"
"v_mfma_f32_16x16x32_bf16 v[196:199], a[140:143], a[92:95], -4.0\n"
"v_exp_f32 v254, v254\n"
"v_mfma_f32_16x16x32_bf16 v[196:199], a[136:139], a[88:91], v[196:199]\n"
"ds_read_b128 a[152:155], v216 offset:37376\n"
"v_accvgpr_write_b32 a0, v228\n"
"v_mfma_f32_16x16x32_bf16 v[196:199], a[132:135], a[84:87], v[196:199]\n"
"v_exp_f32 v255, v255\n"
"v_mfma_f32_16x16x32_bf16 v[196:199], a[128:131], a[80:83], v[196:199]\n"
"v_cvt_pk_bf16_f32 v191, v254, v255\n"
"v_accvgpr_write_b32 a1, v230\n"
"v_mfma_f32_16x16x32_bf16 v[248:251], a[128:131], a[96:99], -4.0\n"
"v_exp_f32 v192, v192\n"
"v_mfma_f32_16x16x32_bf16 v[248:251], a[132:135], a[100:103], v[248:251]\n"
"ds_read_b128 a[156:159], v216 offset:37632\n"
"v_accvgpr_write_b32 a2, v232\n"
"v_mfma_f32_16x16x32_bf16 v[248:251], a[136:139], a[104:107], v[248:251]\n"
"v_exp_f32 v193, v193\n"
"v_mfma_f32_16x16x32_bf16 v[248:251], a[140:143], a[108:111], v[248:251]\n"
"ds_read_b64_tr_b16 a[176:177], v217 offset:16640\n"
"v_cvt_pk_bf16_f32 v128, v192, v193\n"
"v_mfma_f32_16x16x32_bf16 v[252:255], a[140:143], a[124:127], -4.0\n"
"v_exp_f32 v194, v194\n"
"v_mfma_f32_16x16x32_bf16 v[252:255], a[136:139], a[120:123], v[252:255]\n"
"ds_read_b64_tr_b16 a[178:179], v217 offset:20736\n"
"v_accvgpr_write_b32 a3, v234\n"
"v_mfma_f32_16x16x32_bf16 v[252:255], a[132:135], a[116:119], v[252:255]\n"
"s_add_u32 m0, s65, 49152\n"
"v_exp_f32 v195, v195\n"
"v_mfma_f32_16x16x32_bf16 v[252:255], a[128:131], a[112:115], v[252:255]\n"
"global_load_lds_dwordx4 v219, s[50:51] offset:3072\n"
"v_cvt_pk_bf16_f32 v129, v194, v195\n"
"v_smfmac_f32_16x16x64_bf16 a[192:195], a[208:211], v[160:167], v215\n"
"v_lshlrev_b32 v228, 16, v204\n"
"v_smfmac_f32_16x16x64_bf16 a[196:199], a[208:211], v[168:175], v215\n"
"ds_read_b64_tr_b16 a[180:181], v217 offset:24832\n"
"v_smfmac_f32_16x16x64_bf16 a[200:203], a[208:211], v[176:183], v215\n"
"ds_read_b128 v[200:203], v218 offset:768\n"
"v_smfmac_f32_16x16x64_bf16 a[204:207], a[208:211], v[184:191], v215\n"
"ds_read_b64_tr_b16 a[182:183], v217 offset:28928\n"
"v_mfma_f32_16x16x32_bf16 v[0:3], a[160:163], v[160:163], v[0:3]\n"
"v_exp_f32 v196, v196\n"
"v_mfma_f32_16x16x32_bf16 v[0:3], a[164:167], v[164:167], v[0:3]\n"
"s_waitcnt lgkmcnt(6)\n"
"ds_read_b64_tr_b16 a[184:185], v217 offset:16648\n"
"v_and_b32 v229, s67, v204\n"
"v_mfma_f32_16x16x32_bf16 v[4:7], a[172:175], v[164:167], v[4:7]\n"
"v_exp_f32 v197, v197\n"
"v_mfma_f32_16x16x32_bf16 v[4:7], a[168:171], v[160:163], v[4:7]\n"
"v_cvt_pk_bf16_f32 v136, v196, v197\n"
"v_lshlrev_b32 v230, 16, v205\n"
"v_mfma_f32_16x16x32_bf16 v[12:15], a[168:171], v[168:171], v[12:15]\n"
"v_exp_f32 v198, v198\n"
"v_mfma_f32_16x16x32_bf16 v[12:15], a[172:175], v[172:175], v[12:15]\n"
"s_bfe_u32 s48, s38, 0x30010\n"
"ds_read_b64_tr_b16 a[186:187], v217 offset:20744\n"
"v_and_b32 v231, s67, v205\n"
"v_mfma_f32_16x16x32_bf16 v[8:11], a[164:167], v[172:175], v[8:11]\n"
"s_lshl_b32 s48, s48, 14\n"
"v_exp_f32 v199, v199\n"
"v_mfma_f32_16x16x32_bf16 v[8:11], a[160:163], v[168:171], v[8:11]\n"
"s_add_u32 s36, s79, s48\n"
"ds_read_b64_tr_b16 a[188:189], v217 offset:24840\n"
"v_cvt_pk_bf16_f32 v137, v198, v199\n"
"v_mfma_f32_16x16x32_bf16 v[16:19], a[160:163], v[176:179], v[16:19]\n"
"s_add_u32 s48, s42, s36\n"
"v_exp_f32 v248, v248\n"
"v_mfma_f32_16x16x32_bf16 v[16:19], a[164:167], v[180:183], v[16:19]\n"
"s_addc_u32 s49, s43, 0\n"
"ds_read_b64_tr_b16 a[190:191], v217 offset:28936\n"
"v_lshlrev_b32 v232, 16, v206\n"
"v_mfma_f32_16x16x32_bf16 v[20:23], a[172:175], v[180:183], v[20:23]\n"
"s_add_u32 m0, s64, 0\n"
"v_exp_f32 v249, v249\n"
"v_mfma_f32_16x16x32_bf16 v[20:23], a[168:171], v[176:179], v[20:23]\n"
"global_load_lds_dwordx4 v219, s[48:49] offset:0\n"
"v_cvt_pk_bf16_f32 v144, v248, v249\n"
"v_mfma_f32_16x16x32_bf16 v[28:31], a[168:171], v[184:187], v[28:31]\n"
"v_exp_f32 v250, v250\n"
"v_mfma_f32_16x16x32_bf16 v[28:31], a[172:175], v[188:191], v[28:31]\n"
"v_and_b32 v233, s67, v206\n"
"v_lshlrev_b32 v234, 16, v207\n"
"v_mfma_f32_16x16x32_bf16 v[24:27], a[164:167], v[188:191], v[24:27]\n"
"v_exp_f32 v251, v251\n"
"v_mfma_f32_16x16x32_bf16 v[24:27], a[160:163], v[184:187], v[24:27]\n"
"v_cvt_pk_bf16_f32 v145, v250, v251\n"
"v_and_b32 v235, s67, v207\n"
"v_mfma_f32_16x16x32_bf16 v[192:195], a[144:147], a[64:67], -4.0\n"
"v_exp_f32 v252, v252\n"
"v_mfma_f32_16x16x32_bf16 v[192:195], a[148:151], a[68:71], v[192:195]\n"
"ds_read_b128 a[128:131], v216 offset:40960\n"
"v_mul_f32 v228, s68, v228\n"
"v_mfma_f32_16x16x32_bf16 v[192:195], a[152:155], a[72:75], v[192:195]\n"
"s_waitcnt lgkmcnt(4)\n"
"v_exp_f32 v253, v253\n"
"v_mfma_f32_16x16x32_bf16 v[192:195], a[156:159], a[76:79], v[192:195]\n"
"ds_read_b128 a[132:135], v216 offset:41216\n"
"v_cvt_pk_bf16_f32 v152, v252, v253\n"
"v_mfma_f32_16x16x32_bf16 v[196:199], a[156:159], a[92:95], -4.0\n"
"v_exp_f32 v254, v254\n"
"v_mfma_f32_16x16x32_bf16 v[196:199], a[152:155], a[88:91], v[196:199]\n"
"ds_read_b128 a[136:139], v216 offset:41472\n"
"v_mul_f32 v229, s68, v229\n"
"v_mfma_f32_16x16x32_bf16 v[196:199], a[148:151], a[84:87], v[196:199]\n"
"s_add_u32 m0, s64, 0\n"
"v_exp_f32 v255, v255\n"
"v_mfma_f32_16x16x32_bf16 v[196:199], a[144:147], a[80:83], v[196:199]\n"
"global_load_lds_dwordx4 v219, s[48:49] offset:1024\n"
"v_cvt_pk_bf16_f32 v153, v254, v255\n"
"v_mfma_f32_16x16x32_bf16 v[248:251], a[144:147], a[96:99], -4.0\n"
"v_exp_f32 v192, v192\n"
"v_mfma_f32_16x16x32_bf16 v[248:251], a[148:151], a[100:103], v[248:251]\n"
"ds_read_b128 a[140:143], v216 offset:41728\n"
"v_mul_f32 v230, s68, v230\n"
"v_mfma_f32_16x16x32_bf16 v[248:251], a[152:155], a[104:107], v[248:251]\n"
"v_exp_f32 v193, v193\n"
"v_mfma_f32_16x16x32_bf16 v[248:251], a[156:159], a[108:111], v[248:251]\n"
"ds_read_b64_tr_b16 a[160:161], v217 offset:16896\n"
"v_cvt_pk_bf16_f32 v130, v192, v193\n"
"v_mfma_f32_16x16x32_bf16 v[252:255], a[156:159], a[124:127], -4.0\n"
"v_exp_f32 v194, v194\n"
"v_mfma_f32_16x16x32_bf16 v[252:255], a[152:155], a[120:123], v[252:255]\n"
"ds_read_b64_tr_b16 a[162:163], v217 offset:20992\n"
"v_mul_f32 v231, s68, v231\n"
"v_mfma_f32_16x16x32_bf16 v[252:255], a[148:151], a[116:119], v[252:255]\n"
"s_add_u32 m0, s64, 0\n"
"v_exp_f32 v195, v195\n"
"v_mfma_f32_16x16x32_bf16 v[252:255], a[144:147], a[112:115], v[252:255]\n"
"global_load_lds_dwordx4 v220, s[48:49] offset:2048\n"
"v_cvt_pk_bf16_f32 v131, v194, v195\n"
"v_mfma_f32_16x16x32_bf16 v[32:35], a[176:179], v[160:163], v[32:35]\n"
"v_exp_f32 v196, v196\n"
"v_mfma_f32_16x16x32_bf16 v[32:35], a[180:183], v[164:167], v[32:35]\n"
"s_waitcnt lgkmcnt(5)\n"
"ds_read_b64_tr_b16 a[164:165], v217 offset:25088\n"
"buffer_store_dwordx4 a[232:235], v223, s[56:59], s71 offen offset:0 sc1 nt\n"
"v_mul_f32 v232, s68, v232\n"
"v_mul_f32 v233, s68, v233\n"
"v_mfma_f32_16x16x32_bf16 v[36:39], a[188:191], v[164:167], v[36:39]\n"
"v_exp_f32 v197, v197\n"
"v_mfma_f32_16x16x32_bf16 v[36:39], a[184:187], v[160:163], v[36:39]\n"
"s_add_u32 s35, s55, s71\n"
"ds_read_b64_tr_b16 a[166:167], v217 offset:29184\n"
"v_cvt_pk_bf16_f32 v138, v196, v197\n"
"v_mfma_f32_16x16x32_bf16 v[44:47], a[184:187], v[168:171], v[44:47]\n"
"s_add_u32 s52, s40, s35\n"
"v_exp_f32 v198, v198\n"
"v_mfma_f32_16x16x32_bf16 v[44:47], a[188:191], v[172:175], v[44:47]\n"
"s_addc_u32 s53, s41, 0\n"
"ds_read_b64_tr_b16 a[168:169], v217 offset:16904\n"
"v_mul_f32 v234, s68, v234\n"
"v_mfma_f32_16x16x32_bf16 v[40:43], a[180:183], v[172:175], v[40:43]\n"
"s_add_u32 m0, s66, 0\n"
"v_exp_f32 v199, v199\n"
"v_mfma_f32_16x16x32_bf16 v[40:43], a[176:179], v[168:171], v[40:43]\n"
"global_load_lds_dwordx4 v221, s[52:53] offset:0\n"
"v_cvt_pk_bf16_f32 v139, v198, v199\n"
"v_mfma_f32_16x16x32_bf16 v[48:51], a[176:179], v[176:179], v[48:51]\n"
"v_exp_f32 v248, v248\n"
"v_mfma_f32_16x16x32_bf16 v[48:51], a[180:183], v[180:183], v[48:51]\n"
"ds_read_b64_tr_b16 a[170:171], v217 offset:21000\n"
"v_mul_f32 v235, s68, v235\n"
"v_mfma_f32_16x16x32_bf16 v[52:55], a[188:191], v[180:183], v[52:55]\n"
"v_exp_f32 v249, v249\n"
"v_mfma_f32_16x16x32_bf16 v[52:55], a[184:187], v[176:179], v[52:55]\n"
"ds_read_b64_tr_b16 a[172:173], v217 offset:25096\n"
"v_cvt_pk_bf16_f32 v146, v248, v249\n"
"v_mfma_f32_16x16x32_bf16 v[60:63], a[184:187], v[184:187], v[60:63]\n"
"v_exp_f32 v250, v250\n"
"v_mfma_f32_16x16x32_bf16 v[60:63], a[188:191], v[188:191], v[60:63]\n"
"ds_read_b64_tr_b16 a[174:175], v217 offset:29192\n"
"v_cvt_pk_bf16_f32 v228, v228, v229\n"
"v_mfma_f32_16x16x32_bf16 v[56:59], a[180:183], v[188:191], v[56:59]\n"
"s_add_u32 m0, s64, 0\n"
"v_exp_f32 v251, v251\n"
"v_mfma_f32_16x16x32_bf16 v[56:59], a[176:179], v[184:187], v[56:59]\n"
"global_load_lds_dwordx4 v220, s[48:49] offset:3072\n"
"v_cvt_pk_bf16_f32 v147, v250, v251\n"
"v_mfma_f32_16x16x32_bf16 v[192:195], a[128:131], a[64:67], -4.0\n"
"s_waitcnt lgkmcnt(5)\n"
"v_exp_f32 v252, v252\n"
"v_mfma_f32_16x16x32_bf16 v[192:195], a[132:135], a[68:71], v[192:195]\n"
"ds_read_b128 a[144:147], v216 offset:45056\n"
"v_cvt_pk_bf16_f32 v230, v230, v231\n"
"v_mfma_f32_16x16x32_bf16 v[192:195], a[136:139], a[72:75], v[192:195]\n"
"v_exp_f32 v253, v253\n"
"v_mfma_f32_16x16x32_bf16 v[192:195], a[140:143], a[76:79], v[192:195]\n"
"ds_read_b128 a[148:151], v216 offset:45312\n"
"v_cvt_pk_bf16_f32 v154, v252, v253\n"
"v_mfma_f32_16x16x32_bf16 v[196:199], a[140:143], a[92:95], -4.0\n"
"v_exp_f32 v254, v254\n"
"v_mfma_f32_16x16x32_bf16 v[196:199], a[136:139], a[88:91], v[196:199]\n"
"ds_read_b128 a[152:155], v216 offset:45568\n"
"v_cvt_pk_bf16_f32 v232, v232, v233\n"
"v_mfma_f32_16x16x32_bf16 v[196:199], a[132:135], a[84:87], v[196:199]\n"
"v_exp_f32 v255, v255\n"
"v_mfma_f32_16x16x32_bf16 v[196:199], a[128:131], a[80:83], v[196:199]\n"
"v_cvt_pk_bf16_f32 v155, v254, v255\n"
"v_cvt_pk_bf16_f32 v234, v234, v235\n"
"v_mfma_f32_16x16x32_bf16 v[248:251], a[128:131], a[96:99], -4.0\n"
"v_exp_f32 v192, v192\n"
"v_mfma_f32_16x16x32_bf16 v[248:251], a[132:135], a[100:103], v[248:251]\n"
"ds_read_b128 a[156:159], v216 offset:45824\n"
"v_accvgpr_write_b32 a4, v228\n"
"v_mfma_f32_16x16x32_bf16 v[248:251], a[136:139], a[104:107], v[248:251]\n"
"v_exp_f32 v193, v193\n"
"v_mfma_f32_16x16x32_bf16 v[248:251], a[140:143], a[108:111], v[248:251]\n"
"ds_read_b64_tr_b16 a[176:177], v217 offset:17152\n"
"v_cvt_pk_bf16_f32 v132, v192, v193\n"
"v_mfma_f32_16x16x32_bf16 v[252:255], a[140:143], a[124:127], -4.0\n"
"v_exp_f32 v194, v194\n"
"v_mfma_f32_16x16x32_bf16 v[252:255], a[136:139], a[120:123], v[252:255]\n"
"ds_read_b64_tr_b16 a[178:179], v217 offset:21248\n"
"v_accvgpr_write_b32 a5, v230\n"
"v_mfma_f32_16x16x32_bf16 v[252:255], a[132:135], a[116:119], v[252:255]\n"
"v_exp_f32 v195, v195\n"
"v_mfma_f32_16x16x32_bf16 v[252:255], a[128:131], a[112:115], v[252:255]\n"
"v_cvt_pk_bf16_f32 v133, v194, v195\n"
"v_accvgpr_write_b32 a6, v232\n"
"v_mfma_f32_16x16x32_bf16 v[64:67], a[160:163], v[160:163], v[64:67]\n"
"s_waitcnt lgkmcnt(6)\n"
"v_exp_f32 v196, v196\n"
"v_mfma_f32_16x16x32_bf16 v[64:67], a[164:167], v[164:167], v[64:67]\n"
"ds_read_b64_tr_b16 a[180:181], v217 offset:25344\n"
"v_accvgpr_write_b32 a7, v234\n"
"v_mfma_f32_16x16x32_bf16 v[68:71], a[172:175], v[164:167], v[68:71]\n"
"v_exp_f32 v197, v197\n"
"v_mfma_f32_16x16x32_bf16 v[68:71], a[168:171], v[160:163], v[68:71]\n"
"ds_read_b64_tr_b16 a[182:183], v217 offset:29440\n"
"v_cvt_pk_bf16_f32 v140, v196, v197\n"
"v_mfma_f32_16x16x32_bf16 v[76:79], a[168:171], v[168:171], v[76:79]\n"
"v_exp_f32 v198, v198\n"
"v_mfma_f32_16x16x32_bf16 v[76:79], a[172:175], v[172:175], v[76:79]\n"
"ds_read_b64_tr_b16 a[184:185], v217 offset:17160\n"
"v_lshlrev_b32 v228, 16, v244\n"
"v_mfma_f32_16x16x32_bf16 v[72:75], a[164:167], v[172:175], v[72:75]\n"
"s_add_u32 m0, s66, 0\n"
"v_exp_f32 v199, v199\n"
"v_mfma_f32_16x16x32_bf16 v[72:75], a[160:163], v[168:171], v[72:75]\n"
"global_load_lds_dwordx4 v221, s[52:53] offset:1024\n"
"v_cvt_pk_bf16_f32 v141, v198, v199\n"
"v_mfma_f32_16x16x32_bf16 v[80:83], a[160:163], v[176:179], v[80:83]\n"
"v_exp_f32 v248, v248\n"
"v_mfma_f32_16x16x32_bf16 v[80:83], a[164:167], v[180:183], v[80:83]\n"
"ds_read_b64_tr_b16 a[186:187], v217 offset:21256\n"
"v_and_b32 v229, s67, v244\n"
"v_mfma_f32_16x16x32_bf16 v[84:87], a[172:175], v[180:183], v[84:87]\n"
"v_exp_f32 v249, v249\n"
"v_mfma_f32_16x16x32_bf16 v[84:87], a[168:171], v[176:179], v[84:87]\n"
"ds_read_b64_tr_b16 a[188:189], v217 offset:25352\n"
"v_cvt_pk_bf16_f32 v148, v248, v249\n"
"v_mfma_f32_16x16x32_bf16 v[92:95], a[168:171], v[184:187], v[92:95]\n"
"v_exp_f32 v250, v250\n"
"v_mfma_f32_16x16x32_bf16 v[92:95], a[172:175], v[188:191], v[92:95]\n"
"ds_read_b64_tr_b16 a[190:191], v217 offset:29448\n"
"v_lshlrev_b32 v230, 16, v245\n"
"v_mfma_f32_16x16x32_bf16 v[88:91], a[164:167], v[188:191], v[88:91]\n"
"v_exp_f32 v251, v251\n"
"v_mfma_f32_16x16x32_bf16 v[88:91], a[160:163], v[184:187], v[88:91]\n"
"s_waitcnt vmcnt(13) lgkmcnt(6)\n"
"v_cvt_pk_bf16_f32 v149, v250, v251\n"
"v_and_b32 v231, s67, v245\n"
"v_mfma_f32_16x16x32_bf16 v[192:195], a[144:147], a[64:67], -4.0\n"
"s_barrier\n"
"v_exp_f32 v252, v252\n"
"v_mfma_f32_16x16x32_bf16 v[192:195], a[148:151], a[68:71], v[192:195]\n"
"s_bfe_u32 s50, s38, 0x30010\n"
"ds_read_b128 a[128:131], v216 offset:49152\n"
"v_lshlrev_b32 v232, 16, v246\n"
"v_mfma_f32_16x16x32_bf16 v[192:195], a[152:155], a[72:75], v[192:195]\n"
"s_lshl_b32 s50, s50, 14\n"
"v_exp_f32 v253, v253\n"
"v_mfma_f32_16x16x32_bf16 v[192:195], a[156:159], a[76:79], v[192:195]\n"
"s_add_u32 s39, s54, s50\n"
"ds_read_b128 a[132:135], v216 offset:49408\n"
"v_cvt_pk_bf16_f32 v156, v252, v253\n"
"v_mfma_f32_16x16x32_bf16 v[196:199], a[156:159], a[92:95], -4.0\n"
"s_add_u32 s50, s44, s39\n"
"v_exp_f32 v254, v254\n"
"v_mfma_f32_16x16x32_bf16 v[196:199], a[152:155], a[88:91], v[196:199]\n"
"s_addc_u32 s51, s45, 0\n"
"ds_read_b128 a[136:139], v216 offset:49664\n"
"v_and_b32 v233, s67, v246\n"
"v_mfma_f32_16x16x32_bf16 v[196:199], a[148:151], a[84:87], v[196:199]\n"
"s_add_u32 m0, s65, 0\n"
"v_exp_f32 v255, v255\n"
"v_mfma_f32_16x16x32_bf16 v[196:199], a[144:147], a[80:83], v[196:199]\n"
"global_load_lds_dwordx4 v219, s[50:51] offset:0\n"
"v_cvt_pk_bf16_f32 v157, v254, v255\n"
"v_mfma_f32_16x16x32_bf16 v[248:251], a[144:147], a[96:99], -4.0\n"
"v_exp_f32 v192, v192\n"
"v_mfma_f32_16x16x32_bf16 v[248:251], a[148:151], a[100:103], v[248:251]\n"
"ds_read_b128 a[140:143], v216 offset:49920\n"
"v_lshlrev_b32 v234, 16, v247\n"
"v_mfma_f32_16x16x32_bf16 v[248:251], a[152:155], a[104:107], v[248:251]\n"
"v_exp_f32 v193, v193\n"
"v_mfma_f32_16x16x32_bf16 v[248:251], a[156:159], a[108:111], v[248:251]\n"
"ds_read_b64_tr_b16 a[160:161], v217 offset:32768\n"
"buffer_store_dwordx4 a[236:239], v223, s[56:59], s71 offen offset:2048 sc1 nt\n"
"v_cvt_pk_bf16_f32 v134, v192, v193\n"
"v_and_b32 v235, s67, v247\n"
"v_pk_mul_f32 v[228:229], v[228:229], s[68:69]\n"
"v_pk_mul_f32 v[230:231], v[230:231], s[68:69]\n"
"v_pk_mul_f32 v[232:233], v[232:233], s[68:69]\n"
"v_pk_mul_f32 v[234:235], v[234:235], s[68:69]\n"
"v_cvt_pk_bf16_f32 v228, v228, v229\n"
"v_cvt_pk_bf16_f32 v230, v230, v231\n"
"v_mfma_f32_16x16x32_bf16 v[252:255], a[156:159], a[124:127], -4.0\n"
"v_exp_f32 v194, v194\n"
"v_mfma_f32_16x16x32_bf16 v[252:255], a[152:155], a[120:123], v[252:255]\n"
"ds_read_b64_tr_b16 a[162:163], v217 offset:36864\n"
"v_cvt_pk_bf16_f32 v232, v232, v233\n"
"v_mfma_f32_16x16x32_bf16 v[252:255], a[148:151], a[116:119], v[252:255]\n"
"v_exp_f32 v195, v195\n"
"v_mfma_f32_16x16x32_bf16 v[252:255], a[144:147], a[112:115], v[252:255]\n"
"v_cvt_pk_bf16_f32 v135, v194, v195\n"
"v_cvt_pk_bf16_f32 v234, v234, v235\n"
"v_mfma_f32_16x16x32_bf16 v[96:99], a[176:179], v[160:163], v[96:99]\n"
"s_waitcnt lgkmcnt(4)\n"
"v_exp_f32 v196, v196\n"
"v_mfma_f32_16x16x32_bf16 v[96:99], a[180:183], v[164:167], v[96:99]\n"
"ds_read_b64_tr_b16 a[164:165], v217 offset:40960\n"
"v_accvgpr_write_b32 a8, v228\n"
"v_mfma_f32_16x16x32_bf16 v[100:103], a[188:191], v[164:167], v[100:103]\n"
"v_exp_f32 v197, v197\n"
"v_mfma_f32_16x16x32_bf16 v[100:103], a[184:187], v[160:163], v[100:103]\n"
"ds_read_b64_tr_b16 a[166:167], v217 offset:45056\n"
"v_cvt_pk_bf16_f32 v142, v196, v197\n"
"v_mfma_f32_16x16x32_bf16 v[108:111], a[184:187], v[168:171], v[108:111]\n"
"v_exp_f32 v198, v198\n"
"v_mfma_f32_16x16x32_bf16 v[108:111], a[188:191], v[172:175], v[108:111]\n"
"ds_read_b64_tr_b16 a[168:169], v217 offset:32776\n"
"v_accvgpr_write_b32 a9, v230\n"
"v_mfma_f32_16x16x32_bf16 v[104:107], a[180:183], v[172:175], v[104:107]\n"
"s_add_u32 m0, s65, 0\n"
"v_exp_f32 v199, v199\n"
"v_mfma_f32_16x16x32_bf16 v[104:107], a[176:179], v[168:171], v[104:107]\n"
"global_load_lds_dwordx4 v219, s[50:51] offset:1024\n"
"v_cvt_pk_bf16_f32 v143, v198, v199\n"
"v_mfma_f32_16x16x32_bf16 v[112:115], a[176:179], v[176:179], v[112:115]\n"
"v_exp_f32 v248, v248\n"
"v_mfma_f32_16x16x32_bf16 v[112:115], a[180:183], v[180:183], v[112:115]\n"
"s_waitcnt vmcnt(15)\n"
"ds_read_b64_tr_b16 a[170:171], v217 offset:36872\n"
"ds_read_b128 v[204:207], v218 offset:4096\n"
"v_mfma_f32_16x16x32_bf16 v[116:119], a[188:191], v[180:183], v[116:119]\n"
"v_exp_f32 v249, v249\n"
"v_mfma_f32_16x16x32_bf16 v[116:119], a[184:187], v[176:179], v[116:119]\n"
"ds_read_b64_tr_b16 a[172:173], v217 offset:40968\n"
"v_cvt_pk_bf16_f32 v150, v248, v249\n"
"v_mfma_f32_16x16x32_bf16 v[124:127], a[184:187], v[184:187], v[124:127]\n"
"v_exp_f32 v250, v250\n"
"v_mfma_f32_16x16x32_bf16 v[124:127], a[188:191], v[188:191], v[124:127]\n"
"ds_read_b64_tr_b16 a[174:175], v217 offset:45064\n"
"v_accvgpr_write_b32 a10, v232\n"
"v_mfma_f32_16x16x32_bf16 v[120:123], a[180:183], v[188:191], v[120:123]\n"
"s_add_u32 m0, s65, 0\n"
"v_exp_f32 v251, v251\n"
"v_mfma_f32_16x16x32_bf16 v[120:123], a[176:179], v[184:187], v[120:123]\n"
"global_load_lds_dwordx4 v219, s[50:51] offset:2048\n"
"v_cvt_pk_bf16_f32 v151, v250, v251\n"
"v_mfma_f32_16x16x32_bf16 v[192:195], a[128:131], a[64:67], -4.0\n"
"v_exp_f32 v252, v252\n"
"v_mfma_f32_16x16x32_bf16 v[192:195], a[132:135], a[68:71], v[192:195]\n"
"s_waitcnt lgkmcnt(6)\n"
"ds_read_b128 a[144:147], v216 offset:53248\n"
"v_accvgpr_write_b32 a11, v234\n"
"v_mfma_f32_16x16x32_bf16 v[192:195], a[136:139], a[72:75], v[192:195]\n"
"v_exp_f32 v253, v253\n"
"v_mfma_f32_16x16x32_bf16 v[192:195], a[140:143], a[76:79], v[192:195]\n"
"ds_read_b128 a[148:151], v216 offset:53504\n"
"v_cvt_pk_bf16_f32 v158, v252, v253\n"
"v_mfma_f32_16x16x32_bf16 v[196:199], a[140:143], a[92:95], -4.0\n"
"v_exp_f32 v254, v254\n"
"v_mfma_f32_16x16x32_bf16 v[196:199], a[136:139], a[88:91], v[196:199]\n"
"ds_read_b128 a[152:155], v216 offset:53760\n"
"v_lshlrev_b32 v228, 16, v200\n"
"v_mfma_f32_16x16x32_bf16 v[196:199], a[132:135], a[84:87], v[196:199]\n"
"v_exp_f32 v255, v255\n"
"v_mfma_f32_16x16x32_bf16 v[196:199], a[128:131], a[80:83], v[196:199]\n"
"ds_read_b128 v[244:247], v218 offset:4352\n"
"v_cvt_pk_bf16_f32 v159, v254, v255\n"
"v_mfma_f32_16x16x32_bf16 v[248:251], a[128:131], a[96:99], -4.0\n"
"v_exp_f32 v192, v192\n"
"v_mfma_f32_16x16x32_bf16 v[248:251], a[132:135], a[100:103], v[248:251]\n"
"ds_read_b128 a[156:159], v216 offset:54016\n"
"v_and_b32 v229, s67, v200\n"
"v_mfma_f32_16x16x32_bf16 v[248:251], a[136:139], a[104:107], v[248:251]\n"
"v_exp_f32 v193, v193\n"
"v_mfma_f32_16x16x32_bf16 v[248:251], a[140:143], a[108:111], v[248:251]\n"
"ds_read_b64_tr_b16 a[176:177], v217 offset:33024\n"
"v_cvt_pk_bf16_f32 v160, v192, v193\n"
"v_mfma_f32_16x16x32_bf16 v[252:255], a[140:143], a[124:127], -4.0\n"
"v_exp_f32 v194, v194\n"
"v_mfma_f32_16x16x32_bf16 v[252:255], a[136:139], a[120:123], v[252:255]\n"
"ds_read_b64_tr_b16 a[178:179], v217 offset:37120\n"
"v_lshlrev_b32 v230, 16, v201\n"
"v_mfma_f32_16x16x32_bf16 v[252:255], a[132:135], a[116:119], v[252:255]\n"
"s_add_u32 m0, s65, 0\n"
"v_exp_f32 v195, v195\n"
"v_mfma_f32_16x16x32_bf16 v[252:255], a[128:131], a[112:115], v[252:255]\n"
"global_load_lds_dwordx4 v219, s[50:51] offset:3072\n"
"v_cvt_pk_bf16_f32 v161, v194, v195\n"
"v_smfmac_f32_16x16x64_bf16 a[192:195], a[208:211], v[128:135], v215\n"
"v_and_b32 v231, s67, v201\n"
"v_smfmac_f32_16x16x64_bf16 a[196:199], a[208:211], v[136:143], v215\n"
"ds_read_b64_tr_b16 a[180:181], v217 offset:41216\n"
"v_smfmac_f32_16x16x64_bf16 a[200:203], a[208:211], v[144:151], v215\n"
"v_lshlrev_b32 v232, 16, v202\n"
"v_and_b32 v233, s67, v202\n"
"v_lshlrev_b32 v234, 16, v203\n"
"v_and_b32 v235, s67, v203\n"
"v_pk_mul_f32 v[228:229], v[228:229], s[68:69]\n"
"v_pk_mul_f32 v[230:231], v[230:231], s[68:69]\n"
"v_pk_mul_f32 v[232:233], v[232:233], s[68:69]\n"
"v_pk_mul_f32 v[234:235], v[234:235], s[68:69]\n"
"v_cvt_pk_bf16_f32 v228, v228, v229\n"
"v_cvt_pk_bf16_f32 v230, v230, v231\n"
"v_smfmac_f32_16x16x64_bf16 a[204:207], a[208:211], v[152:159], v215\n"
"ds_read_b64_tr_b16 a[182:183], v217 offset:45312\n"
"v_mfma_f32_16x16x32_bf16 v[0:3], a[160:163], v[128:131], v[0:3]\n"
"s_waitcnt lgkmcnt(6)\n"
"v_exp_f32 v196, v196\n"
"v_mfma_f32_16x16x32_bf16 v[0:3], a[164:167], v[132:135], v[0:3]\n"
"ds_read_b64_tr_b16 a[184:185], v217 offset:33032\n"
"v_cvt_pk_bf16_f32 v232, v232, v233\n"
"v_mfma_f32_16x16x32_bf16 v[4:7], a[172:175], v[132:135], v[4:7]\n"
"s_add_u32 m0, s66, 0\n"
"v_exp_f32 v197, v197\n"
"v_mfma_f32_16x16x32_bf16 v[4:7], a[168:171], v[128:131], v[4:7]\n"
"global_load_lds_dwordx4 v222, s[52:53] offset:2048\n"
"v_cvt_pk_bf16_f32 v168, v196, v197\n"
"v_mfma_f32_16x16x32_bf16 v[12:15], a[168:171], v[136:139], v[12:15]\n"
"v_exp_f32 v198, v198\n"
"v_mfma_f32_16x16x32_bf16 v[12:15], a[172:175], v[140:143], v[12:15]\n"
"s_bfe_u32 s48, s38, 0x30014\n"
"ds_read_b64_tr_b16 a[186:187], v217 offset:37128\n"
"v_cvt_pk_bf16_f32 v234, v234, v235\n"
"v_mfma_f32_16x16x32_bf16 v[8:11], a[164:167], v[140:143], v[8:11]\n"
"s_lshl_b32 s48, s48, 14\n"
"v_exp_f32 v199, v199\n"
"v_mfma_f32_16x16x32_bf16 v[8:11], a[160:163], v[136:139], v[8:11]\n"
"s_add_u32 s36, s79, s48\n"
"ds_read_b64_tr_b16 a[188:189], v217 offset:41224\n"
"v_cvt_pk_bf16_f32 v169, v198, v199\n"
"v_mfma_f32_16x16x32_bf16 v[16:19], a[160:163], v[144:147], v[16:19]\n"
"s_add_u32 s48, s42, s36\n"
"v_exp_f32 v248, v248\n"
"v_mfma_f32_16x16x32_bf16 v[16:19], a[164:167], v[148:151], v[16:19]\n"
"s_addc_u32 s49, s43, 0\n"
"ds_read_b64_tr_b16 a[190:191], v217 offset:45320\n"
"v_accvgpr_write_b32 a12, v228\n"
"v_mfma_f32_16x16x32_bf16 v[20:23], a[172:175], v[148:151], v[20:23]\n"
"s_add_u32 m0, s64, 16384\n"
"v_exp_f32 v249, v249\n"
"v_mfma_f32_16x16x32_bf16 v[20:23], a[168:171], v[144:147], v[20:23]\n"
"global_load_lds_dwordx4 v219, s[48:49] offset:0\n"
"v_cvt_pk_bf16_f32 v176, v248, v249\n"
"v_mfma_f32_16x16x32_bf16 v[28:31], a[168:171], v[152:155], v[28:31]\n"
"v_exp_f32 v250, v250\n"
"v_mfma_f32_16x16x32_bf16 v[28:31], a[172:175], v[156:159], v[28:31]\n"
"v_accvgpr_write_b32 a13, v230\n"
"v_accvgpr_write_b32 a14, v232\n"
"v_mfma_f32_16x16x32_bf16 v[24:27], a[164:167], v[156:159], v[24:27]\n"
"v_exp_f32 v251, v251\n"
"v_mfma_f32_16x16x32_bf16 v[24:27], a[160:163], v[152:155], v[24:27]\n"
"v_cvt_pk_bf16_f32 v177, v250, v251\n"
"v_accvgpr_write_b32 a15, v234\n"
"v_mfma_f32_16x16x32_bf16 v[192:195], a[144:147], a[64:67], -4.0\n"
"v_exp_f32 v252, v252\n"
"v_mfma_f32_16x16x32_bf16 v[192:195], a[148:151], a[68:71], v[192:195]\n"
"ds_read_b128 a[128:131], v216 offset:57344\n"
"v_lshlrev_b32 v228, 16, v204\n"
"v_mfma_f32_16x16x32_bf16 v[192:195], a[152:155], a[72:75], v[192:195]\n"
"s_waitcnt lgkmcnt(4)\n"
"v_exp_f32 v253, v253\n"
"v_mfma_f32_16x16x32_bf16 v[192:195], a[156:159], a[76:79], v[192:195]\n"
"ds_read_b128 a[132:135], v216 offset:57600\n"
"ds_read_b128 v[200:203], v218 offset:4608\n"
"v_cvt_pk_bf16_f32 v184, v252, v253\n"
"v_mfma_f32_16x16x32_bf16 v[196:199], a[156:159], a[92:95], -4.0\n"
"v_exp_f32 v254, v254\n"
"v_mfma_f32_16x16x32_bf16 v[196:199], a[152:155], a[88:91], v[196:199]\n"
"ds_read_b128 a[136:139], v216 offset:57856\n"
"v_and_b32 v229, s67, v204\n"
"v_mfma_f32_16x16x32_bf16 v[196:199], a[148:151], a[84:87], v[196:199]\n"
"s_add_u32 m0, s64, 16384\n"
"v_exp_f32 v255, v255\n"
"v_mfma_f32_16x16x32_bf16 v[196:199], a[144:147], a[80:83], v[196:199]\n"
"global_load_lds_dwordx4 v219, s[48:49] offset:1024\n"
"v_cvt_pk_bf16_f32 v185, v254, v255\n"
"v_mfma_f32_16x16x32_bf16 v[248:251], a[144:147], a[96:99], -4.0\n"
"v_exp_f32 v192, v192\n"
"v_mfma_f32_16x16x32_bf16 v[248:251], a[148:151], a[100:103], v[248:251]\n"
"ds_read_b128 a[140:143], v216 offset:58112\n"
"v_lshlrev_b32 v230, 16, v205\n"
"v_mfma_f32_16x16x32_bf16 v[248:251], a[152:155], a[104:107], v[248:251]\n"
"v_exp_f32 v193, v193\n"
"v_mfma_f32_16x16x32_bf16 v[248:251], a[156:159], a[108:111], v[248:251]\n"
"ds_read_b64_tr_b16 a[160:161], v217 offset:33280\n"
"buffer_store_dwordx4 a[240:243], v223, s[56:59], s37 offen offset:0 sc1 nt\n"
"v_cvt_pk_bf16_f32 v162, v192, v193\n"
"v_and_b32 v231, s67, v205\n"
"v_mfma_f32_16x16x32_bf16 v[252:255], a[156:159], a[124:127], -4.0\n"
"v_exp_f32 v194, v194\n"
"v_mfma_f32_16x16x32_bf16 v[252:255], a[152:155], a[120:123], v[252:255]\n"
"ds_read_b64_tr_b16 a[162:163], v217 offset:37376\n"
"v_lshlrev_b32 v232, 16, v206\n"
"v_mfma_f32_16x16x32_bf16 v[252:255], a[148:151], a[116:119], v[252:255]\n"
"s_add_u32 m0, s64, 16384\n"
"v_exp_f32 v195, v195\n"
"v_mfma_f32_16x16x32_bf16 v[252:255], a[144:147], a[112:115], v[252:255]\n"
"global_load_lds_dwordx4 v220, s[48:49] offset:2048\n"
"v_cvt_pk_bf16_f32 v163, v194, v195\n"
"v_mfma_f32_16x16x32_bf16 v[32:35], a[176:179], v[128:131], v[32:35]\n"
"v_exp_f32 v196, v196\n"
"v_mfma_f32_16x16x32_bf16 v[32:35], a[180:183], v[132:135], v[32:35]\n"
"s_waitcnt lgkmcnt(6)\n"
"ds_read_b64_tr_b16 a[164:165], v217 offset:41472\n"
"v_and_b32 v233, s67, v206\n"
"v_mfma_f32_16x16x32_bf16 v[36:39], a[188:191], v[132:135], v[36:39]\n"
"v_exp_f32 v197, v197\n"
"v_mfma_f32_16x16x32_bf16 v[36:39], a[184:187], v[128:131], v[36:39]\n"
"ds_read_b64_tr_b16 a[166:167], v217 offset:45568\n"
"v_cvt_pk_bf16_f32 v170, v196, v197\n"
"v_mfma_f32_16x16x32_bf16 v[44:47], a[184:187], v[136:139], v[44:47]\n"
"v_exp_f32 v198, v198\n"
"v_mfma_f32_16x16x32_bf16 v[44:47], a[188:191], v[140:143], v[44:47]\n"
"ds_read_b64_tr_b16 a[168:169], v217 offset:33288\n"
"v_lshlrev_b32 v234, 16, v207\n"
"v_mfma_f32_16x16x32_bf16 v[40:43], a[180:183], v[140:143], v[40:43]\n"
"v_exp_f32 v199, v199\n"
"v_mfma_f32_16x16x32_bf16 v[40:43], a[176:179], v[136:139], v[40:43]\n"
"v_cvt_pk_bf16_f32 v171, v198, v199\n"
"v_and_b32 v235, s67, v207\n"
"v_mfma_f32_16x16x32_bf16 v[48:51], a[176:179], v[144:147], v[48:51]\n"
"v_exp_f32 v248, v248\n"
"v_mfma_f32_16x16x32_bf16 v[48:51], a[180:183], v[148:151], v[48:51]\n"
"ds_read_b64_tr_b16 a[170:171], v217 offset:37384\n"
"v_mul_f32 v228, s68, v228\n"
"v_mfma_f32_16x16x32_bf16 v[52:55], a[188:191], v[148:151], v[52:55]\n"
"v_exp_f32 v249, v249\n"
"v_mfma_f32_16x16x32_bf16 v[52:55], a[184:187], v[144:147], v[52:55]\n"
"ds_read_b64_tr_b16 a[172:173], v217 offset:41480\n"
"v_cvt_pk_bf16_f32 v178, v248, v249\n"
"v_mfma_f32_16x16x32_bf16 v[60:63], a[184:187], v[152:155], v[60:63]\n"
"v_exp_f32 v250, v250\n"
"v_mfma_f32_16x16x32_bf16 v[60:63], a[188:191], v[156:159], v[60:63]\n"
"ds_read_b64_tr_b16 a[174:175], v217 offset:45576\n"
"v_mul_f32 v229, s68, v229\n"
"v_mfma_f32_16x16x32_bf16 v[56:59], a[180:183], v[156:159], v[56:59]\n"
"s_add_u32 m0, s64, 16384\n"
"v_exp_f32 v251, v251\n"
"v_mfma_f32_16x16x32_bf16 v[56:59], a[176:179], v[152:155], v[56:59]\n"
"global_load_lds_dwordx4 v220, s[48:49] offset:3072\n"
"v_cvt_pk_bf16_f32 v179, v250, v251\n"
"v_mfma_f32_16x16x32_bf16 v[192:195], a[128:131], a[64:67], -4.0\n"
"s_waitcnt lgkmcnt(6)\n"
"v_exp_f32 v252, v252\n"
"v_mfma_f32_16x16x32_bf16 v[192:195], a[132:135], a[68:71], v[192:195]\n"
"ds_read_b128 a[144:147], v216 offset:61440\n"
"v_mul_f32 v230, s68, v230\n"
"v_mfma_f32_16x16x32_bf16 v[192:195], a[136:139], a[72:75], v[192:195]\n"
"v_exp_f32 v253, v253\n"
"v_mfma_f32_16x16x32_bf16 v[192:195], a[140:143], a[76:79], v[192:195]\n"
"ds_read_b128 a[148:151], v216 offset:61696\n"
"v_cvt_pk_bf16_f32 v186, v252, v253\n"
"v_mfma_f32_16x16x32_bf16 v[196:199], a[140:143], a[92:95], -4.0\n"
"v_exp_f32 v254, v254\n"
"v_mfma_f32_16x16x32_bf16 v[196:199], a[136:139], a[88:91], v[196:199]\n"
"ds_read_b128 a[152:155], v216 offset:61952\n"
"v_mul_f32 v231, s68, v231\n"
"v_mfma_f32_16x16x32_bf16 v[196:199], a[132:135], a[84:87], v[196:199]\n"
"s_add_u32 m0, s66, 0\n"
"v_exp_f32 v255, v255\n"
"v_mfma_f32_16x16x32_bf16 v[196:199], a[128:131], a[80:83], v[196:199]\n"
"global_load_lds_dwordx4 v222, s[52:53] offset:3072\n"
"v_cvt_pk_bf16_f32 v187, v254, v255\n"
"v_mul_f32 v232, s68, v232\n"
"v_mul_f32 v233, s68, v233\n"
"v_mul_f32 v234, s68, v234\n"
"v_mfma_f32_16x16x32_bf16 v[248:251], a[128:131], a[96:99], -4.0\n"
"v_exp_f32 v192, v192\n"
"v_mfma_f32_16x16x32_bf16 v[248:251], a[132:135], a[100:103], v[248:251]\n"
"ds_read_b128 a[156:159], v216 offset:62208\n"
"v_mul_f32 v235, s68, v235\n"
"v_mfma_f32_16x16x32_bf16 v[248:251], a[136:139], a[104:107], v[248:251]\n"
"v_exp_f32 v193, v193\n"
"v_mfma_f32_16x16x32_bf16 v[248:251], a[140:143], a[108:111], v[248:251]\n"
"ds_read_b64_tr_b16 a[176:177], v217 offset:33536\n"
"v_cvt_pk_bf16_f32 v164, v192, v193\n"
"v_mfma_f32_16x16x32_bf16 v[252:255], a[140:143], a[124:127], -4.0\n"
"v_exp_f32 v194, v194\n"
"v_mfma_f32_16x16x32_bf16 v[252:255], a[136:139], a[120:123], v[252:255]\n"
"ds_read_b64_tr_b16 a[178:179], v217 offset:37632\n"
"v_cvt_pk_bf16_f32 v228, v228, v229\n"
"v_mfma_f32_16x16x32_bf16 v[252:255], a[132:135], a[116:119], v[252:255]\n"
"v_exp_f32 v195, v195\n"
"v_mfma_f32_16x16x32_bf16 v[252:255], a[128:131], a[112:115], v[252:255]\n"
".Lattn_pad2:\n"
"s_cbranch_vccnz .Lattn_ret\n"
"v_cvt_pk_bf16_f32 v165, v194, v195\n"
"v_cvt_pk_bf16_f32 v230, v230, v231\n"
"v_mfma_f32_16x16x32_bf16 v[64:67], a[160:163], v[128:131], v[64:67]\n"
"s_waitcnt lgkmcnt(5)\n"
"v_exp_f32 v196, v196\n"
"v_mfma_f32_16x16x32_bf16 v[64:67], a[164:167], v[132:135], v[64:67]\n"
"ds_read_b64_tr_b16 a[180:181], v217 offset:41728\n"
"v_cvt_pk_bf16_f32 v232, v232, v233\n"
"v_mfma_f32_16x16x32_bf16 v[68:71], a[172:175], v[132:135], v[68:71]\n"
"v_exp_f32 v197, v197\n"
"v_mfma_f32_16x16x32_bf16 v[68:71], a[168:171], v[128:131], v[68:71]\n"
"ds_read_b64_tr_b16 a[182:183], v217 offset:45824\n"
"v_cvt_pk_bf16_f32 v172, v196, v197\n"
"v_mfma_f32_16x16x32_bf16 v[76:79], a[168:171], v[136:139], v[76:79]\n"
"v_exp_f32 v198, v198\n"
"v_mfma_f32_16x16x32_bf16 v[76:79], a[172:175], v[140:143], v[76:79]\n"
"ds_read_b64_tr_b16 a[184:185], v217 offset:33544\n"
"v_cvt_pk_bf16_f32 v234, v234, v235\n"
"v_mfma_f32_16x16x32_bf16 v[72:75], a[164:167], v[140:143], v[72:75]\n"
"v_exp_f32 v199, v199\n"
"v_mfma_f32_16x16x32_bf16 v[72:75], a[160:163], v[136:139], v[72:75]\n"
"v_cvt_pk_bf16_f32 v173, v198, v199\n"
"v_accvgpr_write_b32 a16, v228\n"
"v_mfma_f32_16x16x32_bf16 v[80:83], a[160:163], v[144:147], v[80:83]\n"
"v_exp_f32 v248, v248\n"
"v_mfma_f32_16x16x32_bf16 v[80:83], a[164:167], v[148:151], v[80:83]\n"
"ds_read_b64_tr_b16 a[186:187], v217 offset:37640\n"
"v_accvgpr_write_b32 a17, v230\n"
"v_mfma_f32_16x16x32_bf16 v[84:87], a[172:175], v[148:151], v[84:87]\n"
"v_exp_f32 v249, v249\n"
"v_mfma_f32_16x16x32_bf16 v[84:87], a[168:171], v[144:147], v[84:87]\n"
"ds_read_b64_tr_b16 a[188:189], v217 offset:41736\n"
"v_cvt_pk_bf16_f32 v180, v248, v249\n"
"v_mfma_f32_16x16x32_bf16 v[92:95], a[168:171], v[152:155], v[92:95]\n"
"v_exp_f32 v250, v250\n"
"v_mfma_f32_16x16x32_bf16 v[92:95], a[172:175], v[156:159], v[92:95]\n"
"ds_read_b64_tr_b16 a[190:191], v217 offset:45832\n"
"v_accvgpr_write_b32 a18, v232\n"
"v_mfma_f32_16x16x32_bf16 v[88:91], a[164:167], v[156:159], v[88:91]\n"
"v_exp_f32 v251, v251\n"
"v_mfma_f32_16x16x32_bf16 v[88:91], a[160:163], v[152:155], v[88:91]\n"
"s_waitcnt vmcnt(13) lgkmcnt(6)\n"
"v_cvt_pk_bf16_f32 v181, v250, v251\n"
"v_accvgpr_write_b32 a19, v234\n"
"v_mfma_f32_16x16x32_bf16 v[192:195], a[144:147], a[64:67], -4.0\n"
"s_barrier\n"
"v_exp_f32 v252, v252\n"
"v_mfma_f32_16x16x32_bf16 v[192:195], a[148:151], a[68:71], v[192:195]\n"
"s_bfe_u32 s50, s38, 0x30014\n"
"ds_read_b128 a[128:131], v216 offset:0\n"
"v_lshlrev_b32 v228, 16, v244\n"
"v_mfma_f32_16x16x32_bf16 v[192:195], a[152:155], a[72:75], v[192:195]\n"
"s_lshl_b32 s50, s50, 14\n"
"v_exp_f32 v253, v253\n"
"v_mfma_f32_16x16x32_bf16 v[192:195], a[156:159], a[76:79], v[192:195]\n"
"s_add_u32 s39, s54, s50\n"
"ds_read_b128 a[132:135], v216 offset:256\n"
"ds_read_b128 v[204:207], v218 offset:4864\n"
"v_cvt_pk_bf16_f32 v188, v252, v253\n"
"v_mfma_f32_16x16x32_bf16 v[196:199], a[156:159], a[92:95], -4.0\n"
"s_add_u32 s50, s44, s39\n"
"v_exp_f32 v254, v254\n"
"v_mfma_f32_16x16x32_bf16 v[196:199], a[152:155], a[88:91], v[196:199]\n"
"s_addc_u32 s51, s45, 0\n"
"ds_read_b128 a[136:139], v216 offset:512\n"
"v_and_b32 v229, s67, v244\n"
"v_mfma_f32_16x16x32_bf16 v[196:199], a[148:151], a[84:87], v[196:199]\n"
"s_add_u32 m0, s65, 16384\n"
"v_exp_f32 v255, v255\n"
"v_mfma_f32_16x16x32_bf16 v[196:199], a[144:147], a[80:83], v[196:199]\n"
"global_load_lds_dwordx4 v219, s[50:51] offset:0\n"
"v_cvt_pk_bf16_f32 v189, v254, v255\n"
"v_mfma_f32_16x16x32_bf16 v[248:251], a[144:147], a[96:99], -4.0\n"
"v_exp_f32 v192, v192\n"
"v_mfma_f32_16x16x32_bf16 v[248:251], a[148:151], a[100:103], v[248:251]\n"
"ds_read_b128 a[140:143], v216 offset:768\n"
"v_lshlrev_b32 v230, 16, v245\n"
"v_mfma_f32_16x16x32_bf16 v[248:251], a[152:155], a[104:107], v[248:251]\n"
"v_exp_f32 v193, v193\n"
"v_mfma_f32_16x16x32_bf16 v[248:251], a[156:159], a[108:111], v[248:251]\n"
"s_add_u32 s35, s55, s37\n"
"ds_read_b64_tr_b16 a[160:161], v217 offset:49152\n"
"buffer_store_dwordx4 a[244:247], v223, s[56:59], s37 offen offset:2048 sc1 nt\n"
"v_cvt_pk_bf16_f32 v166, v192, v193\n"
"v_and_b32 v231, s67, v245\n"
"v_mfma_f32_16x16x32_bf16 v[252:255], a[156:159], a[124:127], -4.0\n"
"s_add_u32 s52, s40, s35\n"
"v_exp_f32 v194, v194\n"
"v_mfma_f32_16x16x32_bf16 v[252:255], a[152:155], a[120:123], v[252:255]\n"
"s_addc_u32 s53, s41, 0\n"
"ds_read_b64_tr_b16 a[162:163], v217 offset:53248\n"
"v_lshlrev_b32 v232, 16, v246\n"
"v_mfma_f32_16x16x32_bf16 v[252:255], a[148:151], a[116:119], v[252:255]\n"
"s_add_u32 m0, s66, 4096\n"
"v_exp_f32 v195, v195\n"
"v_mfma_f32_16x16x32_bf16 v[252:255], a[144:147], a[112:115], v[252:255]\n"
"s_waitcnt lgkmcnt(4)\n"
"global_load_lds_dwordx4 v221, s[52:53] offset:0\n"
"v_cvt_pk_bf16_f32 v167, v194, v195\n"
"v_and_b32 v233, s67, v246\n"
"v_lshlrev_b32 v234, 16, v247\n"
"v_and_b32 v235, s67, v247\n"
"v_pk_mul_f32 v[228:229], v[228:229], s[68:69]\n"
"v_pk_mul_f32 v[230:231], v[230:231], s[68:69]\n"
"v_pk_mul_f32 v[232:233], v[232:233], s[68:69]\n"
"v_pk_mul_f32 v[234:235], v[234:235], s[68:69]\n"
"v_cvt_pk_bf16_f32 v228, v228, v229\n"
"v_cvt_pk_bf16_f32 v230, v230, v231\n"
"v_mfma_f32_16x16x32_bf16 v[96:99], a[176:179], v[128:131], v[96:99]\n"
"v_exp_f32 v196, v196\n"
"v_mfma_f32_16x16x32_bf16 v[96:99], a[180:183], v[132:135], v[96:99]\n"
"ds_read_b64_tr_b16 a[164:165], v217 offset:57344\n"
"v_cvt_pk_bf16_f32 v232, v232, v233\n"
"v_mfma_f32_16x16x32_bf16 v[100:103], a[188:191], v[132:135], v[100:103]\n"
"v_exp_f32 v197, v197\n"
"v_mfma_f32_16x16x32_bf16 v[100:103], a[184:187], v[128:131], v[100:103]\n"
"ds_read_b64_tr_b16 a[166:167], v217 offset:61440\n"
"v_cvt_pk_bf16_f32 v174, v196, v197\n"
"v_mfma_f32_16x16x32_bf16 v[108:111], a[184:187], v[136:139], v[108:111]\n"
"v_exp_f32 v198, v198\n"
"v_mfma_f32_16x16x32_bf16 v[108:111], a[188:191], v[140:143], v[108:111]\n"
"ds_read_b64_tr_b16 a[168:169], v217 offset:49160\n"
"v_cvt_pk_bf16_f32 v234, v234, v235\n"
"v_mfma_f32_16x16x32_bf16 v[104:107], a[180:183], v[140:143], v[104:107]\n"
"s_add_u32 m0, s65, 16384\n"
"v_exp_f32 v199, v199\n"
"v_mfma_f32_16x16x32_bf16 v[104:107], a[176:179], v[136:139], v[104:107]\n"
"global_load_lds_dwordx4 v219, s[50:51] offset:1024\n"
"v_cvt_pk_bf16_f32 v175, v198, v199\n"
"v_mfma_f32_16x16x32_bf16 v[112:115], a[176:179], v[144:147], v[112:115]\n"
"v_exp_f32 v248, v248\n"
"v_mfma_f32_16x16x32_bf16 v[112:115], a[180:183], v[148:151], v[112:115]\n"
"ds_read_b64_tr_b16 a[170:171], v217 offset:53256\n"
"v_accvgpr_write_b32 a20, v228\n"
"v_mfma_f32_16x16x32_bf16 v[116:119], a[188:191], v[148:151], v[116:119]\n"
"v_exp_f32 v249, v249\n"
"v_mfma_f32_16x16x32_bf16 v[116:119], a[184:187], v[144:147], v[116:119]\n"
"ds_read_b64_tr_b16 a[172:173], v217 offset:57352\n"
"v_cvt_pk_bf16_f32 v182, v248, v249\n"
"v_mfma_f32_16x16x32_bf16 v[124:127], a[184:187], v[152:155], v[124:127]\n"
"v_exp_f32 v250, v250\n"
"v_mfma_f32_16x16x32_bf16 v[124:127], a[188:191], v[156:159], v[124:127]\n"
"ds_read_b64_tr_b16 a[174:175], v217 offset:61448\n"
"v_accvgpr_write_b32 a21, v230\n"
"v_mfma_f32_16x16x32_bf16 v[120:123], a[180:183], v[156:159], v[120:123]\n"
"s_add_u32 m0, s65, 16384\n"
"v_exp_f32 v251, v251\n"
"v_mfma_f32_16x16x32_bf16 v[120:123], a[176:179], v[152:155], v[120:123]\n"
"global_load_lds_dwordx4 v219, s[50:51] offset:2048\n"
"v_cvt_pk_bf16_f32 v183, v250, v251\n"
"v_mfma_f32_16x16x32_bf16 v[192:195], a[128:131], a[64:67], -4.0\n"
"v_exp_f32 v252, v252\n"
"v_mfma_f32_16x16x32_bf16 v[192:195], a[132:135], a[68:71], v[192:195]\n"
"s_waitcnt lgkmcnt(5)\n"
"ds_read_b128 a[144:147], v216 offset:4096\n"
"v_accvgpr_write_b32 a22, v232\n"
"v_mfma_f32_16x16x32_bf16 v[192:195], a[136:139], a[72:75], v[192:195]\n"
"v_exp_f32 v253, v253\n"
"v_mfma_f32_16x16x32_bf16 v[192:195], a[140:143], a[76:79], v[192:195]\n"
"ds_read_b128 a[148:151], v216 offset:4352\n"
"v_cvt_pk_bf16_f32 v190, v252, v253\n"
"v_mfma_f32_16x16x32_bf16 v[196:199], a[140:143], a[92:95], -4.0\n"
"v_exp_f32 v254, v254\n"
"v_mfma_f32_16x16x32_bf16 v[196:199], a[136:139], a[88:91], v[196:199]\n"
"ds_read_b128 a[152:155], v216 offset:4608\n"
"v_accvgpr_write_b32 a23, v234\n"
"v_mfma_f32_16x16x32_bf16 v[196:199], a[132:135], a[84:87], v[196:199]\n"
"v_exp_f32 v255, v255\n"
"v_mfma_f32_16x16x32_bf16 v[196:199], a[128:131], a[80:83], v[196:199]\n"
"v_cvt_pk_bf16_f32 v191, v254, v255\n"
"v_lshlrev_b32 v228, 16, v200\n"
"v_mfma_f32_16x16x32_bf16 v[248:251], a[128:131], a[96:99], -4.0\n"
"v_exp_f32 v192, v192\n"
"v_mfma_f32_16x16x32_bf16 v[248:251], a[132:135], a[100:103], v[248:251]\n"
"ds_read_b128 a[156:159], v216 offset:4864\n"
"v_and_b32 v229, s67, v200\n"
"v_mfma_f32_16x16x32_bf16 v[248:251], a[136:139], a[104:107], v[248:251]\n"
"v_exp_f32 v193, v193\n"
"v_mfma_f32_16x16x32_bf16 v[248:251], a[140:143], a[108:111], v[248:251]\n"
"ds_read_b64_tr_b16 a[176:177], v217 offset:49408\n"
"v_cvt_pk_bf16_f32 v128, v192, v193\n"
"v_mfma_f32_16x16x32_bf16 v[252:255], a[140:143], a[124:127], -4.0\n"
"v_exp_f32 v194, v194\n"
"v_mfma_f32_16x16x32_bf16 v[252:255], a[136:139], a[120:123], v[252:255]\n"
"ds_read_b64_tr_b16 a[178:179], v217 offset:53504\n"
"v_lshlrev_b32 v230, 16, v201\n"
"v_mfma_f32_16x16x32_bf16 v[252:255], a[132:135], a[116:119], v[252:255]\n"
"s_add_u32 m0, s65, 16384\n"
"v_exp_f32 v195, v195\n"
"v_mfma_f32_16x16x32_bf16 v[252:255], a[128:131], a[112:115], v[252:255]\n"
"global_load_lds_dwordx4 v219, s[50:51] offset:3072\n"
"v_cvt_pk_bf16_f32 v129, v194, v195\n"
"v_smfmac_f32_16x16x64_bf16 a[192:195], a[208:211], v[160:167], v215\n"
"v_and_b32 v231, s67, v201\n"
"v_smfmac_f32_16x16x64_bf16 a[196:199], a[208:211], v[168:175], v215\n"
"ds_read_b64_tr_b16 a[180:181], v217 offset:57600\n"
"v_smfmac_f32_16x16x64_bf16 a[200:203], a[208:211], v[176:183], v215\n"
"v_lshlrev_b32 v232, 16, v202\n"
"v_smfmac_f32_16x16x64_bf16 a[204:207], a[208:211], v[184:191], v215\n"
"ds_read_b64_tr_b16 a[182:183], v217 offset:61696\n"
"v_mfma_f32_16x16x32_bf16 v[0:3], a[160:163], v[160:163], v[0:3]\n"
"s_waitcnt lgkmcnt(6)\n"
"v_exp_f32 v196, v196\n"
"v_mfma_f32_16x16x32_bf16 v[0:3], a[164:167], v[164:167], v[0:3]\n"
"ds_read_b64_tr_b16 a[184:185], v217 offset:49416\n"
"v_and_b32 v233, s67, v202\n"
"v_mfma_f32_16x16x32_bf16 v[4:7], a[172:175], v[164:167], v[4:7]\n"
"v_exp_f32 v197, v197\n"
"v_mfma_f32_16x16x32_bf16 v[4:7], a[168:171], v[160:163], v[4:7]\n"
"v_cvt_pk_bf16_f32 v136, v196, v197\n"
"v_lshlrev_b32 v234, 16, v203\n"
"v_mfma_f32_16x16x32_bf16 v[12:15], a[168:171], v[168:171], v[12:15]\n"
"v_exp_f32 v198, v198\n"
"v_mfma_f32_16x16x32_bf16 v[12:15], a[172:175], v[172:175], v[12:15]\n"
"s_bfe_u32 s48, s38, 0x30018\n"
"ds_read_b64_tr_b16 a[186:187], v217 offset:53512\n"
"v_and_b32 v235, s67, v203\n"
"v_mfma_f32_16x16x32_bf16 v[8:11], a[164:167], v[172:175], v[8:11]\n"
"s_lshl_b32 s48, s48, 14\n"
"v_exp_f32 v199, v199\n"
"v_mfma_f32_16x16x32_bf16 v[8:11], a[160:163], v[168:171], v[8:11]\n"
"s_add_u32 s36, s79, s48\n"
"ds_read_b64_tr_b16 a[188:189], v217 offset:57608\n"
"v_cvt_pk_bf16_f32 v137, v198, v199\n"
"v_mfma_f32_16x16x32_bf16 v[16:19], a[160:163], v[176:179], v[16:19]\n"
"s_add_u32 s48, s42, s36\n"
"v_exp_f32 v248, v248\n"
"v_mfma_f32_16x16x32_bf16 v[16:19], a[164:167], v[180:183], v[16:19]\n"
"s_addc_u32 s49, s43, 0\n"
"ds_read_b64_tr_b16 a[190:191], v217 offset:61704\n"
"v_mul_f32 v228, s68, v228\n"
"v_mfma_f32_16x16x32_bf16 v[20:23], a[172:175], v[180:183], v[20:23]\n"
"s_add_u32 m0, s64, 32768\n"
"v_exp_f32 v249, v249\n"
"v_mfma_f32_16x16x32_bf16 v[20:23], a[168:171], v[176:179], v[20:23]\n"
"global_load_lds_dwordx4 v219, s[48:49] offset:0\n"
"v_cvt_pk_bf16_f32 v144, v248, v249\n"
"v_mfma_f32_16x16x32_bf16 v[28:31], a[168:171], v[184:187], v[28:31]\n"
"v_exp_f32 v250, v250\n"
"v_mfma_f32_16x16x32_bf16 v[28:31], a[172:175], v[188:191], v[28:31]\n"
"buffer_store_dwordx4 a[248:251], v223, s[56:59], s99 offen offset:128 sc1 nt\n"
"v_mul_f32 v229, s68, v229\n"
"v_mul_f32 v230, s68, v230\n"
"v_mul_f32 v231, s68, v231\n"
"v_mfma_f32_16x16x32_bf16 v[24:27], a[164:167], v[188:191], v[24:27]\n"
"s_add_u32 m0, s66, 4096\n"
"v_exp_f32 v251, v251\n"
"v_mfma_f32_16x16x32_bf16 v[24:27], a[160:163], v[184:187], v[24:27]\n"
"global_load_lds_dwordx4 v221, s[52:53] offset:1024\n"
"v_cvt_pk_bf16_f32 v145, v250, v251\n"
"v_mfma_f32_16x16x32_bf16 v[192:195], a[144:147], a[64:67], -4.0\n"
"v_exp_f32 v252, v252\n"
"v_mfma_f32_16x16x32_bf16 v[192:195], a[148:151], a[68:71], v[192:195]\n"
"s_waitcnt lgkmcnt(3)\n"
"ds_read_b128 a[128:131], v216 offset:8192\n"
"v_mul_f32 v232, s68, v232\n"
"v_mfma_f32_16x16x32_bf16 v[192:195], a[152:155], a[72:75], v[192:195]\n"
"v_exp_f32 v253, v253\n"
"v_mfma_f32_16x16x32_bf16 v[192:195], a[156:159], a[76:79], v[192:195]\n"
"ds_read_b128 a[132:135], v216 offset:8448\n"
"v_cvt_pk_bf16_f32 v152, v252, v253\n"
"v_mfma_f32_16x16x32_bf16 v[196:199], a[156:159], a[92:95], -4.0\n"
"v_exp_f32 v254, v254\n"
"v_mfma_f32_16x16x32_bf16 v[196:199], a[152:155], a[88:91], v[196:199]\n"
"ds_read_b128 a[136:139], v216 offset:8704\n"
"v_mul_f32 v233, s68, v233\n"
"v_mfma_f32_16x16x32_bf16 v[196:199], a[148:151], a[84:87], v[196:199]\n"
"s_add_u32 m0, s64, 32768\n"
"v_exp_f32 v255, v255\n"
"v_mfma_f32_16x16x32_bf16 v[196:199], a[144:147], a[80:83], v[196:199]\n"
"global_load_lds_dwordx4 v219, s[48:49] offset:1024\n"
"v_cvt_pk_bf16_f32 v153, v254, v255\n"
"v_mfma_f32_16x16x32_bf16 v[248:251], a[144:147], a[96:99], -4.0\n"
"v_exp_f32 v192, v192\n"
"v_mfma_f32_16x16x32_bf16 v[248:251], a[148:151], a[100:103], v[248:251]\n"
"ds_read_b128 a[140:143], v216 offset:8960\n"
"v_mul_f32 v234, s68, v234\n"
"v_mfma_f32_16x16x32_bf16 v[248:251], a[152:155], a[104:107], v[248:251]\n"
"v_exp_f32 v193, v193\n"
"v_mfma_f32_16x16x32_bf16 v[248:251], a[156:159], a[108:111], v[248:251]\n"
"ds_read_b64_tr_b16 a[160:161], v217 offset:49664\n"
"v_cvt_pk_bf16_f32 v130, v192, v193\n"
"v_mfma_f32_16x16x32_bf16 v[252:255], a[156:159], a[124:127], -4.0\n"
"v_exp_f32 v194, v194\n"
"v_mfma_f32_16x16x32_bf16 v[252:255], a[152:155], a[120:123], v[252:255]\n"
"ds_read_b64_tr_b16 a[162:163], v217 offset:53760\n"
"v_mul_f32 v235, s68, v235\n"
"v_mfma_f32_16x16x32_bf16 v[252:255], a[148:151], a[116:119], v[252:255]\n"
"s_add_u32 m0, s64, 32768\n"
"v_exp_f32 v195, v195\n"
"v_mfma_f32_16x16x32_bf16 v[252:255], a[144:147], a[112:115], v[252:255]\n"
"global_load_lds_dwordx4 v220, s[48:49] offset:2048\n"
"v_cvt_pk_bf16_f32 v131, v194, v195\n"
"v_mfma_f32_16x16x32_bf16 v[32:35], a[176:179], v[160:163], v[32:35]\n"
"v_exp_f32 v196, v196\n"
"v_mfma_f32_16x16x32_bf16 v[32:35], a[180:183], v[164:167], v[32:35]\n"
"s_waitcnt lgkmcnt(5)\n"
"ds_read_b64_tr_b16 a[164:165], v217 offset:57856\n"
"v_cvt_pk_bf16_f32 v228, v228, v229\n"
"v_mfma_f32_16x16x32_bf16 v[36:39], a[188:191], v[164:167], v[36:39]\n"
"v_exp_f32 v197, v197\n"
"v_mfma_f32_16x16x32_bf16 v[36:39], a[184:187], v[160:163], v[36:39]\n"
"ds_read_b64_tr_b16 a[166:167], v217 offset:61952\n"
"v_cvt_pk_bf16_f32 v138, v196, v197\n"
"v_mfma_f32_16x16x32_bf16 v[44:47], a[184:187], v[168:171], v[44:47]\n"
"v_exp_f32 v198, v198\n"
"v_mfma_f32_16x16x32_bf16 v[44:47], a[188:191], v[172:175], v[44:47]\n"
"ds_read_b64_tr_b16 a[168:169], v217 offset:49672\n"
"v_cvt_pk_bf16_f32 v230, v230, v231\n"
"v_mfma_f32_16x16x32_bf16 v[40:43], a[180:183], v[172:175], v[40:43]\n"
"v_exp_f32 v199, v199\n"
"v_mfma_f32_16x16x32_bf16 v[40:43], a[176:179], v[168:171], v[40:43]\n"
"v_cvt_pk_bf16_f32 v139, v198, v199\n"
"v_cvt_pk_bf16_f32 v232, v232, v233\n"
"v_mfma_f32_16x16x32_bf16 v[48:51], a[176:179], v[176:179], v[48:51]\n"
"v_exp_f32 v248, v248\n"
"v_mfma_f32_16x16x32_bf16 v[48:51], a[180:183], v[180:183], v[48:51]\n"
"ds_read_b64_tr_b16 a[170:171], v217 offset:53768\n"
"v_cvt_pk_bf16_f32 v234, v234, v235\n"
"v_mfma_f32_16x16x32_bf16 v[52:55], a[188:191], v[180:183], v[52:55]\n"
"v_exp_f32 v249, v249\n"
"v_mfma_f32_16x16x32_bf16 v[52:55], a[184:187], v[176:179], v[52:55]\n"
"ds_read_b64_tr_b16 a[172:173], v217 offset:57864\n"
"v_cvt_pk_bf16_f32 v146, v248, v249\n"
"v_mfma_f32_16x16x32_bf16 v[60:63], a[184:187], v[184:187], v[60:63]\n"
"v_exp_f32 v250, v250\n"
"v_mfma_f32_16x16x32_bf16 v[60:63], a[188:191], v[188:191], v[60:63]\n"
"ds_read_b64_tr_b16 a[174:175], v217 offset:61960\n"
"v_accvgpr_write_b32 a24, v228\n"
"v_mfma_f32_16x16x32_bf16 v[56:59], a[180:183], v[188:191], v[56:59]\n"
"s_add_u32 m0, s64, 32768\n"
"v_exp_f32 v251, v251\n"
"v_mfma_f32_16x16x32_bf16 v[56:59], a[176:179], v[184:187], v[56:59]\n"
"global_load_lds_dwordx4 v220, s[48:49] offset:3072\n"
"v_cvt_pk_bf16_f32 v147, v250, v251\n"
"v_mfma_f32_16x16x32_bf16 v[192:195], a[128:131], a[64:67], -4.0\n"
"s_waitcnt lgkmcnt(6)\n"
"v_exp_f32 v252, v252\n"
"v_mfma_f32_16x16x32_bf16 v[192:195], a[132:135], a[68:71], v[192:195]\n"
"ds_read_b128 a[144:147], v216 offset:12288\n"
"v_accvgpr_write_b32 a25, v230\n"
"v_mfma_f32_16x16x32_bf16 v[192:195], a[136:139], a[72:75], v[192:195]\n"
"v_exp_f32 v253, v253\n"
"v_mfma_f32_16x16x32_bf16 v[192:195], a[140:143], a[76:79], v[192:195]\n"
"ds_read_b128 a[148:151], v216 offset:12544\n"
"v_cvt_pk_bf16_f32 v154, v252, v253\n"
"v_mfma_f32_16x16x32_bf16 v[196:199], a[140:143], a[92:95], -4.0\n"
"v_exp_f32 v254, v254\n"
"v_mfma_f32_16x16x32_bf16 v[196:199], a[136:139], a[88:91], v[196:199]\n"
"ds_read_b128 a[152:155], v216 offset:12800\n"
"v_accvgpr_write_b32 a26, v232\n"
"v_mfma_f32_16x16x32_bf16 v[196:199], a[132:135], a[84:87], v[196:199]\n"
"v_exp_f32 v255, v255\n"
"v_mfma_f32_16x16x32_bf16 v[196:199], a[128:131], a[80:83], v[196:199]\n"
"v_cvt_pk_bf16_f32 v155, v254, v255\n"
"v_accvgpr_write_b32 a27, v234\n"
"v_mfma_f32_16x16x32_bf16 v[248:251], a[128:131], a[96:99], -4.0\n"
"v_exp_f32 v192, v192\n"
"v_mfma_f32_16x16x32_bf16 v[248:251], a[132:135], a[100:103], v[248:251]\n"
"ds_read_b128 a[156:159], v216 offset:13056\n"
"v_lshlrev_b32 v228, 16, v204\n"
"v_mfma_f32_16x16x32_bf16 v[248:251], a[136:139], a[104:107], v[248:251]\n"
"v_exp_f32 v193, v193\n"
"v_mfma_f32_16x16x32_bf16 v[248:251], a[140:143], a[108:111], v[248:251]\n"
"ds_read_b64_tr_b16 a[176:177], v217 offset:49920\n"
"v_cvt_pk_bf16_f32 v132, v192, v193\n"
"v_mfma_f32_16x16x32_bf16 v[252:255], a[140:143], a[124:127], -4.0\n"
"v_exp_f32 v194, v194\n"
"v_mfma_f32_16x16x32_bf16 v[252:255], a[136:139], a[120:123], v[252:255]\n"
"ds_read_b64_tr_b16 a[178:179], v217 offset:54016\n"
"v_and_b32 v229, s67, v204\n"
"v_mfma_f32_16x16x32_bf16 v[252:255], a[132:135], a[116:119], v[252:255]\n"
"v_exp_f32 v195, v195\n"
"v_mfma_f32_16x16x32_bf16 v[252:255], a[128:131], a[112:115], v[252:255]\n"
"v_cvt_pk_bf16_f32 v133, v194, v195\n"
"v_lshlrev_b32 v230, 16, v205\n"
"v_mfma_f32_16x16x32_bf16 v[64:67], a[160:163], v[160:163], v[64:67]\n"
"s_waitcnt lgkmcnt(6)\n"
"v_exp_f32 v196, v196\n"
"v_mfma_f32_16x16x32_bf16 v[64:67], a[164:167], v[164:167], v[64:67]\n"
"ds_read_b64_tr_b16 a[180:181], v217 offset:58112\n"
"v_and_b32 v231, s67, v205\n"
"v_mfma_f32_16x16x32_bf16 v[68:71], a[172:175], v[164:167], v[68:71]\n"
"v_exp_f32 v197, v197\n"
"v_mfma_f32_16x16x32_bf16 v[68:71], a[168:171], v[160:163], v[68:71]\n"
"ds_read_b64_tr_b16 a[182:183], v217 offset:62208\n"
"v_cvt_pk_bf16_f32 v140, v196, v197\n"
"v_mfma_f32_16x16x32_bf16 v[76:79], a[168:171], v[168:171], v[76:79]\n"
"v_exp_f32 v198, v198\n"
"v_mfma_f32_16x16x32_bf16 v[76:79], a[172:175], v[172:175], v[76:79]\n"
"ds_read_b64_tr_b16 a[184:185], v217 offset:49928\n"
"v_lshlrev_b32 v232, 16, v206\n"
"v_mfma_f32_16x16x32_bf16 v[72:75], a[164:167], v[172:175], v[72:75]\n"
"s_add_u32 m0, s66, 4096\n"
"v_exp_f32 v199, v199\n"
"v_mfma_f32_16x16x32_bf16 v[72:75], a[160:163], v[168:171], v[72:75]\n"
"global_load_lds_dwordx4 v222, s[52:53] offset:2048\n"
"v_cvt_pk_bf16_f32 v141, v198, v199\n"
"v_mfma_f32_16x16x32_bf16 v[80:83], a[160:163], v[176:179], v[80:83]\n"
"v_exp_f32 v248, v248\n"
"v_mfma_f32_16x16x32_bf16 v[80:83], a[164:167], v[180:183], v[80:83]\n"
"ds_read_b64_tr_b16 a[186:187], v217 offset:54024\n"
"v_and_b32 v233, s67, v206\n"
"v_mfma_f32_16x16x32_bf16 v[84:87], a[172:175], v[180:183], v[84:87]\n"
"v_exp_f32 v249, v249\n"
"v_mfma_f32_16x16x32_bf16 v[84:87], a[168:171], v[176:179], v[84:87]\n"
"ds_read_b64_tr_b16 a[188:189], v217 offset:58120\n"
"v_cvt_pk_bf16_f32 v148, v248, v249\n"
"v_mfma_f32_16x16x32_bf16 v[92:95], a[168:171], v[184:187], v[92:95]\n"
"v_exp_f32 v250, v250\n"
"v_mfma_f32_16x16x32_bf16 v[92:95], a[172:175], v[188:191], v[92:95]\n"
"ds_read_b64_tr_b16 a[190:191], v217 offset:62216\n"
"v_lshlrev_b32 v234, 16, v207\n"
"v_mfma_f32_16x16x32_bf16 v[88:91], a[164:167], v[188:191], v[88:91]\n"
"v_exp_f32 v251, v251\n"
"v_mfma_f32_16x16x32_bf16 v[88:91], a[160:163], v[184:187], v[88:91]\n"
"s_waitcnt vmcnt(14) lgkmcnt(6)\n"
"buffer_store_dwordx4 a[252:255], v223, s[56:59], s99 offen offset:2176 sc1 nt\n"
"v_cvt_pk_bf16_f32 v149, v250, v251\n"
"v_and_b32 v235, s67, v207\n"
"v_pk_mul_f32 v[228:229], v[228:229], s[68:69]\n"
"v_pk_mul_f32 v[230:231], v[230:231], s[68:69]\n"
"v_pk_mul_f32 v[232:233], v[232:233], s[68:69]\n"
"v_pk_mul_f32 v[234:235], v[234:235], s[68:69]\n"
"v_cvt_pk_bf16_f32 v228, v228, v229\n"
"v_cvt_pk_bf16_f32 v230, v230, v231\n"
"v_mfma_f32_16x16x32_bf16 v[192:195], a[144:147], a[64:67], -4.0\n"
"s_barrier\n"
"v_exp_f32 v252, v252\n"
"v_mfma_f32_16x16x32_bf16 v[192:195], a[148:151], a[68:71], v[192:195]\n"
"s_bfe_u32 s50, s38, 0x30018\n"
"ds_read_b128 a[128:131], v216 offset:16384\n"
"v_cvt_pk_bf16_f32 v232, v232, v233\n"
"v_mfma_f32_16x16x32_bf16 v[192:195], a[152:155], a[72:75], v[192:195]\n"
"s_lshl_b32 s50, s50, 14\n"
"v_exp_f32 v253, v253\n"
"v_mfma_f32_16x16x32_bf16 v[192:195], a[156:159], a[76:79], v[192:195]\n"
"s_add_u32 s39, s54, s50\n"
"ds_read_b128 a[132:135], v216 offset:16640\n"
"v_cvt_pk_bf16_f32 v156, v252, v253\n"
"v_mfma_f32_16x16x32_bf16 v[196:199], a[156:159], a[92:95], -4.0\n"
"s_add_u32 s50, s44, s39\n"
"v_exp_f32 v254, v254\n"
"v_mfma_f32_16x16x32_bf16 v[196:199], a[152:155], a[88:91], v[196:199]\n"
"s_addc_u32 s51, s45, 0\n"
"ds_read_b128 a[136:139], v216 offset:16896\n"
"v_cvt_pk_bf16_f32 v234, v234, v235\n"
"v_mfma_f32_16x16x32_bf16 v[196:199], a[148:151], a[84:87], v[196:199]\n"
"s_add_u32 m0, s65, 32768\n"
"v_exp_f32 v255, v255\n"
"v_mfma_f32_16x16x32_bf16 v[196:199], a[144:147], a[80:83], v[196:199]\n"
"global_load_lds_dwordx4 v219, s[50:51] offset:0\n"
"v_cvt_pk_bf16_f32 v157, v254, v255\n"
"v_mfma_f32_16x16x32_bf16 v[248:251], a[144:147], a[96:99], -4.0\n"
"v_exp_f32 v192, v192\n"
"v_mfma_f32_16x16x32_bf16 v[248:251], a[148:151], a[100:103], v[248:251]\n"
"ds_read_b128 a[140:143], v216 offset:17152\n"
"v_accvgpr_write_b32 a28, v228\n"
"v_mfma_f32_16x16x32_bf16 v[248:251], a[152:155], a[104:107], v[248:251]\n"
"v_exp_f32 v193, v193\n"
"v_mfma_f32_16x16x32_bf16 v[248:251], a[156:159], a[108:111], v[248:251]\n"
"ds_read_b64_tr_b16 a[160:161], v217 offset:0\n"
"v_cvt_pk_bf16_f32 v134, v192, v193\n"
"v_mfma_f32_16x16x32_bf16 v[252:255], a[156:159], a[124:127], -4.0\n"
"v_exp_f32 v194, v194\n"
"v_mfma_f32_16x16x32_bf16 v[252:255], a[152:155], a[120:123], v[252:255]\n"
"ds_read_b64_tr_b16 a[162:163], v217 offset:4096\n"
"v_accvgpr_write_b32 a29, v230\n"
"v_mfma_f32_16x16x32_bf16 v[252:255], a[148:151], a[116:119], v[252:255]\n"
"v_exp_f32 v195, v195\n"
"v_mfma_f32_16x16x32_bf16 v[252:255], a[144:147], a[112:115], v[252:255]\n"
"s_waitcnt vmcnt(15)\n"
"ds_read_b128 v[244:247], v218 offset:0\n"
"v_cvt_pk_bf16_f32 v135, v194, v195\n"
"v_mfma_f32_16x16x32_bf16 v[96:99], a[176:179], v[160:163], v[96:99]\n"
"s_waitcnt lgkmcnt(7)\n"
"v_exp_f32 v196, v196\n"
"v_mfma_f32_16x16x32_bf16 v[96:99], a[180:183], v[164:167], v[96:99]\n"
"ds_read_b64_tr_b16 a[164:165], v217 offset:8192\n"
"ds_read_b128 v[200:203], v218 offset:256\n"
"v_mfma_f32_16x16x32_bf16 v[100:103], a[188:191], v[164:167], v[100:103]\n"
"v_exp_f32 v197, v197\n"
"v_mfma_f32_16x16x32_bf16 v[100:103], a[184:187], v[160:163], v[100:103]\n"
"ds_read_b64_tr_b16 a[166:167], v217 offset:12288\n"
"v_cvt_pk_bf16_f32 v142, v196, v197\n"
"v_mfma_f32_16x16x32_bf16 v[108:111], a[184:187], v[168:171], v[108:111]\n"
"v_exp_f32 v198, v198\n"
"v_mfma_f32_16x16x32_bf16 v[108:111], a[188:191], v[172:175], v[108:111]\n"
"ds_read_b64_tr_b16 a[168:169], v217 offset:8\n"
"v_accvgpr_write_b32 a30, v232\n"
"v_mfma_f32_16x16x32_bf16 v[104:107], a[180:183], v[172:175], v[104:107]\n"
"s_add_u32 m0, s65, 32768\n"
"v_exp_f32 v199, v199\n"
"v_mfma_f32_16x16x32_bf16 v[104:107], a[176:179], v[168:171], v[104:107]\n"
"global_load_lds_dwordx4 v219, s[50:51] offset:1024\n"
"v_cvt_pk_bf16_f32 v143, v198, v199\n"
"v_mfma_f32_16x16x32_bf16 v[112:115], a[176:179], v[176:179], v[112:115]\n"
"v_exp_f32 v248, v248\n"
"v_mfma_f32_16x16x32_bf16 v[112:115], a[180:183], v[180:183], v[112:115]\n"
"ds_read_b64_tr_b16 a[170:171], v217 offset:4104\n"
"v_accvgpr_write_b32 a31, v234\n"
"v_mfma_f32_16x16x32_bf16 v[116:119], a[188:191], v[180:183], v[116:119]\n"
"v_exp_f32 v249, v249\n"
"v_mfma_f32_16x16x32_bf16 v[116:119], a[184:187], v[176:179], v[116:119]\n"
"ds_read_b64_tr_b16 a[172:173], v217 offset:8200\n"
"v_cvt_pk_bf16_f32 v150, v248, v249\n"
"v_mfma_f32_16x16x32_bf16 v[124:127], a[184:187], v[184:187], v[124:127]\n"
"v_exp_f32 v250, v250\n"
"v_mfma_f32_16x16x32_bf16 v[124:127], a[188:191], v[188:191], v[124:127]\n"
"s_waitcnt lgkmcnt(6)\n"
"ds_read_b64_tr_b16 a[174:175], v217 offset:12296\n"
"v_lshlrev_b32 v228, 16, v244\n"
"v_mfma_f32_16x16x32_bf16 v[120:123], a[180:183], v[188:191], v[120:123]\n"
"s_add_u32 m0, s65, 32768\n"
"v_exp_f32 v251, v251\n"
"v_mfma_f32_16x16x32_bf16 v[120:123], a[176:179], v[184:187], v[120:123]\n"
"global_load_lds_dwordx4 v219, s[50:51] offset:2048\n"
"v_cvt_pk_bf16_f32 v151, v250, v251\n"
"v_mfma_f32_16x16x32_bf16 v[192:195], a[128:131], a[64:67], -4.0\n"
"v_exp_f32 v252, v252\n"
"v_mfma_f32_16x16x32_bf16 v[192:195], a[132:135], a[68:71], v[192:195]\n"
"ds_read_b128 a[144:147], v216 offset:20480\n"
"v_and_b32 v229, s67, v244\n"
"v_mfma_f32_16x16x32_bf16 v[192:195], a[136:139], a[72:75], v[192:195]\n"
"v_exp_f32 v253, v253\n"
"v_mfma_f32_16x16x32_bf16 v[192:195], a[140:143], a[76:79], v[192:195]\n"
"ds_read_b128 a[148:151], v216 offset:20736\n"
"ds_read_b128 v[204:207], v218 offset:512\n"
"v_cvt_pk_bf16_f32 v158, v252, v253\n"
"v_mfma_f32_16x16x32_bf16 v[196:199], a[140:143], a[92:95], -4.0\n"
"v_exp_f32 v254, v254\n"
"v_mfma_f32_16x16x32_bf16 v[196:199], a[136:139], a[88:91], v[196:199]\n"
"ds_read_b128 a[152:155], v216 offset:20992\n"
"v_lshlrev_b32 v230, 16, v245\n"
"v_mfma_f32_16x16x32_bf16 v[196:199], a[132:135], a[84:87], v[196:199]\n"
"s_add_u32 m0, s66, 4096\n"
"v_exp_f32 v255, v255\n"
"v_mfma_f32_16x16x32_bf16 v[196:199], a[128:131], a[80:83], v[196:199]\n"
"global_load_lds_dwordx4 v222, s[52:53] offset:3072\n"
"v_cvt_pk_bf16_f32 v159, v254, v255\n"
"v_mfma_f32_16x16x32_bf16 v[248:251], a[128:131], a[96:99], -4.0\n"
"v_exp_f32 v192, v192\n"
"v_mfma_f32_16x16x32_bf16 v[248:251], a[132:135], a[100:103], v[248:251]\n"
"ds_read_b128 a[156:159], v216 offset:21248\n"
"v_and_b32 v231, s67, v245\n"
"v_mfma_f32_16x16x32_bf16 v[248:251], a[136:139], a[104:107], v[248:251]\n"
"v_exp_f32 v193, v193\n"
"v_mfma_f32_16x16x32_bf16 v[248:251], a[140:143], a[108:111], v[248:251]\n"
"ds_read_b64_tr_b16 a[176:177], v217 offset:256\n"
"v_cvt_pk_bf16_f32 v160, v192, v193\n"
"v_mfma_f32_16x16x32_bf16 v[252:255], a[140:143], a[124:127], -4.0\n"
"v_exp_f32 v194, v194\n"
"v_mfma_f32_16x16x32_bf16 v[252:255], a[136:139], a[120:123], v[252:255]\n"
"ds_read_b64_tr_b16 a[178:179], v217 offset:4352\n"
"v_lshlrev_b32 v232, 16, v246\n"
"v_mfma_f32_16x16x32_bf16 v[252:255], a[132:135], a[116:119], v[252:255]\n"
"s_add_u32 m0, s65, 32768\n"
"v_exp_f32 v195, v195\n"
"v_mfma_f32_16x16x32_bf16 v[252:255], a[128:131], a[112:115], v[252:255]\n"
"global_load_lds_dwordx4 v219, s[50:51] offset:3072\n"
"v_cvt_pk_bf16_f32 v161, v194, v195\n"
"v_smfmac_f32_16x16x64_bf16 a[192:195], a[208:211], v[128:135], v215\n"
"v_and_b32 v233, s67, v246\n"
"v_smfmac_f32_16x16x64_bf16 a[196:199], a[208:211], v[136:143], v215\n"
"ds_read_b64_tr_b16 a[180:181], v217 offset:8448\n"
"v_smfmac_f32_16x16x64_bf16 a[200:203], a[208:211], v[144:151], v215\n"
"buffer_store_dwordx4 v[236:239], v223, s[56:59], s70 offen offset:128 sc1 nt\n"
"v_lshlrev_b32 v234, 16, v247\n"
"v_and_b32 v235, s67, v247\n"
"v_pk_mul_f32 v[228:229], v[228:229], s[68:69]\n"
"v_pk_mul_f32 v[230:231], v[230:231], s[68:69]\n"
"v_pk_mul_f32 v[232:233], v[232:233], s[68:69]\n"
"v_pk_mul_f32 v[234:235], v[234:235], s[68:69]\n"
"v_cvt_pk_bf16_f32 v228, v228, v229\n"
"v_cvt_pk_bf16_f32 v230, v230, v231\n"
"v_smfmac_f32_16x16x64_bf16 a[204:207], a[208:211], v[152:159], v215\n"
"ds_read_b64_tr_b16 a[182:183], v217 offset:12544\n"
"v_mfma_f32_16x16x32_bf16 v[0:3], a[160:163], v[128:131], v[0:3]\n"
"s_waitcnt lgkmcnt(5)\n"
"v_exp_f32 v196, v196\n"
"v_mfma_f32_16x16x32_bf16 v[0:3], a[164:167], v[132:135], v[0:3]\n"
"ds_read_b64_tr_b16 a[184:185], v217 offset:264\n"
"v_cvt_pk_bf16_f32 v232, v232, v233\n"
"v_mfma_f32_16x16x32_bf16 v[4:7], a[172:175], v[132:135], v[4:7]\n"
"v_exp_f32 v197, v197\n"
"v_mfma_f32_16x16x32_bf16 v[4:7], a[168:171], v[128:131], v[4:7]\n"
"v_cvt_pk_bf16_f32 v168, v196, v197\n"
"v_cvt_pk_bf16_f32 v234, v234, v235\n"
"v_mfma_f32_16x16x32_bf16 v[12:15], a[168:171], v[136:139], v[12:15]\n"
"v_exp_f32 v198, v198\n"
"v_mfma_f32_16x16x32_bf16 v[12:15], a[172:175], v[140:143], v[12:15]\n"
"s_bfe_u32 s48, s38, 0x3001c\n"
"ds_read_b64_tr_b16 a[186:187], v217 offset:4360\n"
"v_accvgpr_write_b32 a32, v228\n"
"v_mfma_f32_16x16x32_bf16 v[8:11], a[164:167], v[140:143], v[8:11]\n"
"s_lshl_b32 s48, s48, 14\n"
"v_exp_f32 v199, v199\n"
"v_mfma_f32_16x16x32_bf16 v[8:11], a[160:163], v[136:139], v[8:11]\n"
"s_add_u32 s36, s79, s48\n"
"ds_read_b64_tr_b16 a[188:189], v217 offset:8456\n"
"v_cvt_pk_bf16_f32 v169, v198, v199\n"
"v_mfma_f32_16x16x32_bf16 v[16:19], a[160:163], v[144:147], v[16:19]\n"
"s_add_u32 s48, s42, s36\n"
".Lattn_pad3:\n"
"s_cbranch_vccnz .Lattn_ret\n"
"v_exp_f32 v248, v248\n"
"v_mfma_f32_16x16x32_bf16 v[16:19], a[164:167], v[148:151], v[16:19]\n"
"s_addc_u32 s49, s43, 0\n"
"ds_read_b64_tr_b16 a[190:191], v217 offset:12552\n"
"v_accvgpr_write_b32 a33, v230\n"
"v_mfma_f32_16x16x32_bf16 v[20:23], a[172:175], v[148:151], v[20:23]\n"
"s_add_u32 m0, s64, 49152\n"
"v_exp_f32 v249, v249\n"
"v_mfma_f32_16x16x32_bf16 v[20:23], a[168:171], v[144:147], v[20:23]\n"
"global_load_lds_dwordx4 v219, s[48:49] offset:0\n"
"v_cvt_pk_bf16_f32 v176, v248, v249\n"
"v_mfma_f32_16x16x32_bf16 v[28:31], a[168:171], v[152:155], v[28:31]\n"
"v_exp_f32 v250, v250\n"
"v_mfma_f32_16x16x32_bf16 v[28:31], a[172:175], v[156:159], v[28:31]\n"
"v_accvgpr_write_b32 a34, v232\n"
"v_accvgpr_write_b32 a35, v234\n"
"v_mfma_f32_16x16x32_bf16 v[24:27], a[164:167], v[156:159], v[24:27]\n"
"v_exp_f32 v251, v251\n"
"v_mfma_f32_16x16x32_bf16 v[24:27], a[160:163], v[152:155], v[24:27]\n"
"v_cvt_pk_bf16_f32 v177, v250, v251\n"
"v_lshlrev_b32 v228, 16, v200\n"
"v_mfma_f32_16x16x32_bf16 v[192:195], a[144:147], a[64:67], -4.0\n"
"v_exp_f32 v252, v252\n"
"v_mfma_f32_16x16x32_bf16 v[192:195], a[148:151], a[68:71], v[192:195]\n"
"s_mov_b32 s98, s61\n"
"ds_read_b128 a[128:131], v216 offset:24576\n"
"v_and_b32 v229, s67, v200\n"
"v_mfma_f32_16x16x32_bf16 v[192:195], a[152:155], a[72:75], v[192:195]\n"
"s_waitcnt lgkmcnt(4)\n"
"v_exp_f32 v253, v253\n"
"v_mfma_f32_16x16x32_bf16 v[192:195], a[156:159], a[76:79], v[192:195]\n"
"s_mov_b32 s61, s62\n"
"ds_read_b128 a[132:135], v216 offset:24832\n"
"ds_read_b128 v[244:247], v218 offset:768\n"
"v_cvt_pk_bf16_f32 v184, v252, v253\n"
"v_mfma_f32_16x16x32_bf16 v[196:199], a[156:159], a[92:95], -4.0\n"
"v_exp_f32 v254, v254\n"
"v_mfma_f32_16x16x32_bf16 v[196:199], a[152:155], a[88:91], v[196:199]\n"
"s_add_u32 s74, s60, 32\n"
"ds_read_b128 a[136:139], v216 offset:25088\n"
"v_lshlrev_b32 v230, 16, v201\n"
"v_mfma_f32_16x16x32_bf16 v[196:199], a[148:151], a[84:87], v[196:199]\n"
"s_add_u32 m0, s64, 49152\n"
"v_exp_f32 v255, v255\n"
"v_mfma_f32_16x16x32_bf16 v[196:199], a[144:147], a[80:83], v[196:199]\n"
"s_sub_u32 s60, s60, 160\n"
"global_load_lds_dwordx4 v219, s[48:49] offset:1024\n"
"v_cvt_pk_bf16_f32 v185, v254, v255\n"
"v_mfma_f32_16x16x32_bf16 v[248:251], a[144:147], a[96:99], -4.0\n"
"s_cselect_b32 s60, s74, s60\n"
"v_exp_f32 v192, v192\n"
"v_mfma_f32_16x16x32_bf16 v[248:251], a[148:151], a[100:103], v[248:251]\n"
"ds_read_b128 a[140:143], v216 offset:25344\n"
"v_and_b32 v231, s67, v201\n"
"v_mfma_f32_16x16x32_bf16 v[248:251], a[152:155], a[104:107], v[248:251]\n"
"s_lshr_b32 s80, s80, 1\n"
"v_exp_f32 v193, v193\n"
"v_mfma_f32_16x16x32_bf16 v[248:251], a[156:159], a[108:111], v[248:251]\n"
"s_cselect_b32 s73, -1, 0\n"
"ds_read_b64_tr_b16 a[160:161], v217 offset:512\n"
"v_cvt_pk_bf16_f32 v162, v192, v193\n"
"v_mfma_f32_16x16x32_bf16 v[252:255], a[156:159], a[124:127], -4.0\n"
"v_exp_f32 v194, v194\n"
"v_mfma_f32_16x16x32_bf16 v[252:255], a[152:155], a[120:123], v[252:255]\n"
"s_mul_i32 s74, s76, 3\n"
"ds_read_b64_tr_b16 a[162:163], v217 offset:4608\n"
"v_lshlrev_b32 v232, 16, v202\n"
"v_mfma_f32_16x16x32_bf16 v[252:255], a[148:151], a[116:119], v[252:255]\n"
"s_add_u32 m0, s64, 49152\n"
"v_exp_f32 v195, v195\n"
"v_mfma_f32_16x16x32_bf16 v[252:255], a[144:147], a[112:115], v[252:255]\n"
"s_add_u32 s74, s74, s60\n"
"global_load_lds_dwordx4 v220, s[48:49] offset:2048\n"
"v_cvt_pk_bf16_f32 v163, v194, v195\n"
"v_and_b32 v233, s67, v202\n"
"v_lshlrev_b32 v234, 16, v203\n"
"v_and_b32 v235, s67, v203\n"
"v_pk_mul_f32 v[228:229], v[228:229], s[68:69]\n"
"v_pk_mul_f32 v[230:231], v[230:231], s[68:69]\n"
"v_pk_mul_f32 v[232:233], v[232:233], s[68:69]\n"
"v_pk_mul_f32 v[234:235], v[234:235], s[68:69]\n"
"v_cvt_pk_bf16_f32 v228, v228, v229\n"
"v_cvt_pk_bf16_f32 v230, v230, v231\n"
"v_mfma_f32_16x16x32_bf16 v[32:35], a[176:179], v[128:131], v[32:35]\n"
"s_waitcnt lgkmcnt(4)\n"
"v_exp_f32 v196, v196\n"
"v_mfma_f32_16x16x32_bf16 v[32:35], a[180:183], v[132:135], v[32:35]\n"
"s_lshl_b32 s74, s74, 16\n"
"ds_read_b64_tr_b16 a[164:165], v217 offset:8704\n"
"v_cvt_pk_bf16_f32 v232, v232, v233\n"
"v_mfma_f32_16x16x32_bf16 v[36:39], a[188:191], v[132:135], v[36:39]\n"
"v_exp_f32 v197, v197\n"
"v_mfma_f32_16x16x32_bf16 v[36:39], a[184:187], v[128:131], v[36:39]\n"
"s_add_u32 s62, s74, s78\n"
"ds_read_b64_tr_b16 a[166:167], v217 offset:12800\n"
"v_cvt_pk_bf16_f32 v170, v196, v197\n"
"v_mfma_f32_16x16x32_bf16 v[44:47], a[184:187], v[136:139], v[44:47]\n"
"v_exp_f32 v198, v198\n"
"v_mfma_f32_16x16x32_bf16 v[44:47], a[188:191], v[140:143], v[44:47]\n"
"ds_read_b64_tr_b16 a[168:169], v217 offset:520\n"
"v_cvt_pk_bf16_f32 v234, v234, v235\n"
"v_mfma_f32_16x16x32_bf16 v[40:43], a[180:183], v[140:143], v[40:43]\n"
"v_exp_f32 v199, v199\n"
"v_mfma_f32_16x16x32_bf16 v[40:43], a[176:179], v[136:139], v[40:43]\n"
"v_cvt_pk_bf16_f32 v171, v198, v199\n"
"v_accvgpr_write_b32 a36, v228\n"
"v_mfma_f32_16x16x32_bf16 v[48:51], a[176:179], v[144:147], v[48:51]\n"
"v_exp_f32 v248, v248\n"
"v_mfma_f32_16x16x32_bf16 v[48:51], a[180:183], v[148:151], v[48:51]\n"
"ds_read_b64_tr_b16 a[170:171], v217 offset:4616\n"
"v_accvgpr_write_b32 a37, v230\n"
"v_mfma_f32_16x16x32_bf16 v[52:55], a[188:191], v[148:151], v[52:55]\n"
"v_exp_f32 v249, v249\n"
"v_mfma_f32_16x16x32_bf16 v[52:55], a[184:187], v[144:147], v[52:55]\n"
"ds_read_b64_tr_b16 a[172:173], v217 offset:8712\n"
"v_cvt_pk_bf16_f32 v178, v248, v249\n"
"v_mfma_f32_16x16x32_bf16 v[60:63], a[184:187], v[152:155], v[60:63]\n"
"v_exp_f32 v250, v250\n"
"v_mfma_f32_16x16x32_bf16 v[60:63], a[188:191], v[156:159], v[60:63]\n"
"ds_read_b64_tr_b16 a[174:175], v217 offset:12808\n"
"v_accvgpr_write_b32 a38, v232\n"
"v_mfma_f32_16x16x32_bf16 v[56:59], a[180:183], v[156:159], v[56:59]\n"
"s_add_u32 m0, s64, 49152\n"
"v_exp_f32 v251, v251\n"
"v_mfma_f32_16x16x32_bf16 v[56:59], a[176:179], v[152:155], v[56:59]\n"
"global_load_lds_dwordx4 v220, s[48:49] offset:3072\n"
"v_cvt_pk_bf16_f32 v179, v250, v251\n"
"v_mfma_f32_16x16x32_bf16 v[192:195], a[128:131], a[64:67], -4.0\n"
"v_exp_f32 v252, v252\n"
"v_mfma_f32_16x16x32_bf16 v[192:195], a[132:135], a[68:71], v[192:195]\n"
"s_waitcnt lgkmcnt(5)\n"
"ds_read_b128 a[144:147], v216 offset:28672\n"
"v_accvgpr_write_b32 a39, v234\n"
"v_mfma_f32_16x16x32_bf16 v[192:195], a[136:139], a[72:75], v[192:195]\n"
"v_exp_f32 v253, v253\n"
"v_mfma_f32_16x16x32_bf16 v[192:195], a[140:143], a[76:79], v[192:195]\n"
"ds_read_b128 a[148:151], v216 offset:28928\n"
"v_cvt_pk_bf16_f32 v186, v252, v253\n"
"v_mfma_f32_16x16x32_bf16 v[196:199], a[140:143], a[92:95], -4.0\n"
"v_exp_f32 v254, v254\n"
"v_mfma_f32_16x16x32_bf16 v[196:199], a[136:139], a[88:91], v[196:199]\n"
"ds_read_b128 a[152:155], v216 offset:29184\n"
"v_lshlrev_b32 v228, 16, v204\n"
"v_mfma_f32_16x16x32_bf16 v[196:199], a[132:135], a[84:87], v[196:199]\n"
"v_exp_f32 v255, v255\n"
"v_mfma_f32_16x16x32_bf16 v[196:199], a[128:131], a[80:83], v[196:199]\n"
"v_cvt_pk_bf16_f32 v187, v254, v255\n"
"v_and_b32 v229, s67, v204\n"
"v_mfma_f32_16x16x32_bf16 v[248:251], a[128:131], a[96:99], -4.0\n"
"v_exp_f32 v192, v192\n"
"v_mfma_f32_16x16x32_bf16 v[248:251], a[132:135], a[100:103], v[248:251]\n"
"ds_read_b128 a[156:159], v216 offset:29440\n"
"v_lshlrev_b32 v230, 16, v205\n"
"v_mfma_f32_16x16x32_bf16 v[248:251], a[136:139], a[104:107], v[248:251]\n"
"v_exp_f32 v193, v193\n"
"v_mfma_f32_16x16x32_bf16 v[248:251], a[140:143], a[108:111], v[248:251]\n"
"ds_read_b64_tr_b16 a[176:177], v217 offset:768\n"
"v_cvt_pk_bf16_f32 v164, v192, v193\n"
"v_mfma_f32_16x16x32_bf16 v[252:255], a[140:143], a[124:127], -4.0\n"
"v_exp_f32 v194, v194\n"
"v_mfma_f32_16x16x32_bf16 v[252:255], a[136:139], a[120:123], v[252:255]\n"
"ds_read_b64_tr_b16 a[178:179], v217 offset:4864\n"
"v_and_b32 v231, s67, v205\n"
"v_mfma_f32_16x16x32_bf16 v[252:255], a[132:135], a[116:119], v[252:255]\n"
"v_exp_f32 v195, v195\n"
"v_mfma_f32_16x16x32_bf16 v[252:255], a[128:131], a[112:115], v[252:255]\n"
"v_cvt_pk_bf16_f32 v165, v194, v195\n"
"v_lshlrev_b32 v232, 16, v206\n"
"v_mfma_f32_16x16x32_bf16 v[64:67], a[160:163], v[128:131], v[64:67]\n"
"s_waitcnt lgkmcnt(6)\n"
"v_exp_f32 v196, v196\n"
"v_mfma_f32_16x16x32_bf16 v[64:67], a[164:167], v[132:135], v[64:67]\n"
"ds_read_b64_tr_b16 a[180:181], v217 offset:8960\n"
"v_and_b32 v233, s67, v206\n"
"v_mfma_f32_16x16x32_bf16 v[68:71], a[172:175], v[132:135], v[68:71]\n"
"v_exp_f32 v197, v197\n"
"v_mfma_f32_16x16x32_bf16 v[68:71], a[168:171], v[128:131], v[68:71]\n"
"ds_read_b64_tr_b16 a[182:183], v217 offset:13056\n"
"v_cvt_pk_bf16_f32 v172, v196, v197\n"
"v_mfma_f32_16x16x32_bf16 v[76:79], a[168:171], v[136:139], v[76:79]\n"
"v_exp_f32 v198, v198\n"
"v_mfma_f32_16x16x32_bf16 v[76:79], a[172:175], v[140:143], v[76:79]\n"
"ds_read_b64_tr_b16 a[184:185], v217 offset:776\n"
"v_lshlrev_b32 v234, 16, v207\n"
"v_mfma_f32_16x16x32_bf16 v[72:75], a[164:167], v[140:143], v[72:75]\n"
"v_exp_f32 v199, v199\n"
"v_mfma_f32_16x16x32_bf16 v[72:75], a[160:163], v[136:139], v[72:75]\n"
"v_cvt_pk_bf16_f32 v173, v198, v199\n"
"v_and_b32 v235, s67, v207\n"
"v_mfma_f32_16x16x32_bf16 v[80:83], a[160:163], v[144:147], v[80:83]\n"
"v_exp_f32 v248, v248\n"
"v_mfma_f32_16x16x32_bf16 v[80:83], a[164:167], v[148:151], v[80:83]\n"
"ds_read_b64_tr_b16 a[186:187], v217 offset:4872\n"
"v_mul_f32 v228, s68, v228\n"
"v_mfma_f32_16x16x32_bf16 v[84:87], a[172:175], v[148:151], v[84:87]\n"
"v_exp_f32 v249, v249\n"
"v_mfma_f32_16x16x32_bf16 v[84:87], a[168:171], v[144:147], v[84:87]\n"
"ds_read_b64_tr_b16 a[188:189], v217 offset:8968\n"
"v_cvt_pk_bf16_f32 v180, v248, v249\n"
"v_mfma_f32_16x16x32_bf16 v[92:95], a[168:171], v[152:155], v[92:95]\n"
"v_exp_f32 v250, v250\n"
"v_mfma_f32_16x16x32_bf16 v[92:95], a[172:175], v[156:159], v[92:95]\n"
"ds_read_b64_tr_b16 a[190:191], v217 offset:13064\n"
"v_mul_f32 v229, s68, v229\n"
"v_mfma_f32_16x16x32_bf16 v[88:91], a[164:167], v[156:159], v[88:91]\n"
"v_exp_f32 v251, v251\n"
"v_mfma_f32_16x16x32_bf16 v[88:91], a[160:163], v[152:155], v[88:91]\n"
"s_waitcnt vmcnt(12) lgkmcnt(6)\n"
"buffer_store_dwordx4 v[240:243], v223, s[56:59], s70 offen offset:2176 sc1 nt\n"
"v_cvt_pk_bf16_f32 v181, v250, v251\n"
"v_mul_f32 v230, s68, v230\n"
"v_mul_f32 v231, s68, v231\n"
"v_mfma_f32_16x16x32_bf16 v[192:195], a[144:147], a[64:67], -4.0\n"
"s_barrier\n"
"v_exp_f32 v252, v252\n"
"v_mfma_f32_16x16x32_bf16 v[192:195], a[148:151], a[68:71], v[192:195]\n"
"s_bfe_u32 s50, s38, 0x3001c\n"
"ds_read_b128 a[128:131], v216 offset:32768\n"
"v_mul_f32 v232, s68, v232\n"
"v_mfma_f32_16x16x32_bf16 v[192:195], a[152:155], a[72:75], v[192:195]\n"
"s_lshl_b32 s50, s50, 14\n"
"v_exp_f32 v253, v253\n"
"v_mfma_f32_16x16x32_bf16 v[192:195], a[156:159], a[76:79], v[192:195]\n"
"s_add_u32 s39, s54, s50\n"
"ds_read_b128 a[132:135], v216 offset:33024\n"
"v_cvt_pk_bf16_f32 v188, v252, v253\n"
"v_mfma_f32_16x16x32_bf16 v[196:199], a[156:159], a[92:95], -4.0\n"
"s_add_u32 s50, s44, s39\n"
"v_exp_f32 v254, v254\n"
"v_mfma_f32_16x16x32_bf16 v[196:199], a[152:155], a[88:91], v[196:199]\n"
"s_addc_u32 s51, s45, 0\n"
"ds_read_b128 a[136:139], v216 offset:33280\n"
"v_mul_f32 v233, s68, v233\n"
"v_mfma_f32_16x16x32_bf16 v[196:199], a[148:151], a[84:87], v[196:199]\n"
"s_add_u32 m0, s65, 49152\n"
"v_exp_f32 v255, v255\n"
"v_mfma_f32_16x16x32_bf16 v[196:199], a[144:147], a[80:83], v[196:199]\n"
"global_load_lds_dwordx4 v219, s[50:51] offset:0\n"
"v_cvt_pk_bf16_f32 v189, v254, v255\n"
"v_mfma_f32_16x16x32_bf16 v[248:251], a[144:147], a[96:99], -4.0\n"
"v_exp_f32 v192, v192\n"
"v_mfma_f32_16x16x32_bf16 v[248:251], a[148:151], a[100:103], v[248:251]\n"
"ds_read_b128 a[140:143], v216 offset:33536\n"
"v_mul_f32 v234, s68, v234\n"
"v_mfma_f32_16x16x32_bf16 v[248:251], a[152:155], a[104:107], v[248:251]\n"
"s_mov_b32 s55, s62\n"
"v_exp_f32 v193, v193\n"
"v_mfma_f32_16x16x32_bf16 v[248:251], a[156:159], a[108:111], v[248:251]\n"
"s_add_u32 s35, s55, s99\n"
"ds_read_b64_tr_b16 a[160:161], v217 offset:16384\n"
"v_cvt_pk_bf16_f32 v166, v192, v193\n"
"v_mfma_f32_16x16x32_bf16 v[252:255], a[156:159], a[124:127], -4.0\n"
"s_add_u32 s52, s40, s35\n"
"v_exp_f32 v194, v194\n"
"v_mfma_f32_16x16x32_bf16 v[252:255], a[152:155], a[120:123], v[252:255]\n"
"s_addc_u32 s53, s41, 0\n"
"ds_read_b64_tr_b16 a[162:163], v217 offset:20480\n"
"v_mul_f32 v235, s68, v235\n"
"v_mfma_f32_16x16x32_bf16 v[252:255], a[148:151], a[116:119], v[252:255]\n"
"s_add_u32 m0, s66, 0\n"
"v_exp_f32 v195, v195\n"
"v_mfma_f32_16x16x32_bf16 v[252:255], a[144:147], a[112:115], v[252:255]\n"
"global_load_lds_dwordx4 v221, s[52:53] offset:0\n"
"v_cvt_pk_bf16_f32 v167, v194, v195\n"
"v_mfma_f32_16x16x32_bf16 v[96:99], a[176:179], v[128:131], v[96:99]\n"
"s_waitcnt lgkmcnt(6)\n"
"v_exp_f32 v196, v196\n"
"v_mfma_f32_16x16x32_bf16 v[96:99], a[180:183], v[132:135], v[96:99]\n"
"ds_read_b64_tr_b16 a[164:165], v217 offset:24576\n"
"v_cvt_pk_bf16_f32 v228, v228, v229\n"
"v_mfma_f32_16x16x32_bf16 v[100:103], a[188:191], v[132:135], v[100:103]\n"
"v_exp_f32 v197, v197\n"
"v_mfma_f32_16x16x32_bf16 v[100:103], a[184:187], v[128:131], v[100:103]\n"
"ds_read_b64_tr_b16 a[166:167], v217 offset:28672\n"
"v_cvt_pk_bf16_f32 v174, v196, v197\n"
"v_mfma_f32_16x16x32_bf16 v[108:111], a[184:187], v[136:139], v[108:111]\n"
"v_exp_f32 v198, v198\n"
"v_mfma_f32_16x16x32_bf16 v[108:111], a[188:191], v[140:143], v[108:111]\n"
"ds_read_b64_tr_b16 a[168:169], v217 offset:16392\n"
"v_cvt_pk_bf16_f32 v230, v230, v231\n"
"v_mfma_f32_16x16x32_bf16 v[104:107], a[180:183], v[140:143], v[104:107]\n"
"s_add_u32 m0, s65, 49152\n"
"v_exp_f32 v199, v199\n"
"v_mfma_f32_16x16x32_bf16 v[104:107], a[176:179], v[136:139], v[104:107]\n"
"global_load_lds_dwordx4 v219, s[50:51] offset:1024\n"
"v_cvt_pk_bf16_f32 v175, v198, v199\n"
"v_mfma_f32_16x16x32_bf16 v[112:115], a[176:179], v[144:147], v[112:115]\n"
"v_exp_f32 v248, v248\n"
"v_mfma_f32_16x16x32_bf16 v[112:115], a[180:183], v[148:151], v[112:115]\n"
"ds_read_b64_tr_b16 a[170:171], v217 offset:20488\n"
"v_cvt_pk_bf16_f32 v232, v232, v233\n"
"v_mfma_f32_16x16x32_bf16 v[116:119], a[188:191], v[148:151], v[116:119]\n"
"v_exp_f32 v249, v249\n"
"v_mfma_f32_16x16x32_bf16 v[116:119], a[184:187], v[144:147], v[116:119]\n"
"ds_read_b64_tr_b16 a[172:173], v217 offset:24584\n"
"v_cvt_pk_bf16_f32 v182, v248, v249\n"
"v_mfma_f32_16x16x32_bf16 v[124:127], a[184:187], v[152:155], v[124:127]\n"
"v_exp_f32 v250, v250\n"
"v_mfma_f32_16x16x32_bf16 v[124:127], a[188:191], v[156:159], v[124:127]\n"
"ds_read_b64_tr_b16 a[174:175], v217 offset:28680\n"
"v_cvt_pk_bf16_f32 v234, v234, v235\n"
"v_mfma_f32_16x16x32_bf16 v[120:123], a[180:183], v[156:159], v[120:123]\n"
"s_add_u32 m0, s65, 49152\n"
"v_exp_f32 v251, v251\n"
"v_mfma_f32_16x16x32_bf16 v[120:123], a[176:179], v[152:155], v[120:123]\n"
"s_waitcnt lgkmcnt(6)\n"
"global_load_lds_dwordx4 v219, s[50:51] offset:2048\n"
"v_cvt_pk_bf16_f32 v183, v250, v251\n"
"v_mfma_f32_16x16x32_bf16 v[192:195], a[128:131], a[64:67], -4.0\n"
"v_exp_f32 v252, v252\n"
"v_mfma_f32_16x16x32_bf16 v[192:195], a[132:135], a[68:71], v[192:195]\n"
"ds_read_b128 a[144:147], v216 offset:36864\n"
"v_accvgpr_write_b32 a40, v228\n"
"v_mfma_f32_16x16x32_bf16 v[192:195], a[136:139], a[72:75], v[192:195]\n"
"v_exp_f32 v253, v253\n"
"v_mfma_f32_16x16x32_bf16 v[192:195], a[140:143], a[76:79], v[192:195]\n"
"ds_read_b128 a[148:151], v216 offset:37120\n"
"v_cvt_pk_bf16_f32 v190, v252, v253\n"
"v_mfma_f32_16x16x32_bf16 v[196:199], a[140:143], a[92:95], -4.0\n"
"v_exp_f32 v254, v254\n"
"v_mfma_f32_16x16x32_bf16 v[196:199], a[136:139], a[88:91], v[196:199]\n"
"ds_read_b128 a[152:155], v216 offset:37376\n"
"v_accvgpr_write_b32 a41, v230\n"
"v_mfma_f32_16x16x32_bf16 v[196:199], a[132:135], a[84:87], v[196:199]\n"
"v_exp_f32 v255, v255\n"
"v_mfma_f32_16x16x32_bf16 v[196:199], a[128:131], a[80:83], v[196:199]\n"
"v_cvt_pk_bf16_f32 v191, v254, v255\n"
"v_accvgpr_write_b32 a42, v232\n"
"v_mfma_f32_16x16x32_bf16 v[248:251], a[128:131], a[96:99], -4.0\n"
"v_exp_f32 v192, v192\n"
"v_mfma_f32_16x16x32_bf16 v[248:251], a[132:135], a[100:103], v[248:251]\n"
"ds_read_b128 a[156:159], v216 offset:37632\n"
"v_accvgpr_write_b32 a43, v234\n"
"v_mfma_f32_16x16x32_bf16 v[248:251], a[136:139], a[104:107], v[248:251]\n"
"v_exp_f32 v193, v193\n"
"v_mfma_f32_16x16x32_bf16 v[248:251], a[140:143], a[108:111], v[248:251]\n"
"ds_read_b64_tr_b16 a[176:177], v217 offset:16640\n"
"v_cvt_pk_bf16_f32 v128, v192, v193\n"
"v_mfma_f32_16x16x32_bf16 v[252:255], a[140:143], a[124:127], -4.0\n"
"v_exp_f32 v194, v194\n"
"v_mfma_f32_16x16x32_bf16 v[252:255], a[136:139], a[120:123], v[252:255]\n"
"ds_read_b64_tr_b16 a[178:179], v217 offset:20736\n"
"v_lshlrev_b32 v228, 16, v244\n"
"v_mfma_f32_16x16x32_bf16 v[252:255], a[132:135], a[116:119], v[252:255]\n"
"s_add_u32 m0, s65, 49152\n"
"v_exp_f32 v195, v195\n"
"v_mfma_f32_16x16x32_bf16 v[252:255], a[128:131], a[112:115], v[252:255]\n"
"global_load_lds_dwordx4 v219, s[50:51] offset:3072\n"
"v_cvt_pk_bf16_f32 v129, v194, v195\n"
"v_smfmac_f32_16x16x64_bf16 a[192:195], a[208:211], v[160:167], v215\n"
"v_and_b32 v229, s67, v244\n"
"v_smfmac_f32_16x16x64_bf16 a[196:199], a[208:211], v[168:175], v215\n"
"ds_read_b64_tr_b16 a[180:181], v217 offset:24832\n"
"v_smfmac_f32_16x16x64_bf16 a[200:203], a[208:211], v[176:183], v215\n"
"v_lshlrev_b32 v230, 16, v245\n"
"v_smfmac_f32_16x16x64_bf16 a[204:207], a[208:211], v[184:191], v215\n"
"ds_read_b64_tr_b16 a[182:183], v217 offset:28928\n"
"v_mfma_f32_16x16x32_bf16 v[0:3], a[160:163], v[160:163], v[0:3]\n"
"s_waitcnt lgkmcnt(6)\n"
"v_exp_f32 v196, v196\n"
"v_mfma_f32_16x16x32_bf16 v[0:3], a[164:167], v[164:167], v[0:3]\n"
"ds_read_b64_tr_b16 a[184:185], v217 offset:16648\n"
"v_and_b32 v231, s67, v245\n"
"v_mfma_f32_16x16x32_bf16 v[4:7], a[172:175], v[164:167], v[4:7]\n"
"v_exp_f32 v197, v197\n"
"v_mfma_f32_16x16x32_bf16 v[4:7], a[168:171], v[160:163], v[4:7]\n"
"v_cvt_pk_bf16_f32 v136, v196, v197\n"
"v_lshlrev_b32 v232, 16, v246\n"
"v_mfma_f32_16x16x32_bf16 v[12:15], a[168:171], v[168:171], v[12:15]\n"
"s_mov_b32 s79, s63\n"
"v_exp_f32 v198, v198\n"
"v_mfma_f32_16x16x32_bf16 v[12:15], a[172:175], v[172:175], v[12:15]\n"
"s_bfe_u32 s48, s38, 0x30000\n"
"ds_read_b64_tr_b16 a[186:187], v217 offset:20744\n"
"v_and_b32 v233, s67, v246\n"
"v_mfma_f32_16x16x32_bf16 v[8:11], a[164:167], v[172:175], v[8:11]\n"
"s_lshl_b32 s48, s48, 14\n"
"v_exp_f32 v199, v199\n"
"v_mfma_f32_16x16x32_bf16 v[8:11], a[160:163], v[168:171], v[8:11]\n"
"s_add_u32 s36, s79, s48\n"
"ds_read_b64_tr_b16 a[188:189], v217 offset:24840\n"
"v_cvt_pk_bf16_f32 v137, v198, v199\n"
"v_mfma_f32_16x16x32_bf16 v[16:19], a[160:163], v[176:179], v[16:19]\n"
"s_add_u32 s48, s42, s36\n"
"v_exp_f32 v248, v248\n"
"v_mfma_f32_16x16x32_bf16 v[16:19], a[164:167], v[180:183], v[16:19]\n"
"s_addc_u32 s49, s43, 0\n"
"ds_read_b64_tr_b16 a[190:191], v217 offset:28936\n"
"buffer_store_dwordx4 a[216:219], v223, s[56:59], s71 offen offset:128 sc1 nt\n"
"v_lshlrev_b32 v234, 16, v247\n"
"v_and_b32 v235, s67, v247\n"
"v_pk_mul_f32 v[228:229], v[228:229], s[68:69]\n"
"v_pk_mul_f32 v[230:231], v[230:231], s[68:69]\n"
"v_pk_mul_f32 v[232:233], v[232:233], s[68:69]\n"
"v_pk_mul_f32 v[234:235], v[234:235], s[68:69]\n"
"v_cvt_pk_bf16_f32 v228, v228, v229\n"
"v_cvt_pk_bf16_f32 v230, v230, v231\n"
"v_mfma_f32_16x16x32_bf16 v[20:23], a[172:175], v[180:183], v[20:23]\n"
"s_add_u32 m0, s64, 0\n"
"v_exp_f32 v249, v249\n"
"v_mfma_f32_16x16x32_bf16 v[20:23], a[168:171], v[176:179], v[20:23]\n"
"global_load_lds_dwordx4 v221, s[48:49] offset:0\n"
"v_cvt_pk_bf16_f32 v144, v248, v249\n"
"v_mfma_f32_16x16x32_bf16 v[28:31], a[168:171], v[184:187], v[28:31]\n"
"v_exp_f32 v250, v250\n"
"v_mfma_f32_16x16x32_bf16 v[28:31], a[172:175], v[188:191], v[28:31]\n"
"v_cvt_pk_bf16_f32 v232, v232, v233\n"
"v_cvt_pk_bf16_f32 v234, v234, v235\n"
"v_mfma_f32_16x16x32_bf16 v[24:27], a[164:167], v[188:191], v[24:27]\n"
"v_exp_f32 v251, v251\n"
"v_mfma_f32_16x16x32_bf16 v[24:27], a[160:163], v[184:187], v[24:27]\n"
"v_cvt_pk_bf16_f32 v145, v250, v251\n"
"v_accvgpr_write_b32 a44, v228\n"
"v_mfma_f32_16x16x32_bf16 v[192:195], a[144:147], a[64:67], -4.0\n"
"v_exp_f32 v252, v252\n"
"v_mfma_f32_16x16x32_bf16 v[192:195], a[148:151], a[68:71], v[192:195]\n"
"s_waitcnt lgkmcnt(3)\n"
"ds_read_b128 a[128:131], v216 offset:40960\n"
"v_accvgpr_write_b32 a45, v230\n"
"v_mfma_f32_16x16x32_bf16 v[192:195], a[152:155], a[72:75], v[192:195]\n"
"s_mov_b32 s93, s63\n"
"v_exp_f32 v253, v253\n"
"v_mfma_f32_16x16x32_bf16 v[192:195], a[156:159], a[76:79], v[192:195]\n"
"ds_read_b128 a[132:135], v216 offset:41216\n"
"v_cvt_pk_bf16_f32 v152, v252, v253\n"
"v_mfma_f32_16x16x32_bf16 v[196:199], a[156:159], a[92:95], -4.0\n"
"s_mul_i32 s74, s60, 43691\n"
"v_exp_f32 v254, v254\n"
"v_mfma_f32_16x16x32_bf16 v[196:199], a[152:155], a[88:91], v[196:199]\n"
"ds_read_b128 a[136:139], v216 offset:41472\n"
"v_accvgpr_write_b32 a46, v232\n"
"v_mfma_f32_16x16x32_bf16 v[196:199], a[148:151], a[84:87], v[196:199]\n"
"s_add_u32 m0, s64, 0\n"
"v_exp_f32 v255, v255\n"
"v_mfma_f32_16x16x32_bf16 v[196:199], a[144:147], a[80:83], v[196:199]\n"
"s_lshr_b32 s74, s74, 17\n"
"global_load_lds_dwordx4 v221, s[48:49] offset:1024\n"
"v_cvt_pk_bf16_f32 v153, v254, v255\n"
"v_mfma_f32_16x16x32_bf16 v[248:251], a[144:147], a[96:99], -4.0\n"
"v_exp_f32 v192, v192\n"
"v_mfma_f32_16x16x32_bf16 v[248:251], a[148:151], a[100:103], v[248:251]\n"
"s_add_u32 s74, s74, s76\n"
"ds_read_b128 a[140:143], v216 offset:41728\n"
"v_accvgpr_write_b32 a47, v234\n"
"v_mfma_f32_16x16x32_bf16 v[248:251], a[152:155], a[104:107], v[248:251]\n"
"s_waitcnt vmcnt(15)\n"
"v_exp_f32 v193, v193\n"
"v_mfma_f32_16x16x32_bf16 v[248:251], a[156:159], a[108:111], v[248:251]\n"
"s_lshl_b32 s74, s74, 17\n"
"ds_read_b64_tr_b16 a[160:161], v217 offset:16896\n"
"ds_read_b128 v[200:203], v218 offset:4096\n"
"v_cvt_pk_bf16_f32 v130, v192, v193\n"
"v_mfma_f32_16x16x32_bf16 v[252:255], a[156:159], a[124:127], -4.0\n"
"v_exp_f32 v194, v194\n"
"v_mfma_f32_16x16x32_bf16 v[252:255], a[152:155], a[120:123], v[252:255]\n"
"s_add_u32 s63, s74, s77\n"
"ds_read_b64_tr_b16 a[162:163], v217 offset:20992\n"
"v_mfma_f32_16x16x32_bf16 v[252:255], a[148:151], a[116:119], v[252:255]\n"
"s_add_u32 m0, s64, 0\n"
"v_exp_f32 v195, v195\n"
"v_mfma_f32_16x16x32_bf16 v[252:255], a[144:147], a[112:115], v[252:255]\n"
"global_load_lds_dwordx4 v222, s[48:49] offset:2048\n"
"v_cvt_pk_bf16_f32 v131, v194, v195\n"
"v_mfma_f32_16x16x32_bf16 v[32:35], a[176:179], v[160:163], v[32:35]\n"
"v_exp_f32 v196, v196\n"
"v_mfma_f32_16x16x32_bf16 v[32:35], a[180:183], v[164:167], v[32:35]\n"
"s_waitcnt lgkmcnt(6)\n"
"ds_read_b64_tr_b16 a[164:165], v217 offset:25088\n"
"ds_read_b128 v[204:207], v218 offset:4352\n"
"v_mfma_f32_16x16x32_bf16 v[36:39], a[188:191], v[164:167], v[36:39]\n"
"v_exp_f32 v197, v197\n"
"v_mfma_f32_16x16x32_bf16 v[36:39], a[184:187], v[160:163], v[36:39]\n"
"ds_read_b64_tr_b16 a[166:167], v217 offset:29184\n"
"v_cvt_pk_bf16_f32 v138, v196, v197\n"
"v_mfma_f32_16x16x32_bf16 v[44:47], a[184:187], v[168:171], v[44:47]\n"
"v_exp_f32 v198, v198\n"
"v_mfma_f32_16x16x32_bf16 v[44:47], a[188:191], v[172:175], v[44:47]\n"
"ds_read_b64_tr_b16 a[168:169], v217 offset:16904\n"
"v_mfma_f32_16x16x32_bf16 v[40:43], a[180:183], v[172:175], v[40:43]\n"
"v_exp_f32 v199, v199\n"
"v_mfma_f32_16x16x32_bf16 v[40:43], a[176:179], v[168:171], v[40:43]\n"
"s_waitcnt lgkmcnt(5)\n"
"v_cvt_pk_bf16_f32 v139, v198, v199\n"
"v_lshlrev_b32 v228, 16, v200\n"
"v_mfma_f32_16x16x32_bf16 v[48:51], a[176:179], v[176:179], v[48:51]\n"
"v_exp_f32 v248, v248\n"
"v_mfma_f32_16x16x32_bf16 v[48:51], a[180:183], v[180:183], v[48:51]\n"
"ds_read_b64_tr_b16 a[170:171], v217 offset:21000\n"
"ds_read_b128 v[244:247], v218 offset:4608\n"
"v_mfma_f32_16x16x32_bf16 v[52:55], a[188:191], v[180:183], v[52:55]\n"
"v_exp_f32 v249, v249\n"
"v_mfma_f32_16x16x32_bf16 v[52:55], a[184:187], v[176:179], v[52:55]\n"
"ds_read_b64_tr_b16 a[172:173], v217 offset:25096\n"
"v_cvt_pk_bf16_f32 v146, v248, v249\n"
"v_mfma_f32_16x16x32_bf16 v[60:63], a[184:187], v[184:187], v[60:63]\n"
"v_exp_f32 v250, v250\n"
"v_mfma_f32_16x16x32_bf16 v[60:63], a[188:191], v[188:191], v[60:63]\n"
"ds_read_b64_tr_b16 a[174:175], v217 offset:29192\n"
"v_and_b32 v229, s67, v200\n"
"v_mfma_f32_16x16x32_bf16 v[56:59], a[180:183], v[188:191], v[56:59]\n"
"s_add_u32 m0, s64, 0\n"
"v_exp_f32 v251, v251\n"
"v_mfma_f32_16x16x32_bf16 v[56:59], a[176:179], v[184:187], v[56:59]\n"
"global_load_lds_dwordx4 v222, s[48:49] offset:3072\n"
"v_cvt_pk_bf16_f32 v147, v250, v251\n"
"v_mfma_f32_16x16x32_bf16 v[192:195], a[128:131], a[64:67], -4.0\n"
"v_exp_f32 v252, v252\n"
"v_mfma_f32_16x16x32_bf16 v[192:195], a[132:135], a[68:71], v[192:195]\n"
"ds_read_b128 a[144:147], v216 offset:45056\n"
"v_lshlrev_b32 v230, 16, v201\n"
"v_mfma_f32_16x16x32_bf16 v[192:195], a[136:139], a[72:75], v[192:195]\n"
"v_exp_f32 v253, v253\n"
"v_mfma_f32_16x16x32_bf16 v[192:195], a[140:143], a[76:79], v[192:195]\n"
"ds_read_b128 a[148:151], v216 offset:45312\n"
"v_cvt_pk_bf16_f32 v154, v252, v253\n"
"v_mfma_f32_16x16x32_bf16 v[196:199], a[140:143], a[92:95], -4.0\n"
"v_exp_f32 v254, v254\n"
"v_mfma_f32_16x16x32_bf16 v[196:199], a[136:139], a[88:91], v[196:199]\n"
"ds_read_b128 a[152:155], v216 offset:45568\n"
"v_and_b32 v231, s67, v201\n"
"v_mfma_f32_16x16x32_bf16 v[196:199], a[132:135], a[84:87], v[196:199]\n"
"v_exp_f32 v255, v255\n"
"v_mfma_f32_16x16x32_bf16 v[196:199], a[128:131], a[80:83], v[196:199]\n"
"v_cvt_pk_bf16_f32 v155, v254, v255\n"
"v_lshlrev_b32 v232, 16, v202\n"
"v_mfma_f32_16x16x32_bf16 v[248:251], a[128:131], a[96:99], -4.0\n"
"v_exp_f32 v192, v192\n"
"v_mfma_f32_16x16x32_bf16 v[248:251], a[132:135], a[100:103], v[248:251]\n"
"ds_read_b128 a[156:159], v216 offset:45824\n"
"v_and_b32 v233, s67, v202\n"
"v_mfma_f32_16x16x32_bf16 v[248:251], a[136:139], a[104:107], v[248:251]\n"
"v_exp_f32 v193, v193\n"
"v_mfma_f32_16x16x32_bf16 v[248:251], a[140:143], a[108:111], v[248:251]\n"
"ds_read_b64_tr_b16 a[176:177], v217 offset:17152\n"
"v_cvt_pk_bf16_f32 v132, v192, v193\n"
"v_mfma_f32_16x16x32_bf16 v[252:255], a[140:143], a[124:127], -4.0\n"
"v_exp_f32 v194, v194\n"
"v_mfma_f32_16x16x32_bf16 v[252:255], a[136:139], a[120:123], v[252:255]\n"
"ds_read_b64_tr_b16 a[178:179], v217 offset:21248\n"
"v_lshlrev_b32 v234, 16, v203\n"
"v_mfma_f32_16x16x32_bf16 v[252:255], a[132:135], a[116:119], v[252:255]\n"
"v_exp_f32 v195, v195\n"
"v_mfma_f32_16x16x32_bf16 v[252:255], a[128:131], a[112:115], v[252:255]\n"
"s_waitcnt lgkmcnt(6)\n"
"buffer_store_dwordx4 a[220:223], v223, s[56:59], s71 offen offset:2176 sc1 nt\n"
"v_cvt_pk_bf16_f32 v133, v194, v195\n"
"v_and_b32 v235, s67, v203\n"
"v_pk_mul_f32 v[228:229], v[228:229], s[68:69]\n"
"v_pk_mul_f32 v[230:231], v[230:231], s[68:69]\n"
"v_pk_mul_f32 v[232:233], v[232:233], s[68:69]\n"
"v_pk_mul_f32 v[234:235], v[234:235], s[68:69]\n"
"v_cvt_pk_bf16_f32 v228, v228, v229\n"
"v_cvt_pk_bf16_f32 v230, v230, v231\n"
"v_mfma_f32_16x16x32_bf16 v[64:67], a[160:163], v[160:163], v[64:67]\n"
"v_exp_f32 v196, v196\n"
"v_mfma_f32_16x16x32_bf16 v[64:67], a[164:167], v[164:167], v[64:67]\n"
"ds_read_b64_tr_b16 a[180:181], v217 offset:25344\n"
"v_cvt_pk_bf16_f32 v232, v232, v233\n"
"v_mfma_f32_16x16x32_bf16 v[68:71], a[172:175], v[164:167], v[68:71]\n"
"v_exp_f32 v197, v197\n"
"v_mfma_f32_16x16x32_bf16 v[68:71], a[168:171], v[160:163], v[68:71]\n"
"ds_read_b64_tr_b16 a[182:183], v217 offset:29440\n"
"v_cvt_pk_bf16_f32 v140, v196, v197\n"
"v_mfma_f32_16x16x32_bf16 v[76:79], a[168:171], v[168:171], v[76:79]\n"
"v_exp_f32 v198, v198\n"
"v_mfma_f32_16x16x32_bf16 v[76:79], a[172:175], v[172:175], v[76:79]\n"
"ds_read_b64_tr_b16 a[184:185], v217 offset:17160\n"
"v_cvt_pk_bf16_f32 v234, v234, v235\n"
"v_mfma_f32_16x16x32_bf16 v[72:75], a[164:167], v[172:175], v[72:75]\n"
"s_add_u32 m0, s66, 0\n"
"v_exp_f32 v199, v199\n"
"v_mfma_f32_16x16x32_bf16 v[72:75], a[160:163], v[168:171], v[72:75]\n"
"global_load_lds_dwordx4 v221, s[52:53] offset:1024\n"
"v_cvt_pk_bf16_f32 v141, v198, v199\n"
"v_mfma_f32_16x16x32_bf16 v[80:83], a[160:163], v[176:179], v[80:83]\n"
"v_exp_f32 v248, v248\n"
"v_mfma_f32_16x16x32_bf16 v[80:83], a[164:167], v[180:183], v[80:83]\n"
"ds_read_b64_tr_b16 a[186:187], v217 offset:21256\n"
"v_accvgpr_write_b32 a48, v228\n"
"v_mfma_f32_16x16x32_bf16 v[84:87], a[172:175], v[180:183], v[84:87]\n"
"v_exp_f32 v249, v249\n"
"v_mfma_f32_16x16x32_bf16 v[84:87], a[168:171], v[176:179], v[84:87]\n"
"ds_read_b64_tr_b16 a[188:189], v217 offset:25352\n"
"v_cvt_pk_bf16_f32 v148, v248, v249\n"
"v_mfma_f32_16x16x32_bf16 v[92:95], a[168:171], v[184:187], v[92:95]\n"
"v_exp_f32 v250, v250\n"
"v_mfma_f32_16x16x32_bf16 v[92:95], a[172:175], v[188:191], v[92:95]\n"
"ds_read_b64_tr_b16 a[190:191], v217 offset:29448\n"
"v_accvgpr_write_b32 a49, v230\n"
"v_mfma_f32_16x16x32_bf16 v[88:91], a[164:167], v[188:191], v[88:91]\n"
"s_mov_b32 s54, s93\n"
"v_exp_f32 v251, v251\n"
"v_mfma_f32_16x16x32_bf16 v[88:91], a[160:163], v[184:187], v[88:91]\n"
"s_waitcnt vmcnt(13) lgkmcnt(6)\n"
"v_cvt_pk_bf16_f32 v149, v250, v251\n"
"v_accvgpr_write_b32 a50, v232\n"
"v_mfma_f32_16x16x32_bf16 v[192:195], a[144:147], a[64:67], -4.0\n"
"s_barrier\n"
"v_exp_f32 v252, v252\n"
"v_mfma_f32_16x16x32_bf16 v[192:195], a[148:151], a[68:71], v[192:195]\n"
"s_bfe_u32 s50, s38, 0x30000\n"
"ds_read_b128 a[128:131], v216 offset:49152\n"
"v_accvgpr_write_b32 a51, v234\n"
"v_mfma_f32_16x16x32_bf16 v[192:195], a[152:155], a[72:75], v[192:195]\n"
"s_lshl_b32 s50, s50, 14\n"
"v_exp_f32 v253, v253\n"
"v_mfma_f32_16x16x32_bf16 v[192:195], a[156:159], a[76:79], v[192:195]\n"
"s_add_u32 s39, s54, s50\n"
"ds_read_b128 a[132:135], v216 offset:49408\n"
"v_cvt_pk_bf16_f32 v156, v252, v253\n"
".Lattn_pad4:\n"
"s_cbranch_vccnz .Lattn_ret\n"
"v_mfma_f32_16x16x32_bf16 v[196:199], a[156:159], a[92:95], -4.0\n"
"s_add_u32 s50, s44, s39\n"
"v_exp_f32 v254, v254\n"
"v_mfma_f32_16x16x32_bf16 v[196:199], a[152:155], a[88:91], v[196:199]\n"
"s_addc_u32 s51, s45, 0\n"
"ds_read_b128 a[136:139], v216 offset:49664\n"
"v_lshlrev_b32 v228, 16, v204\n"
"v_mfma_f32_16x16x32_bf16 v[196:199], a[148:151], a[84:87], v[196:199]\n"
"s_add_u32 m0, s65, 0\n"
"v_exp_f32 v255, v255\n"
"v_mfma_f32_16x16x32_bf16 v[196:199], a[144:147], a[80:83], v[196:199]\n"
"global_load_lds_dwordx4 v221, s[50:51] offset:0\n"
"v_cvt_pk_bf16_f32 v157, v254, v255\n"
"v_mfma_f32_16x16x32_bf16 v[248:251], a[144:147], a[96:99], -4.0\n"
"v_exp_f32 v192, v192\n"
"v_mfma_f32_16x16x32_bf16 v[248:251], a[148:151], a[100:103], v[248:251]\n"
"ds_read_b128 a[140:143], v216 offset:49920\n"
"v_and_b32 v229, s67, v204\n"
"v_mfma_f32_16x16x32_bf16 v[248:251], a[152:155], a[104:107], v[248:251]\n"
"v_exp_f32 v193, v193\n"
"v_mfma_f32_16x16x32_bf16 v[248:251], a[156:159], a[108:111], v[248:251]\n"
"ds_read_b64_tr_b16 a[160:161], v217 offset:32768\n"
"v_cvt_pk_bf16_f32 v134, v192, v193\n"
"v_mfma_f32_16x16x32_bf16 v[252:255], a[156:159], a[124:127], -4.0\n"
"v_exp_f32 v194, v194\n"
"v_mfma_f32_16x16x32_bf16 v[252:255], a[152:155], a[120:123], v[252:255]\n"
"ds_read_b64_tr_b16 a[162:163], v217 offset:36864\n"
"v_lshlrev_b32 v230, 16, v205\n"
"v_mfma_f32_16x16x32_bf16 v[252:255], a[148:151], a[116:119], v[252:255]\n"
"v_exp_f32 v195, v195\n"
"v_mfma_f32_16x16x32_bf16 v[252:255], a[144:147], a[112:115], v[252:255]\n"
"ds_read_b128 v[200:203], v218 offset:4864\n"
"v_cvt_pk_bf16_f32 v135, v194, v195\n"
"v_mfma_f32_16x16x32_bf16 v[96:99], a[176:179], v[160:163], v[96:99]\n"
"s_waitcnt lgkmcnt(7)\n"
"v_exp_f32 v196, v196\n"
"v_mfma_f32_16x16x32_bf16 v[96:99], a[180:183], v[164:167], v[96:99]\n"
"ds_read_b64_tr_b16 a[164:165], v217 offset:40960\n"
"v_and_b32 v231, s67, v205\n"
"v_mfma_f32_16x16x32_bf16 v[100:103], a[188:191], v[164:167], v[100:103]\n"
"v_exp_f32 v197, v197\n"
"v_mfma_f32_16x16x32_bf16 v[100:103], a[184:187], v[160:163], v[100:103]\n"
"ds_read_b64_tr_b16 a[166:167], v217 offset:45056\n"
"v_cvt_pk_bf16_f32 v142, v196, v197\n"
"v_mfma_f32_16x16x32_bf16 v[108:111], a[184:187], v[168:171], v[108:111]\n"
"v_exp_f32 v198, v198\n"
"v_mfma_f32_16x16x32_bf16 v[108:111], a[188:191], v[172:175], v[108:111]\n"
"ds_read_b64_tr_b16 a[168:169], v217 offset:32776\n"
"v_lshlrev_b32 v232, 16, v206\n"
"v_and_b32 v233, s67, v206\n"
"v_lshlrev_b32 v234, 16, v207\n"
"v_and_b32 v235, s67, v207\n"
"v_pk_mul_f32 v[228:229], v[228:229], s[68:69]\n"
"v_pk_mul_f32 v[230:231], v[230:231], s[68:69]\n"
"v_pk_mul_f32 v[232:233], v[232:233], s[68:69]\n"
"v_pk_mul_f32 v[234:235], v[234:235], s[68:69]\n"
"v_cvt_pk_bf16_f32 v228, v228, v229\n"
"v_cvt_pk_bf16_f32 v230, v230, v231\n"
"v_mfma_f32_16x16x32_bf16 v[104:107], a[180:183], v[172:175], v[104:107]\n"
"s_add_u32 m0, s65, 0\n"
"v_exp_f32 v199, v199\n"
"v_mfma_f32_16x16x32_bf16 v[104:107], a[176:179], v[168:171], v[104:107]\n"
"global_load_lds_dwordx4 v221, s[50:51] offset:1024\n"
"v_cvt_pk_bf16_f32 v143, v198, v199\n"
"v_mfma_f32_16x16x32_bf16 v[112:115], a[176:179], v[176:179], v[112:115]\n"
"v_exp_f32 v248, v248\n"
"v_mfma_f32_16x16x32_bf16 v[112:115], a[180:183], v[180:183], v[112:115]\n"
"ds_read_b64_tr_b16 a[170:171], v217 offset:36872\n"
"v_cvt_pk_bf16_f32 v232, v232, v233\n"
"v_mfma_f32_16x16x32_bf16 v[116:119], a[188:191], v[180:183], v[116:119]\n"
"v_exp_f32 v249, v249\n"
"v_mfma_f32_16x16x32_bf16 v[116:119], a[184:187], v[176:179], v[116:119]\n"
"ds_read_b64_tr_b16 a[172:173], v217 offset:40968\n"
"v_cvt_pk_bf16_f32 v150, v248, v249\n"
"v_mfma_f32_16x16x32_bf16 v[124:127], a[184:187], v[184:187], v[124:127]\n"
"v_exp_f32 v250, v250\n"
"v_mfma_f32_16x16x32_bf16 v[124:127], a[188:191], v[188:191], v[124:127]\n"
"ds_read_b64_tr_b16 a[174:175], v217 offset:45064\n"
"v_cvt_pk_bf16_f32 v234, v234, v235\n"
"v_mfma_f32_16x16x32_bf16 v[120:123], a[180:183], v[188:191], v[120:123]\n"
"s_add_u32 m0, s65, 0\n"
"v_exp_f32 v251, v251\n"
"v_mfma_f32_16x16x32_bf16 v[120:123], a[176:179], v[184:187], v[120:123]\n"
"s_waitcnt lgkmcnt(5)\n"
"global_load_lds_dwordx4 v221, s[50:51] offset:2048\n"
"v_cvt_pk_bf16_f32 v151, v250, v251\n"
"v_mfma_f32_16x16x32_bf16 v[192:195], a[128:131], a[64:67], -4.0\n"
"v_exp_f32 v252, v252\n"
"v_mfma_f32_16x16x32_bf16 v[192:195], a[132:135], a[68:71], v[192:195]\n"
"ds_read_b128 a[144:147], v216 offset:53248\n"
"v_accvgpr_write_b32 a52, v228\n"
"v_mfma_f32_16x16x32_bf16 v[192:195], a[136:139], a[72:75], v[192:195]\n"
"v_exp_f32 v253, v253\n"
"v_mfma_f32_16x16x32_bf16 v[192:195], a[140:143], a[76:79], v[192:195]\n"
"ds_read_b128 a[148:151], v216 offset:53504\n"
"v_cvt_pk_bf16_f32 v158, v252, v253\n"
"v_mfma_f32_16x16x32_bf16 v[196:199], a[140:143], a[92:95], -4.0\n"
"v_exp_f32 v254, v254\n"
"v_mfma_f32_16x16x32_bf16 v[196:199], a[136:139], a[88:91], v[196:199]\n"
"ds_read_b128 a[152:155], v216 offset:53760\n"
"v_accvgpr_write_b32 a53, v230\n"
"v_mfma_f32_16x16x32_bf16 v[196:199], a[132:135], a[84:87], v[196:199]\n"
"v_exp_f32 v255, v255\n"
"v_mfma_f32_16x16x32_bf16 v[196:199], a[128:131], a[80:83], v[196:199]\n"
"v_cvt_pk_bf16_f32 v159, v254, v255\n"
"v_accvgpr_write_b32 a54, v232\n"
"v_mfma_f32_16x16x32_bf16 v[248:251], a[128:131], a[96:99], -4.0\n"
"v_exp_f32 v192, v192\n"
"v_mfma_f32_16x16x32_bf16 v[248:251], a[132:135], a[100:103], v[248:251]\n"
"ds_read_b128 a[156:159], v216 offset:54016\n"
"v_accvgpr_write_b32 a55, v234\n"
"v_mfma_f32_16x16x32_bf16 v[248:251], a[136:139], a[104:107], v[248:251]\n"
"v_exp_f32 v193, v193\n"
"v_mfma_f32_16x16x32_bf16 v[248:251], a[140:143], a[108:111], v[248:251]\n"
"ds_read_b64_tr_b16 a[176:177], v217 offset:33024\n"
"v_cvt_pk_bf16_f32 v160, v192, v193\n"
"v_mfma_f32_16x16x32_bf16 v[252:255], a[140:143], a[124:127], -4.0\n"
"v_exp_f32 v194, v194\n"
"v_mfma_f32_16x16x32_bf16 v[252:255], a[136:139], a[120:123], v[252:255]\n"
"ds_read_b64_tr_b16 a[178:179], v217 offset:37120\n"
"v_lshlrev_b32 v228, 16, v244\n"
"v_mfma_f32_16x16x32_bf16 v[252:255], a[132:135], a[116:119], v[252:255]\n"
"s_add_u32 m0, s65, 0\n"
"v_exp_f32 v195, v195\n"
"v_mfma_f32_16x16x32_bf16 v[252:255], a[128:131], a[112:115], v[252:255]\n"
"global_load_lds_dwordx4 v221, s[50:51] offset:3072\n"
"v_cvt_pk_bf16_f32 v161, v194, v195\n"
"v_smfmac_f32_16x16x64_bf16 a[192:195], a[208:211], v[128:135], v215\n"
"v_and_b32 v229, s67, v244\n"
"v_smfmac_f32_16x16x64_bf16 a[196:199], a[208:211], v[136:143], v215\n"
"ds_read_b64_tr_b16 a[180:181], v217 offset:41216\n"
"v_smfmac_f32_16x16x64_bf16 a[200:203], a[208:211], v[144:151], v215\n"
"v_lshlrev_b32 v230, 16, v245\n"
"v_smfmac_f32_16x16x64_bf16 a[204:207], a[208:211], v[152:159], v215\n"
"ds_read_b64_tr_b16 a[182:183], v217 offset:45312\n"
"v_mfma_f32_16x16x32_bf16 v[0:3], a[160:163], v[128:131], v[0:3]\n"
"s_waitcnt lgkmcnt(6)\n"
"v_exp_f32 v196, v196\n"
"v_mfma_f32_16x16x32_bf16 v[0:3], a[164:167], v[132:135], v[0:3]\n"
"ds_read_b64_tr_b16 a[184:185], v217 offset:33032\n"
"v_and_b32 v231, s67, v245\n"
"v_mfma_f32_16x16x32_bf16 v[4:7], a[172:175], v[132:135], v[4:7]\n"
"v_exp_f32 v197, v197\n"
"v_mfma_f32_16x16x32_bf16 v[4:7], a[168:171], v[128:131], v[4:7]\n"
"buffer_store_dwordx4 a[224:227], v223, s[56:59], s37 offen offset:128 sc1 nt\n"
"v_cvt_pk_bf16_f32 v168, v196, v197\n"
"v_lshlrev_b32 v232, 16, v246\n"
"v_and_b32 v233, s67, v246\n"
"v_mfma_f32_16x16x32_bf16 v[12:15], a[168:171], v[136:139], v[12:15]\n"
"v_exp_f32 v198, v198\n"
"v_mfma_f32_16x16x32_bf16 v[12:15], a[172:175], v[140:143], v[12:15]\n"
"s_bfe_u32 s48, s38, 0x30004\n"
"ds_read_b64_tr_b16 a[186:187], v217 offset:37128\n"
"v_lshlrev_b32 v234, 16, v247\n"
"v_mfma_f32_16x16x32_bf16 v[8:11], a[164:167], v[140:143], v[8:11]\n"
"s_lshl_b32 s48, s48, 14\n"
"v_exp_f32 v199, v199\n"
"v_mfma_f32_16x16x32_bf16 v[8:11], a[160:163], v[136:139], v[8:11]\n"
"s_add_u32 s36, s79, s48\n"
"ds_read_b64_tr_b16 a[188:189], v217 offset:41224\n"
"v_cvt_pk_bf16_f32 v169, v198, v199\n"
"v_mfma_f32_16x16x32_bf16 v[16:19], a[160:163], v[144:147], v[16:19]\n"
"s_add_u32 s48, s42, s36\n"
"v_exp_f32 v248, v248\n"
"v_mfma_f32_16x16x32_bf16 v[16:19], a[164:167], v[148:151], v[16:19]\n"
"s_addc_u32 s49, s43, 0\n"
"ds_read_b64_tr_b16 a[190:191], v217 offset:45320\n"
"v_and_b32 v235, s67, v247\n"
"v_mfma_f32_16x16x32_bf16 v[20:23], a[172:175], v[148:151], v[20:23]\n"
"s_add_u32 m0, s64, 16384\n"
"v_exp_f32 v249, v249\n"
"v_mfma_f32_16x16x32_bf16 v[20:23], a[168:171], v[144:147], v[20:23]\n"
"global_load_lds_dwordx4 v221, s[48:49] offset:0\n"
"v_cvt_pk_bf16_f32 v176, v248, v249\n"
"v_mfma_f32_16x16x32_bf16 v[28:31], a[168:171], v[152:155], v[28:31]\n"
"v_exp_f32 v250, v250\n"
"v_mfma_f32_16x16x32_bf16 v[28:31], a[172:175], v[156:159], v[28:31]\n"
"v_mul_f32 v228, s68, v228\n"
"v_mul_f32 v229, s68, v229\n"
"v_mfma_f32_16x16x32_bf16 v[24:27], a[164:167], v[156:159], v[24:27]\n"
"v_exp_f32 v251, v251\n"
"v_mfma_f32_16x16x32_bf16 v[24:27], a[160:163], v[152:155], v[24:27]\n"
"v_cvt_pk_bf16_f32 v177, v250, v251\n"
"v_mul_f32 v230, s68, v230\n"
"v_mfma_f32_16x16x32_bf16 v[192:195], a[144:147], a[64:67], -4.0\n"
"v_exp_f32 v252, v252\n"
"v_mfma_f32_16x16x32_bf16 v[192:195], a[148:151], a[68:71], v[192:195]\n"
"s_waitcnt lgkmcnt(3)\n"
"ds_read_b128 a[128:131], v216 offset:57344\n"
"v_mul_f32 v231, s68, v231\n"
"v_mfma_f32_16x16x32_bf16 v[192:195], a[152:155], a[72:75], v[192:195]\n"
"v_exp_f32 v253, v253\n"
"v_mfma_f32_16x16x32_bf16 v[192:195], a[156:159], a[76:79], v[192:195]\n"
"ds_read_b128 a[132:135], v216 offset:57600\n"
"v_cvt_pk_bf16_f32 v184, v252, v253\n"
"v_mfma_f32_16x16x32_bf16 v[196:199], a[156:159], a[92:95], -4.0\n"
"v_exp_f32 v254, v254\n"
"v_mfma_f32_16x16x32_bf16 v[196:199], a[152:155], a[88:91], v[196:199]\n"
"ds_read_b128 a[136:139], v216 offset:57856\n"
"v_mul_f32 v232, s68, v232\n"
"v_mfma_f32_16x16x32_bf16 v[196:199], a[148:151], a[84:87], v[196:199]\n"
"s_add_u32 m0, s64, 16384\n"
"v_exp_f32 v255, v255\n"
"v_mfma_f32_16x16x32_bf16 v[196:199], a[144:147], a[80:83], v[196:199]\n"
"global_load_lds_dwordx4 v221, s[48:49] offset:1024\n"
"v_cvt_pk_bf16_f32 v185, v254, v255\n"
"v_mfma_f32_16x16x32_bf16 v[248:251], a[144:147], a[96:99], -4.0\n"
"v_exp_f32 v192, v192\n"
"v_mfma_f32_16x16x32_bf16 v[248:251], a[148:151], a[100:103], v[248:251]\n"
"ds_read_b128 a[140:143], v216 offset:58112\n"
"v_mul_f32 v233, s68, v233\n"
"v_mfma_f32_16x16x32_bf16 v[248:251], a[152:155], a[104:107], v[248:251]\n"
"v_exp_f32 v193, v193\n"
"v_mfma_f32_16x16x32_bf16 v[248:251], a[156:159], a[108:111], v[248:251]\n"
"ds_read_b64_tr_b16 a[160:161], v217 offset:33280\n"
"v_cvt_pk_bf16_f32 v162, v192, v193\n"
"v_mfma_f32_16x16x32_bf16 v[252:255], a[156:159], a[124:127], -4.0\n"
"v_exp_f32 v194, v194\n"
"v_mfma_f32_16x16x32_bf16 v[252:255], a[152:155], a[120:123], v[252:255]\n"
"ds_read_b64_tr_b16 a[162:163], v217 offset:37376\n"
"v_mul_f32 v234, s68, v234\n"
"v_mfma_f32_16x16x32_bf16 v[252:255], a[148:151], a[116:119], v[252:255]\n"
"s_add_u32 m0, s64, 16384\n"
"v_exp_f32 v195, v195\n"
"v_mfma_f32_16x16x32_bf16 v[252:255], a[144:147], a[112:115], v[252:255]\n"
"global_load_lds_dwordx4 v222, s[48:49] offset:2048\n"
"v_cvt_pk_bf16_f32 v163, v194, v195\n"
"v_mfma_f32_16x16x32_bf16 v[32:35], a[176:179], v[128:131], v[32:35]\n"
"v_exp_f32 v196, v196\n"
"v_mfma_f32_16x16x32_bf16 v[32:35], a[180:183], v[132:135], v[32:35]\n"
"s_waitcnt lgkmcnt(5)\n"
"ds_read_b64_tr_b16 a[164:165], v217 offset:41472\n"
"v_mul_f32 v235, s68, v235\n"
"v_mfma_f32_16x16x32_bf16 v[36:39], a[188:191], v[132:135], v[36:39]\n"
"v_exp_f32 v197, v197\n"
"v_mfma_f32_16x16x32_bf16 v[36:39], a[184:187], v[128:131], v[36:39]\n"
"ds_read_b64_tr_b16 a[166:167], v217 offset:45568\n"
"v_cvt_pk_bf16_f32 v170, v196, v197\n"
"v_mfma_f32_16x16x32_bf16 v[44:47], a[184:187], v[136:139], v[44:47]\n"
"v_exp_f32 v198, v198\n"
"v_mfma_f32_16x16x32_bf16 v[44:47], a[188:191], v[140:143], v[44:47]\n"
"ds_read_b64_tr_b16 a[168:169], v217 offset:33288\n"
"v_cvt_pk_bf16_f32 v228, v228, v229\n"
"v_mfma_f32_16x16x32_bf16 v[40:43], a[180:183], v[140:143], v[40:43]\n"
"v_exp_f32 v199, v199\n"
"v_mfma_f32_16x16x32_bf16 v[40:43], a[176:179], v[136:139], v[40:43]\n"
"v_cvt_pk_bf16_f32 v171, v198, v199\n"
"v_cvt_pk_bf16_f32 v230, v230, v231\n"
"v_mfma_f32_16x16x32_bf16 v[48:51], a[176:179], v[144:147], v[48:51]\n"
"v_exp_f32 v248, v248\n"
"v_mfma_f32_16x16x32_bf16 v[48:51], a[180:183], v[148:151], v[48:51]\n"
"ds_read_b64_tr_b16 a[170:171], v217 offset:37384\n"
"v_cvt_pk_bf16_f32 v232, v232, v233\n"
"v_mfma_f32_16x16x32_bf16 v[52:55], a[188:191], v[148:151], v[52:55]\n"
"v_exp_f32 v249, v249\n"
"v_mfma_f32_16x16x32_bf16 v[52:55], a[184:187], v[144:147], v[52:55]\n"
"ds_read_b64_tr_b16 a[172:173], v217 offset:41480\n"
"v_cvt_pk_bf16_f32 v178, v248, v249\n"
"v_mfma_f32_16x16x32_bf16 v[60:63], a[184:187], v[152:155], v[60:63]\n"
"v_exp_f32 v250, v250\n"
"v_mfma_f32_16x16x32_bf16 v[60:63], a[188:191], v[156:159], v[60:63]\n"
"ds_read_b64_tr_b16 a[174:175], v217 offset:45576\n"
"v_cvt_pk_bf16_f32 v234, v234, v235\n"
"v_mfma_f32_16x16x32_bf16 v[56:59], a[180:183], v[156:159], v[56:59]\n"
"s_add_u32 m0, s64, 16384\n"
"v_exp_f32 v251, v251\n"
"v_mfma_f32_16x16x32_bf16 v[56:59], a[176:179], v[152:155], v[56:59]\n"
"global_load_lds_dwordx4 v222, s[48:49] offset:3072\n"
"v_cvt_pk_bf16_f32 v179, v250, v251\n"
"v_mfma_f32_16x16x32_bf16 v[192:195], a[128:131], a[64:67], -4.0\n"
"s_waitcnt lgkmcnt(6)\n"
"v_exp_f32 v252, v252\n"
"v_mfma_f32_16x16x32_bf16 v[192:195], a[132:135], a[68:71], v[192:195]\n"
"ds_read_b128 a[144:147], v216 offset:61440\n"
"v_accvgpr_write_b32 a56, v228\n"
"v_mfma_f32_16x16x32_bf16 v[192:195], a[136:139], a[72:75], v[192:195]\n"
"v_exp_f32 v253, v253\n"
"v_mfma_f32_16x16x32_bf16 v[192:195], a[140:143], a[76:79], v[192:195]\n"
"ds_read_b128 a[148:151], v216 offset:61696\n"
"v_cvt_pk_bf16_f32 v186, v252, v253\n"
"v_mfma_f32_16x16x32_bf16 v[196:199], a[140:143], a[92:95], -4.0\n"
"v_exp_f32 v254, v254\n"
"v_mfma_f32_16x16x32_bf16 v[196:199], a[136:139], a[88:91], v[196:199]\n"
"ds_read_b128 a[152:155], v216 offset:61952\n"
"v_accvgpr_write_b32 a57, v230\n"
"v_mfma_f32_16x16x32_bf16 v[196:199], a[132:135], a[84:87], v[196:199]\n"
"s_add_u32 m0, s66, 0\n"
"v_exp_f32 v255, v255\n"
"v_mfma_f32_16x16x32_bf16 v[196:199], a[128:131], a[80:83], v[196:199]\n"
"global_load_lds_dwordx4 v222, s[52:53] offset:2048\n"
"v_cvt_pk_bf16_f32 v187, v254, v255\n"
"v_mfma_f32_16x16x32_bf16 v[248:251], a[128:131], a[96:99], -4.0\n"
"v_exp_f32 v192, v192\n"
"v_mfma_f32_16x16x32_bf16 v[248:251], a[132:135], a[100:103], v[248:251]\n"
"ds_read_b128 a[156:159], v216 offset:62208\n"
"v_accvgpr_write_b32 a58, v232\n"
"v_mfma_f32_16x16x32_bf16 v[248:251], a[136:139], a[104:107], v[248:251]\n"
"v_exp_f32 v193, v193\n"
"v_mfma_f32_16x16x32_bf16 v[248:251], a[140:143], a[108:111], v[248:251]\n"
"ds_read_b64_tr_b16 a[176:177], v217 offset:33536\n"
"v_cvt_pk_bf16_f32 v164, v192, v193\n"
"v_mfma_f32_16x16x32_bf16 v[252:255], a[140:143], a[124:127], -4.0\n"
"v_exp_f32 v194, v194\n"
"v_mfma_f32_16x16x32_bf16 v[252:255], a[136:139], a[120:123], v[252:255]\n"
"ds_read_b64_tr_b16 a[178:179], v217 offset:37632\n"
"v_accvgpr_write_b32 a59, v234\n"
"v_mfma_f32_16x16x32_bf16 v[252:255], a[132:135], a[116:119], v[252:255]\n"
"v_exp_f32 v195, v195\n"
"v_mfma_f32_16x16x32_bf16 v[252:255], a[128:131], a[112:115], v[252:255]\n"
"v_cvt_pk_bf16_f32 v165, v194, v195\n"
"v_lshlrev_b32 v228, 16, v200\n"
"v_mfma_f32_16x16x32_bf16 v[64:67], a[160:163], v[128:131], v[64:67]\n"
"s_waitcnt lgkmcnt(6)\n"
"v_exp_f32 v196, v196\n"
"v_mfma_f32_16x16x32_bf16 v[64:67], a[164:167], v[132:135], v[64:67]\n"
"ds_read_b64_tr_b16 a[180:181], v217 offset:41728\n"
"v_and_b32 v229, s67, v200\n"
"v_mfma_f32_16x16x32_bf16 v[68:71], a[172:175], v[132:135], v[68:71]\n"
"v_exp_f32 v197, v197\n"
"v_mfma_f32_16x16x32_bf16 v[68:71], a[168:171], v[128:131], v[68:71]\n"
"ds_read_b64_tr_b16 a[182:183], v217 offset:45824\n"
"v_cvt_pk_bf16_f32 v172, v196, v197\n"
"v_mfma_f32_16x16x32_bf16 v[76:79], a[168:171], v[136:139], v[76:79]\n"
"v_exp_f32 v198, v198\n"
"v_mfma_f32_16x16x32_bf16 v[76:79], a[172:175], v[140:143], v[76:79]\n"
"ds_read_b64_tr_b16 a[184:185], v217 offset:33544\n"
"v_lshlrev_b32 v230, 16, v201\n"
"v_mfma_f32_16x16x32_bf16 v[72:75], a[164:167], v[140:143], v[72:75]\n"
"v_exp_f32 v199, v199\n"
"v_mfma_f32_16x16x32_bf16 v[72:75], a[160:163], v[136:139], v[72:75]\n"
"buffer_store_dwordx4 a[228:231], v223, s[56:59], s37 offen offset:2176 sc1 nt\n"
"v_cvt_pk_bf16_f32 v173, v198, v199\n"
"v_and_b32 v231, s67, v201\n"
"v_lshlrev_b32 v232, 16, v202\n"
"v_mfma_f32_16x16x32_bf16 v[80:83], a[160:163], v[144:147], v[80:83]\n"
"v_exp_f32 v248, v248\n"
"v_mfma_f32_16x16x32_bf16 v[80:83], a[164:167], v[148:151], v[80:83]\n"
"ds_read_b64_tr_b16 a[186:187], v217 offset:37640\n"
"v_and_b32 v233, s67, v202\n"
"v_lshlrev_b32 v234, 16, v203\n"
"v_and_b32 v235, s67, v203\n"
"v_mul_f32 v228, s68, v228\n"
"v_mul_f32 v229, s68, v229\n"
"v_pk_mul_f32 v[230:231], v[230:231], s[68:69]\n"
"v_pk_mul_f32 v[232:233], v[232:233], s[68:69]\n"
"v_pk_mul_f32 v[234:235], v[234:235], s[68:69]\n"
"v_cvt_pk_bf16_f32 v228, v228, v229\n"
"v_cvt_pk_bf16_f32 v230, v230, v231\n"
"v_mfma_f32_16x16x32_bf16 v[84:87], a[172:175], v[148:151], v[84:87]\n"
"v_exp_f32 v249, v249\n"
"v_mfma_f32_16x16x32_bf16 v[84:87], a[168:171], v[144:147], v[84:87]\n"
"ds_read_b64_tr_b16 a[188:189], v217 offset:41736\n"
"v_cvt_pk_bf16_f32 v180, v248, v249\n"
"v_mfma_f32_16x16x32_bf16 v[92:95], a[168:171], v[152:155], v[92:95]\n"
"v_exp_f32 v250, v250\n"
"v_mfma_f32_16x16x32_bf16 v[92:95], a[172:175], v[156:159], v[92:95]\n"
"ds_read_b64_tr_b16 a[190:191], v217 offset:45832\n"
"v_cvt_pk_bf16_f32 v232, v232, v233\n"
"v_mfma_f32_16x16x32_bf16 v[88:91], a[164:167], v[156:159], v[88:91]\n"
"v_exp_f32 v251, v251\n"
"v_mfma_f32_16x16x32_bf16 v[88:91], a[160:163], v[152:155], v[88:91]\n"
"s_waitcnt vmcnt(13) lgkmcnt(5)\n"
"v_cvt_pk_bf16_f32 v181, v250, v251\n"
"v_cvt_pk_bf16_f32 v234, v234, v235\n"
"v_mfma_f32_16x16x32_bf16 v[192:195], a[144:147], a[64:67], -4.0\n"
"s_barrier\n"
"v_exp_f32 v252, v252\n"
"v_mfma_f32_16x16x32_bf16 v[192:195], a[148:151], a[68:71], v[192:195]\n"
"s_bfe_u32 s50, s38, 0x30004\n"
"ds_read_b128 a[128:131], v216 offset:0\n"
"v_accvgpr_write_b32 a60, v228\n"
"v_mfma_f32_16x16x32_bf16 v[192:195], a[152:155], a[72:75], v[192:195]\n"
"s_lshl_b32 s50, s50, 14\n"
"v_exp_f32 v253, v253\n"
"v_mfma_f32_16x16x32_bf16 v[192:195], a[156:159], a[76:79], v[192:195]\n"
"s_add_u32 s39, s54, s50\n"
"ds_read_b128 a[132:135], v216 offset:256\n"
"v_cvt_pk_bf16_f32 v188, v252, v253\n"
"v_mfma_f32_16x16x32_bf16 v[196:199], a[156:159], a[92:95], -4.0\n"
"s_add_u32 s50, s44, s39\n"
"v_exp_f32 v254, v254\n"
"v_mfma_f32_16x16x32_bf16 v[196:199], a[152:155], a[88:91], v[196:199]\n"
"s_addc_u32 s51, s45, 0\n"
"ds_read_b128 a[136:139], v216 offset:512\n"
"v_accvgpr_write_b32 a61, v230\n"
"v_mfma_f32_16x16x32_bf16 v[196:199], a[148:151], a[84:87], v[196:199]\n"
"s_add_u32 m0, s65, 16384\n"
"v_exp_f32 v255, v255\n"
"v_mfma_f32_16x16x32_bf16 v[196:199], a[144:147], a[80:83], v[196:199]\n"
"global_load_lds_dwordx4 v221, s[50:51] offset:0\n"
"v_cvt_pk_bf16_f32 v189, v254, v255\n"
"v_mfma_f32_16x16x32_bf16 v[248:251], a[144:147], a[96:99], -4.0\n"
"v_exp_f32 v192, v192\n"
"v_mfma_f32_16x16x32_bf16 v[248:251], a[148:151], a[100:103], v[248:251]\n"
"ds_read_b128 a[140:143], v216 offset:768\n"
"v_accvgpr_write_b32 a62, v232\n"
"v_mfma_f32_16x16x32_bf16 v[248:251], a[152:155], a[104:107], v[248:251]\n"
"v_exp_f32 v193, v193\n"
"v_mfma_f32_16x16x32_bf16 v[248:251], a[156:159], a[108:111], v[248:251]\n"
"ds_read_b64_tr_b16 a[160:161], v217 offset:49152\n"
"v_cvt_pk_bf16_f32 v166, v192, v193\n"
"v_mfma_f32_16x16x32_bf16 v[252:255], a[156:159], a[124:127], -4.0\n"
"v_exp_f32 v194, v194\n"
"v_mfma_f32_16x16x32_bf16 v[252:255], a[152:155], a[120:123], v[252:255]\n"
"ds_read_b64_tr_b16 a[162:163], v217 offset:53248\n"
"v_accvgpr_write_b32 a63, v234\n"
"v_mfma_f32_16x16x32_bf16 v[252:255], a[148:151], a[116:119], v[252:255]\n"
"v_exp_f32 v195, v195\n"
"v_mfma_f32_16x16x32_bf16 v[252:255], a[144:147], a[112:115], v[252:255]\n"
"v_cvt_pk_bf16_f32 v167, v194, v195\n"
"v_mfma_f32_16x16x32_bf16 v[96:99], a[176:179], v[128:131], v[96:99]\n"
"s_waitcnt lgkmcnt(6)\n"
"v_exp_f32 v196, v196\n"
"v_mfma_f32_16x16x32_bf16 v[96:99], a[180:183], v[132:135], v[96:99]\n"
"ds_read_b64_tr_b16 a[164:165], v217 offset:57344\n"
"v_mfma_f32_16x16x32_bf16 v[100:103], a[188:191], v[132:135], v[100:103]\n"
"v_exp_f32 v197, v197\n"
"v_mfma_f32_16x16x32_bf16 v[100:103], a[184:187], v[128:131], v[100:103]\n"
"ds_read_b64_tr_b16 a[166:167], v217 offset:61440\n"
"v_cvt_pk_bf16_f32 v174, v196, v197\n"
"v_mfma_f32_16x16x32_bf16 v[108:111], a[184:187], v[136:139], v[108:111]\n"
"v_exp_f32 v198, v198\n"
"v_mfma_f32_16x16x32_bf16 v[108:111], a[188:191], v[140:143], v[108:111]\n"
"ds_read_b64_tr_b16 a[168:169], v217 offset:49160\n"
"v_mfma_f32_16x16x32_bf16 v[104:107], a[180:183], v[140:143], v[104:107]\n"
"s_add_u32 m0, s65, 16384\n"
"v_exp_f32 v199, v199\n"
"v_mfma_f32_16x16x32_bf16 v[104:107], a[176:179], v[136:139], v[104:107]\n"
"global_load_lds_dwordx4 v221, s[50:51] offset:1024\n"
"v_cvt_pk_bf16_f32 v175, v198, v199\n"
"v_mfma_f32_16x16x32_bf16 v[112:115], a[176:179], v[144:147], v[112:115]\n"
"s_add_u32 s56, s46, s98\n"
"v_exp_f32 v248, v248\n"
"v_mfma_f32_16x16x32_bf16 v[112:115], a[180:183], v[148:151], v[112:115]\n"
"s_addc_u32 s57, s47, 0\n"
"ds_read_b64_tr_b16 a[170:171], v217 offset:53256\n"
"v_mfma_f32_16x16x32_bf16 v[116:119], a[188:191], v[148:151], v[116:119]\n"
"s_mov_b32 s58, 16384\n"
"v_exp_f32 v249, v249\n"
"v_mfma_f32_16x16x32_bf16 v[116:119], a[184:187], v[144:147], v[116:119]\n"
"ds_read_b64_tr_b16 a[172:173], v217 offset:57352\n"
"v_cvt_pk_bf16_f32 v182, v248, v249\n"
"v_mfma_f32_16x16x32_bf16 v[124:127], a[184:187], v[152:155], v[124:127]\n"
"v_exp_f32 v250, v250\n"
"v_mfma_f32_16x16x32_bf16 v[124:127], a[188:191], v[156:159], v[124:127]\n"
"ds_read_b64_tr_b16 a[174:175], v217 offset:61448\n"
"v_mfma_f32_16x16x32_bf16 v[120:123], a[180:183], v[156:159], v[120:123]\n"
"s_add_u32 m0, s65, 16384\n"
"v_exp_f32 v251, v251\n"
"v_mfma_f32_16x16x32_bf16 v[120:123], a[176:179], v[152:155], v[120:123]\n"
"s_waitcnt lgkmcnt(6)\n"
"global_load_lds_dwordx4 v221, s[50:51] offset:2048\n"
"v_cvt_pk_bf16_f32 v183, v250, v251\n"
".Lattn_A0:\n"
"v_mfma_f32_16x16x32_bf16 v[192:195], a[128:131], a[0:3], -4.0\n"
"v_exp_f32 v252, v252\n"
"v_mfma_f32_16x16x32_bf16 v[192:195], a[132:135], a[4:7], v[192:195]\n"
"ds_read_b128 a[144:147], v216 offset:4096\n"
"v_mfma_f32_16x16x32_bf16 v[192:195], a[136:139], a[8:11], v[192:195]\n"
"v_exp_f32 v253, v253\n"
"v_mfma_f32_16x16x32_bf16 v[192:195], a[140:143], a[12:15], v[192:195]\n"
"ds_read_b128 a[148:151], v216 offset:4352\n"
"v_cvt_pk_bf16_f32 v190, v252, v253\n"
"v_mfma_f32_16x16x32_bf16 v[196:199], a[140:143], a[28:31], -4.0\n"
"v_exp_f32 v254, v254\n"
"v_mfma_f32_16x16x32_bf16 v[196:199], a[136:139], a[24:27], v[196:199]\n"
"ds_read_b128 a[152:155], v216 offset:4608\n"
"v_mfma_f32_16x16x32_bf16 v[196:199], a[132:135], a[20:23], v[196:199]\n"
"v_exp_f32 v255, v255\n"
"v_mfma_f32_16x16x32_bf16 v[196:199], a[128:131], a[16:19], v[196:199]\n"
"v_cvt_pk_bf16_f32 v191, v254, v255\n"
"v_mfma_f32_16x16x32_bf16 v[248:251], a[128:131], a[32:35], -4.0\n"
"v_exp_f32 v192, v192\n"
"v_mfma_f32_16x16x32_bf16 v[248:251], a[132:135], a[36:39], v[248:251]\n"
"ds_read_b128 a[156:159], v216 offset:4864\n"
"v_mfma_f32_16x16x32_bf16 v[248:251], a[136:139], a[40:43], v[248:251]\n"
"v_exp_f32 v193, v193\n"
"v_mfma_f32_16x16x32_bf16 v[248:251], a[140:143], a[44:47], v[248:251]\n"
"ds_read_b64_tr_b16 a[176:177], v217 offset:49408\n"
"v_cvt_pk_bf16_f32 v128, v192, v193\n"
"v_mfma_f32_16x16x32_bf16 v[252:255], a[140:143], a[60:63], -4.0\n"
"v_exp_f32 v194, v194\n"
"v_mfma_f32_16x16x32_bf16 v[252:255], a[136:139], a[56:59], v[252:255]\n"
"ds_read_b64_tr_b16 a[178:179], v217 offset:53504\n"
"v_mfma_f32_16x16x32_bf16 v[252:255], a[132:135], a[52:55], v[252:255]\n"
"s_add_u32 m0, s65, 16384\n"
"v_exp_f32 v195, v195\n"
"v_mfma_f32_16x16x32_bf16 v[252:255], a[128:131], a[48:51], v[252:255]\n"
"global_load_lds_dwordx4 v219, s[50:51] offset:3072\n"
"v_cvt_pk_bf16_f32 v129, v194, v195\n"
"v_smfmac_f32_16x16x64_bf16 a[192:195], a[208:211], v[160:167], v215\n"
"v_smfmac_f32_16x16x64_bf16 a[196:199], a[208:211], v[168:175], v215\n"
"ds_read_b64_tr_b16 a[180:181], v217 offset:57600\n"
"v_smfmac_f32_16x16x64_bf16 a[200:203], a[208:211], v[176:183], v215\n"
"v_smfmac_f32_16x16x64_bf16 a[204:207], a[208:211], v[184:191], v215\n"
"ds_read_b64_tr_b16 a[182:183], v217 offset:61696\n"
"v_mfma_f32_16x16x32_bf16 v[0:3], a[160:163], v[160:163], v[0:3]\n"
"s_waitcnt lgkmcnt(7)\n"
"v_exp_f32 v196, v196\n"
"v_mfma_f32_16x16x32_bf16 v[0:3], a[164:167], v[164:167], v[0:3]\n"
"ds_read_b64_tr_b16 a[184:185], v217 offset:49416\n"
"v_accvgpr_read_b32 v208, a192\n"
"v_accvgpr_read_b32 v213, a193\n"
"v_mfma_f32_16x16x32_bf16 v[4:7], a[172:175], v[164:167], v[4:7]\n"
"v_exp_f32 v197, v197\n"
"v_mfma_f32_16x16x32_bf16 v[4:7], a[168:171], v[160:163], v[4:7]\n"
"v_cvt_pk_bf16_f32 v136, v196, v197\n"
"v_add_f32 v208, v208, v213\n"
"v_accvgpr_read_b32 v210, a196\n"
"v_mfma_f32_16x16x32_bf16 v[12:15], a[168:171], v[168:171], v[12:15]\n"
"v_exp_f32 v198, v198\n"
"v_mfma_f32_16x16x32_bf16 v[12:15], a[172:175], v[172:175], v[12:15]\n"
"s_bfe_u32 s48, s38, 0x30008\n"
"ds_read_b64_tr_b16 a[186:187], v217 offset:53512\n"
"v_accvgpr_read_b32 v213, a197\n"
"v_add_f32 v210, v210, v213\n"
"v_mfma_f32_16x16x32_bf16 v[8:11], a[164:167], v[172:175], v[8:11]\n"
"s_lshl_b32 s48, s48, 14\n"
"v_exp_f32 v199, v199\n"
"v_mfma_f32_16x16x32_bf16 v[8:11], a[160:163], v[168:171], v[8:11]\n"
"s_add_u32 s36, s79, s48\n"
"ds_read_b64_tr_b16 a[188:189], v217 offset:57608\n"
"v_cvt_pk_bf16_f32 v137, v198, v199\n"
"v_max3_u32 v209, v209, v208, v210\n"
"v_min3_u32 v211, v211, v208, v210\n"
"v_mfma_f32_16x16x32_bf16 v[16:19], a[160:163], v[176:179], v[16:19]\n"
"s_add_u32 s48, s42, s36\n"
"v_exp_f32 v248, v248\n"
"v_mfma_f32_16x16x32_bf16 v[16:19], a[164:167], v[180:183], v[16:19]\n"
"s_addc_u32 s49, s43, 0\n"
"ds_read_b64_tr_b16 a[190:191], v217 offset:61704\n"
"v_rcp_f32 v208, v208\n"
"v_rcp_f32 v210, v210\n"
"v_mfma_f32_16x16x32_bf16 v[20:23], a[172:175], v[180:183], v[20:23]\n"
"s_add_u32 m0, s64, 32768\n"
"v_exp_f32 v249, v249\n"
"v_mfma_f32_16x16x32_bf16 v[20:23], a[168:171], v[176:179], v[20:23]\n"
"global_load_lds_dwordx4 v219, s[48:49] offset:0\n"
"v_cvt_pk_bf16_f32 v144, v248, v249\n"
"v_accvgpr_read_b32 v212, a200\n"
"v_accvgpr_read_b32 v213, a201\n"
"v_mfma_f32_16x16x32_bf16 v[28:31], a[168:171], v[184:187], v[28:31]\n"
"v_exp_f32 v250, v250\n"
"v_mfma_f32_16x16x32_bf16 v[28:31], a[172:175], v[188:191], v[28:31]\n"
"v_add_f32 v212, v212, v213\n"
"v_accvgpr_read_b32 v214, a204\n"
"v_mfma_f32_16x16x32_bf16 v[24:27], a[164:167], v[188:191], v[24:27]\n"
"v_exp_f32 v251, v251\n"
"v_mfma_f32_16x16x32_bf16 v[24:27], a[160:163], v[184:187], v[24:27]\n"
"v_cvt_pk_bf16_f32 v145, v250, v251\n"
"v_accvgpr_read_b32 v213, a205\n"
"v_add_f32 v214, v214, v213\n"
"v_mfma_f32_16x16x32_bf16 v[192:195], a[144:147], a[0:3], -4.0\n"
"s_waitcnt lgkmcnt(3)\n"
"v_exp_f32 v252, v252\n"
"v_mfma_f32_16x16x32_bf16 v[192:195], a[148:151], a[4:7], v[192:195]\n"
"ds_read_b128 a[128:131], v216 offset:8192\n"
"v_max3_u32 v209, v209, v212, v214\n"
"v_mfma_f32_16x16x32_bf16 v[192:195], a[152:155], a[8:11], v[192:195]\n"
"v_exp_f32 v253, v253\n"
"v_mfma_f32_16x16x32_bf16 v[192:195], a[156:159], a[12:15], v[192:195]\n"
"ds_read_b128 a[132:135], v216 offset:8448\n"
"v_cvt_pk_bf16_f32 v152, v252, v253\n"
"v_min3_u32 v211, v211, v212, v214\n"
"v_rcp_f32 v212, v212\n"
"v_mfma_f32_16x16x32_bf16 v[196:199], a[156:159], a[28:31], -4.0\n"
"v_exp_f32 v254, v254\n"
"v_mfma_f32_16x16x32_bf16 v[196:199], a[152:155], a[24:27], v[196:199]\n"
"ds_read_b128 a[136:139], v216 offset:8704\n"
"v_accvgpr_write_b32 a192, 0\n"
"v_mfma_f32_16x16x32_bf16 v[196:199], a[148:151], a[20:23], v[196:199]\n"
"s_add_u32 m0, s64, 32768\n"
"v_exp_f32 v255, v255\n"
"v_mfma_f32_16x16x32_bf16 v[196:199], a[144:147], a[16:19], v[196:199]\n"
"global_load_lds_dwordx4 v219, s[48:49] offset:1024\n"
"v_cvt_pk_bf16_f32 v153, v254, v255\n"
"v_rcp_f32 v214, v214\n"
"v_mfma_f32_16x16x32_bf16 v[248:251], a[144:147], a[32:35], -4.0\n"
"v_exp_f32 v192, v192\n"
"v_mfma_f32_16x16x32_bf16 v[248:251], a[148:151], a[36:39], v[248:251]\n"
"ds_read_b128 a[140:143], v216 offset:8960\n"
"v_accvgpr_write_b32 a193, 0\n"
"v_mfma_f32_16x16x32_bf16 v[248:251], a[152:155], a[40:43], v[248:251]\n"
"v_exp_f32 v193, v193\n"
"v_mfma_f32_16x16x32_bf16 v[248:251], a[156:159], a[44:47], v[248:251]\n"
"ds_read_b64_tr_b16 a[160:161], v217 offset:49664\n"
"v_cvt_pk_bf16_f32 v130, v192, v193\n"
"v_accvgpr_write_b32 a196, 0\n"
"v_accvgpr_write_b32 a197, 0\n"
"v_accvgpr_write_b32 a200, 0\n"
"v_pk_mul_f32 v[0:1], v[0:1], v[208:209] op_sel_hi:[1,0]\n"
"v_pk_mul_f32 v[2:3], v[2:3], v[208:209] op_sel_hi:[1,0]\n"
"v_pk_mul_f32 v[4:5], v[4:5], v[208:209] op_sel_hi:[1,0]\n"
"v_pk_mul_f32 v[6:7], v[6:7], v[208:209] op_sel_hi:[1,0]\n"
"v_cvt_pk_bf16_f32 v0, v0, v1\n"
"v_cvt_pk_bf16_f32 v1, v2, v3\n"
"v_mfma_f32_16x16x32_bf16 v[252:255], a[156:159], a[60:63], -4.0\n"
".Lattn_pad5:\n"
"s_cbranch_vccnz .Lattn_ret\n"
"v_exp_f32 v194, v194\n"
"v_mfma_f32_16x16x32_bf16 v[252:255], a[152:155], a[56:59], v[252:255]\n"
"ds_read_b64_tr_b16 a[162:163], v217 offset:53760\n"
"v_cvt_pk_bf16_f32 v2, v4, v5\n"
"v_mfma_f32_16x16x32_bf16 v[252:255], a[148:151], a[52:55], v[252:255]\n"
"s_add_u32 m0, s64, 32768\n"
"v_exp_f32 v195, v195\n"
"v_mfma_f32_16x16x32_bf16 v[252:255], a[144:147], a[48:51], v[252:255]\n"
"global_load_lds_dwordx4 v220, s[48:49] offset:2048\n"
"v_cvt_pk_bf16_f32 v131, v194, v195\n"
"v_mfma_f32_16x16x32_bf16 v[32:35], a[176:179], v[160:163], v[32:35]\n"
"v_exp_f32 v196, v196\n"
"v_mfma_f32_16x16x32_bf16 v[32:35], a[180:183], v[164:167], v[32:35]\n"
"s_waitcnt lgkmcnt(4)\n"
"ds_read_b64_tr_b16 a[164:165], v217 offset:57856\n"
"v_cvt_pk_bf16_f32 v3, v6, v7\n"
"v_mfma_f32_16x16x32_bf16 v[36:39], a[188:191], v[164:167], v[36:39]\n"
"v_exp_f32 v197, v197\n"
"v_mfma_f32_16x16x32_bf16 v[36:39], a[184:187], v[160:163], v[36:39]\n"
"ds_read_b64_tr_b16 a[166:167], v217 offset:61952\n"
"v_cvt_pk_bf16_f32 v138, v196, v197\n"
"v_mfma_f32_16x16x32_bf16 v[44:47], a[184:187], v[168:171], v[44:47]\n"
"v_exp_f32 v198, v198\n"
"v_mfma_f32_16x16x32_bf16 v[44:47], a[188:191], v[172:175], v[44:47]\n"
"ds_read_b64_tr_b16 a[168:169], v217 offset:49672\n"
"ds_write_b128 v224, v[0:3] offset:49152\n"
"v_pk_mul_f32 v[8:9], v[8:9], v[210:211] op_sel_hi:[1,0]\n"
"v_pk_mul_f32 v[10:11], v[10:11], v[210:211] op_sel_hi:[1,0]\n"
"v_pk_mul_f32 v[12:13], v[12:13], v[210:211] op_sel_hi:[1,0]\n"
"v_pk_mul_f32 v[14:15], v[14:15], v[210:211] op_sel_hi:[1,0]\n"
"v_cvt_pk_bf16_f32 v8, v8, v9\n"
"v_cvt_pk_bf16_f32 v9, v10, v11\n"
"v_mfma_f32_16x16x32_bf16 v[40:43], a[180:183], v[172:175], v[40:43]\n"
"s_add_u32 m0, s66, 0\n"
"v_exp_f32 v199, v199\n"
"v_mfma_f32_16x16x32_bf16 v[40:43], a[176:179], v[168:171], v[40:43]\n"
"global_load_lds_dwordx4 v220, s[52:53] offset:3072\n"
"v_cvt_pk_bf16_f32 v139, v198, v199\n"
"v_mfma_f32_16x16x32_bf16 v[48:51], a[176:179], v[176:179], v[48:51]\n"
"v_exp_f32 v248, v248\n"
"v_mfma_f32_16x16x32_bf16 v[48:51], a[180:183], v[180:183], v[48:51]\n"
"ds_read_b64_tr_b16 a[170:171], v217 offset:53768\n"
"v_cvt_pk_bf16_f32 v10, v12, v13\n"
"v_mfma_f32_16x16x32_bf16 v[52:55], a[188:191], v[180:183], v[52:55]\n"
"v_exp_f32 v249, v249\n"
"v_mfma_f32_16x16x32_bf16 v[52:55], a[184:187], v[176:179], v[52:55]\n"
"ds_read_b64_tr_b16 a[172:173], v217 offset:57864\n"
"v_cvt_pk_bf16_f32 v146, v248, v249\n"
"v_cvt_pk_bf16_f32 v11, v14, v15\n"
"v_mfma_f32_16x16x32_bf16 v[60:63], a[184:187], v[184:187], v[60:63]\n"
"v_exp_f32 v250, v250\n"
"v_mfma_f32_16x16x32_bf16 v[60:63], a[188:191], v[188:191], v[60:63]\n"
"ds_read_b64_tr_b16 a[174:175], v217 offset:61960\n"
"ds_write_b128 v224, v[8:11] offset:51200\n"
"v_pk_mul_f32 v[32:33], v[32:33], v[208:209] op_sel_hi:[1,0]\n"
"v_pk_mul_f32 v[34:35], v[34:35], v[208:209] op_sel_hi:[1,0]\n"
"v_pk_mul_f32 v[36:37], v[36:37], v[208:209] op_sel_hi:[1,0]\n"
"v_pk_mul_f32 v[38:39], v[38:39], v[208:209] op_sel_hi:[1,0]\n"
"v_cvt_pk_bf16_f32 v32, v32, v33\n"
"v_cvt_pk_bf16_f32 v33, v34, v35\n"
"v_mfma_f32_16x16x32_bf16 v[56:59], a[180:183], v[188:191], v[56:59]\n"
"s_add_u32 m0, s64, 32768\n"
"v_exp_f32 v251, v251\n"
"v_mfma_f32_16x16x32_bf16 v[56:59], a[176:179], v[184:187], v[56:59]\n"
"global_load_lds_dwordx4 v220, s[48:49] offset:3072\n"
"v_cvt_pk_bf16_f32 v147, v250, v251\n"
"v_cvt_pk_bf16_f32 v34, v36, v37\n"
"v_cvt_pk_bf16_f32 v35, v38, v39\n"
"v_mfma_f32_16x16x32_bf16 v[192:195], a[128:131], a[0:3], -4.0\n"
"v_exp_f32 v252, v252\n"
"v_mfma_f32_16x16x32_bf16 v[192:195], a[132:135], a[4:7], v[192:195]\n"
"s_waitcnt lgkmcnt(4)\n"
"ds_read_b128 a[144:147], v216 offset:12288\n"
"v_accvgpr_write_b32 a201, 0\n"
"v_mfma_f32_16x16x32_bf16 v[192:195], a[136:139], a[8:11], v[192:195]\n"
"v_exp_f32 v253, v253\n"
"v_mfma_f32_16x16x32_bf16 v[192:195], a[140:143], a[12:15], v[192:195]\n"
"ds_read_b128 a[148:151], v216 offset:12544\n"
"ds_write_b128 v225, v[32:35] offset:49152\n"
"v_cvt_pk_bf16_f32 v154, v252, v253\n"
"v_pk_mul_f32 v[40:41], v[40:41], v[210:211] op_sel_hi:[1,0]\n"
"v_pk_mul_f32 v[42:43], v[42:43], v[210:211] op_sel_hi:[1,0]\n"
"v_pk_mul_f32 v[44:45], v[44:45], v[210:211] op_sel_hi:[1,0]\n"
"v_pk_mul_f32 v[46:47], v[46:47], v[210:211] op_sel_hi:[1,0]\n"
"v_cvt_pk_bf16_f32 v40, v40, v41\n"
"v_cvt_pk_bf16_f32 v41, v42, v43\n"
"v_mfma_f32_16x16x32_bf16 v[196:199], a[140:143], a[28:31], -4.0\n"
"v_exp_f32 v254, v254\n"
"v_mfma_f32_16x16x32_bf16 v[196:199], a[136:139], a[24:27], v[196:199]\n"
"ds_read_b128 a[152:155], v216 offset:12800\n"
"v_cvt_pk_bf16_f32 v42, v44, v45\n"
"v_mfma_f32_16x16x32_bf16 v[196:199], a[132:135], a[20:23], v[196:199]\n"
"v_exp_f32 v255, v255\n"
"v_mfma_f32_16x16x32_bf16 v[196:199], a[128:131], a[16:19], v[196:199]\n"
"ds_read_b128 a[216:219], v226 offset:49152\n"
"v_cvt_pk_bf16_f32 v155, v254, v255\n"
"v_cvt_pk_bf16_f32 v43, v46, v47\n"
"v_mfma_f32_16x16x32_bf16 v[248:251], a[128:131], a[32:35], -4.0\n"
"v_exp_f32 v192, v192\n"
"v_mfma_f32_16x16x32_bf16 v[248:251], a[132:135], a[36:39], v[248:251]\n"
"ds_read_b128 a[156:159], v216 offset:13056\n"
"v_accvgpr_write_b32 a204, 0\n"
"v_mfma_f32_16x16x32_bf16 v[248:251], a[136:139], a[40:43], v[248:251]\n"
"v_exp_f32 v193, v193\n"
"v_mfma_f32_16x16x32_bf16 v[248:251], a[140:143], a[44:47], v[248:251]\n"
"ds_read_b64_tr_b16 a[176:177], v217 offset:49920\n"
"ds_write_b128 v225, v[40:43] offset:51200\n"
"v_cvt_pk_bf16_f32 v132, v192, v193\n"
"v_pk_mul_f32 v[16:17], v[16:17], v[212:213] op_sel_hi:[1,0]\n"
"v_pk_mul_f32 v[18:19], v[18:19], v[212:213] op_sel_hi:[1,0]\n"
"v_pk_mul_f32 v[20:21], v[20:21], v[212:213] op_sel_hi:[1,0]\n"
"v_pk_mul_f32 v[22:23], v[22:23], v[212:213] op_sel_hi:[1,0]\n"
"v_cvt_pk_bf16_f32 v16, v16, v17\n"
"v_cvt_pk_bf16_f32 v17, v18, v19\n"
"v_mfma_f32_16x16x32_bf16 v[252:255], a[140:143], a[60:63], -4.0\n"
"v_exp_f32 v194, v194\n"
"v_mfma_f32_16x16x32_bf16 v[252:255], a[136:139], a[56:59], v[252:255]\n"
"ds_read_b64_tr_b16 a[178:179], v217 offset:54016\n"
"v_cvt_pk_bf16_f32 v18, v20, v21\n"
"v_mfma_f32_16x16x32_bf16 v[252:255], a[132:135], a[52:55], v[252:255]\n"
"v_exp_f32 v195, v195\n"
"v_mfma_f32_16x16x32_bf16 v[252:255], a[128:131], a[48:51], v[252:255]\n"
"ds_read_b128 a[220:223], v226 offset:50176\n"
"ds_read_b128 a[224:227], v226 offset:51200\n"
"v_cvt_pk_bf16_f32 v133, v194, v195\n"
"v_mfma_f32_16x16x32_bf16 v[64:67], a[160:163], v[160:163], v[64:67]\n"
"v_exp_f32 v196, v196\n"
"v_mfma_f32_16x16x32_bf16 v[64:67], a[164:167], v[164:167], v[64:67]\n"
"s_waitcnt lgkmcnt(8)\n"
"ds_read_b64_tr_b16 a[180:181], v217 offset:58112\n"
"ds_read_b128 a[228:231], v226 offset:52224\n"
"v_mfma_f32_16x16x32_bf16 v[68:71], a[172:175], v[164:167], v[68:71]\n"
"v_exp_f32 v197, v197\n"
"v_mfma_f32_16x16x32_bf16 v[68:71], a[168:171], v[160:163], v[68:71]\n"
"ds_read_b64_tr_b16 a[182:183], v217 offset:62208\n"
"v_cvt_pk_bf16_f32 v140, v196, v197\n"
"v_cvt_pk_bf16_f32 v19, v22, v23\n"
"v_mfma_f32_16x16x32_bf16 v[76:79], a[168:171], v[168:171], v[76:79]\n"
"v_exp_f32 v198, v198\n"
"v_mfma_f32_16x16x32_bf16 v[76:79], a[172:175], v[172:175], v[76:79]\n"
"ds_read_b64_tr_b16 a[184:185], v217 offset:49928\n"
"ds_write_b128 v224, v[16:19] offset:49152\n"
"v_pk_mul_f32 v[48:49], v[48:49], v[212:213] op_sel_hi:[1,0]\n"
"v_pk_mul_f32 v[50:51], v[50:51], v[212:213] op_sel_hi:[1,0]\n"
"v_pk_mul_f32 v[52:53], v[52:53], v[212:213] op_sel_hi:[1,0]\n"
"v_pk_mul_f32 v[54:55], v[54:55], v[212:213] op_sel_hi:[1,0]\n"
"v_cvt_pk_bf16_f32 v48, v48, v49\n"
"v_cvt_pk_bf16_f32 v49, v50, v51\n"
"v_mfma_f32_16x16x32_bf16 v[72:75], a[164:167], v[172:175], v[72:75]\n"
"v_exp_f32 v199, v199\n"
"v_mfma_f32_16x16x32_bf16 v[72:75], a[160:163], v[168:171], v[72:75]\n"
"v_cvt_pk_bf16_f32 v141, v198, v199\n"
"v_cvt_pk_bf16_f32 v50, v52, v53\n"
"v_mfma_f32_16x16x32_bf16 v[80:83], a[160:163], v[176:179], v[80:83]\n"
"v_exp_f32 v248, v248\n"
"v_mfma_f32_16x16x32_bf16 v[80:83], a[164:167], v[180:183], v[80:83]\n"
"ds_read_b64_tr_b16 a[186:187], v217 offset:54024\n"
"v_cvt_pk_bf16_f32 v51, v54, v55\n"
"v_mfma_f32_16x16x32_bf16 v[84:87], a[172:175], v[180:183], v[84:87]\n"
"v_exp_f32 v249, v249\n"
"v_mfma_f32_16x16x32_bf16 v[84:87], a[168:171], v[176:179], v[84:87]\n"
"ds_read_b64_tr_b16 a[188:189], v217 offset:58120\n"
"v_cvt_pk_bf16_f32 v148, v248, v249\n"
"v_mfma_f32_16x16x32_bf16 v[92:95], a[168:171], v[184:187], v[92:95]\n"
"v_exp_f32 v250, v250\n"
"v_mfma_f32_16x16x32_bf16 v[92:95], a[172:175], v[188:191], v[92:95]\n"
"ds_read_b64_tr_b16 a[190:191], v217 offset:62216\n"
"ds_write_b128 v225, v[48:51] offset:49152\n"
"v_pk_mul_f32 v[24:25], v[24:25], v[214:215] op_sel_hi:[1,0]\n"
"v_pk_mul_f32 v[26:27], v[26:27], v[214:215] op_sel_hi:[1,0]\n"
"v_pk_mul_f32 v[28:29], v[28:29], v[214:215] op_sel_hi:[1,0]\n"
"v_pk_mul_f32 v[30:31], v[30:31], v[214:215] op_sel_hi:[1,0]\n"
"v_cvt_pk_bf16_f32 v24, v24, v25\n"
"v_cvt_pk_bf16_f32 v25, v26, v27\n"
"v_mfma_f32_16x16x32_bf16 v[88:91], a[164:167], v[188:191], v[88:91]\n"
"v_exp_f32 v251, v251\n"
"v_mfma_f32_16x16x32_bf16 v[88:91], a[160:163], v[184:187], v[88:91]\n"
"s_waitcnt vmcnt(11) lgkmcnt(6)\n"
"buffer_store_dwordx4 a[216:219], v223, s[56:59], s99 offen offset:0 sc1 nt\n"
"v_cvt_pk_bf16_f32 v149, v250, v251\n"
"v_cvt_pk_bf16_f32 v26, v28, v29\n"
"v_cvt_pk_bf16_f32 v27, v30, v31\n"
"v_mfma_f32_16x16x32_bf16 v[192:195], a[144:147], a[0:3], -4.0\n"
"s_barrier\n"
"v_exp_f32 v252, v252\n"
"v_mfma_f32_16x16x32_bf16 v[192:195], a[148:151], a[4:7], v[192:195]\n"
"s_bfe_u32 s50, s38, 0x30008\n"
"ds_read_b128 a[128:131], v216 offset:16384\n"
"v_accvgpr_write_b32 a205, 0\n"
"v_mfma_f32_16x16x32_bf16 v[192:195], a[152:155], a[8:11], v[192:195]\n"
"s_lshl_b32 s50, s50, 14\n"
"v_exp_f32 v253, v253\n"
"v_mfma_f32_16x16x32_bf16 v[192:195], a[156:159], a[12:15], v[192:195]\n"
"s_add_u32 s39, s54, s50\n"
"ds_read_b128 a[132:135], v216 offset:16640\n"
"ds_read_b128 a[232:235], v226 offset:49152\n"
"v_cvt_pk_bf16_f32 v156, v252, v253\n"
"v_mfma_f32_16x16x32_bf16 v[196:199], a[156:159], a[28:31], -4.0\n"
"s_add_u32 s50, s44, s39\n"
"v_exp_f32 v254, v254\n"
"v_mfma_f32_16x16x32_bf16 v[196:199], a[152:155], a[24:27], v[196:199]\n"
"s_addc_u32 s51, s45, 0\n"
"ds_read_b128 a[136:139], v216 offset:16896\n"
"v_mul_f32 v56, v56, v214\n"
"v_mfma_f32_16x16x32_bf16 v[196:199], a[148:151], a[20:23], v[196:199]\n"
"s_add_u32 m0, s65, 32768\n"
"v_exp_f32 v255, v255\n"
"v_mfma_f32_16x16x32_bf16 v[196:199], a[144:147], a[16:19], v[196:199]\n"
"global_load_lds_dwordx4 v219, s[50:51] offset:0\n"
"v_cvt_pk_bf16_f32 v157, v254, v255\n"
"v_mfma_f32_16x16x32_bf16 v[248:251], a[144:147], a[32:35], -4.0\n"
"v_exp_f32 v192, v192\n"
"v_mfma_f32_16x16x32_bf16 v[248:251], a[148:151], a[36:39], v[248:251]\n"
"ds_read_b128 a[140:143], v216 offset:17152\n"
"v_mul_f32 v57, v57, v214\n"
"v_mfma_f32_16x16x32_bf16 v[248:251], a[152:155], a[40:43], v[248:251]\n"
"v_exp_f32 v193, v193\n"
"v_mfma_f32_16x16x32_bf16 v[248:251], a[156:159], a[44:47], v[248:251]\n"
"ds_read_b64_tr_b16 a[160:161], v217 offset:0\n"
"ds_write_b128 v224, v[24:27] offset:2048\n"
"v_cvt_pk_bf16_f32 v134, v192, v193\n"
"v_pk_mul_f32 v[58:59], v[58:59], v[214:215] op_sel_hi:[1,0]\n"
"v_pk_mul_f32 v[60:61], v[60:61], v[214:215] op_sel_hi:[1,0]\n"
"v_pk_mul_f32 v[62:63], v[62:63], v[214:215] op_sel_hi:[1,0]\n"
"v_cvt_pk_bf16_f32 v56, v56, v57\n"
"v_cvt_pk_bf16_f32 v57, v58, v59\n"
"v_mfma_f32_16x16x32_bf16 v[252:255], a[156:159], a[60:63], -4.0\n"
"v_exp_f32 v194, v194\n"
"v_mfma_f32_16x16x32_bf16 v[252:255], a[152:155], a[56:59], v[252:255]\n"
"ds_read_b64_tr_b16 a[162:163], v217 offset:4096\n"
"v_cvt_pk_bf16_f32 v58, v60, v61\n"
"v_mfma_f32_16x16x32_bf16 v[252:255], a[148:151], a[52:55], v[252:255]\n"
"v_exp_f32 v195, v195\n"
"v_mfma_f32_16x16x32_bf16 v[252:255], a[144:147], a[48:51], v[252:255]\n"
"ds_read_b128 a[236:239], v226 offset:50176\n"
"v_cvt_pk_bf16_f32 v135, v194, v195\n"
"v_mfma_f32_16x16x32_bf16 v[96:99], a[176:179], v[160:163], v[96:99]\n"
"v_exp_f32 v196, v196\n"
"v_mfma_f32_16x16x32_bf16 v[96:99], a[180:183], v[164:167], v[96:99]\n"
"s_waitcnt lgkmcnt(6)\n"
"ds_read_b64_tr_b16 a[164:165], v217 offset:8192\n"
"v_cvt_pk_bf16_f32 v59, v62, v63\n"
"v_mfma_f32_16x16x32_bf16 v[100:103], a[188:191], v[164:167], v[100:103]\n"
"v_exp_f32 v197, v197\n"
"v_mfma_f32_16x16x32_bf16 v[100:103], a[184:187], v[160:163], v[100:103]\n"
"ds_read_b64_tr_b16 a[166:167], v217 offset:12288\n"
"v_cvt_pk_bf16_f32 v142, v196, v197\n"
"v_mfma_f32_16x16x32_bf16 v[108:111], a[184:187], v[168:171], v[108:111]\n"
"v_exp_f32 v198, v198\n"
"v_mfma_f32_16x16x32_bf16 v[108:111], a[188:191], v[172:175], v[108:111]\n"
"ds_read_b64_tr_b16 a[168:169], v217 offset:8\n"
"v_mul_f32 v64, v64, v208\n"
"v_mfma_f32_16x16x32_bf16 v[104:107], a[180:183], v[172:175], v[104:107]\n"
"s_add_u32 m0, s65, 32768\n"
"v_exp_f32 v199, v199\n"
"v_mfma_f32_16x16x32_bf16 v[104:107], a[176:179], v[168:171], v[104:107]\n"
"global_load_lds_dwordx4 v219, s[50:51] offset:1024\n"
"v_cvt_pk_bf16_f32 v143, v198, v199\n"
"v_mfma_f32_16x16x32_bf16 v[112:115], a[176:179], v[176:179], v[112:115]\n"
"v_exp_f32 v248, v248\n"
"v_mfma_f32_16x16x32_bf16 v[112:115], a[180:183], v[180:183], v[112:115]\n"
"ds_read_b64_tr_b16 a[170:171], v217 offset:4104\n"
"v_mul_f32 v65, v65, v208\n"
"v_mfma_f32_16x16x32_bf16 v[116:119], a[188:191], v[180:183], v[116:119]\n"
"v_exp_f32 v249, v249\n"
"v_mfma_f32_16x16x32_bf16 v[116:119], a[184:187], v[176:179], v[116:119]\n"
"ds_read_b64_tr_b16 a[172:173], v217 offset:8200\n"
"v_cvt_pk_bf16_f32 v150, v248, v249\n"
"v_mfma_f32_16x16x32_bf16 v[124:127], a[184:187], v[184:187], v[124:127]\n"
"v_exp_f32 v250, v250\n"
"v_mfma_f32_16x16x32_bf16 v[124:127], a[188:191], v[188:191], v[124:127]\n"
"ds_read_b64_tr_b16 a[174:175], v217 offset:12296\n"
"ds_write_b128 v225, v[56:59] offset:2048\n"
"v_pk_mul_f32 v[66:67], v[66:67], v[208:209] op_sel_hi:[1,0]\n"
"v_pk_mul_f32 v[68:69], v[68:69], v[208:209] op_sel_hi:[1,0]\n"
"v_pk_mul_f32 v[70:71], v[70:71], v[208:209] op_sel_hi:[1,0]\n"
"v_cvt_pk_bf16_f32 v64, v64, v65\n"
"v_cvt_pk_bf16_f32 v65, v66, v67\n"
"v_mfma_f32_16x16x32_bf16 v[120:123], a[180:183], v[188:191], v[120:123]\n"
"s_add_u32 m0, s65, 32768\n"
"v_exp_f32 v251, v251\n"
"v_mfma_f32_16x16x32_bf16 v[120:123], a[176:179], v[184:187], v[120:123]\n"
"global_load_lds_dwordx4 v219, s[50:51] offset:2048\n"
"v_cvt_pk_bf16_f32 v151, v250, v251\n"
".Lattn_A1:\n"
"v_mfma_f32_16x16x32_bf16 v[192:195], a[128:131], a[0:3], -4.0\n"
"v_exp_f32 v252, v252\n"
"v_mfma_f32_16x16x32_bf16 v[192:195], a[132:135], a[4:7], v[192:195]\n"
"s_waitcnt lgkmcnt(5)\n"
"ds_read_b128 a[144:147], v216 offset:20480\n"
"v_cvt_pk_bf16_f32 v66, v68, v69\n"
"v_mfma_f32_16x16x32_bf16 v[192:195], a[136:139], a[8:11], v[192:195]\n"
"v_exp_f32 v253, v253\n"
"v_mfma_f32_16x16x32_bf16 v[192:195], a[140:143], a[12:15], v[192:195]\n"
"s_add_u32 s35, s55, s70\n"
"ds_read_b128 a[148:151], v216 offset:20736\n"
"ds_read_b128 a[240:243], v226 offset:2048\n"
"v_cvt_pk_bf16_f32 v158, v252, v253\n"
"v_mfma_f32_16x16x32_bf16 v[196:199], a[140:143], a[28:31], -4.0\n"
"s_add_u32 s52, s40, s35\n"
"v_exp_f32 v254, v254\n"
"v_mfma_f32_16x16x32_bf16 v[196:199], a[136:139], a[24:27], v[196:199]\n"
"s_addc_u32 s53, s41, 0\n"
"ds_read_b128 a[152:155], v216 offset:20992\n"
"v_cvt_pk_bf16_f32 v67, v70, v71\n"
"v_mfma_f32_16x16x32_bf16 v[196:199], a[132:135], a[20:23], v[196:199]\n"
"s_add_u32 m0, s66, 4096\n"
"v_exp_f32 v255, v255\n"
"v_mfma_f32_16x16x32_bf16 v[196:199], a[128:131], a[16:19], v[196:199]\n"
"global_load_lds_dwordx4 v219, s[52:53] offset:0\n"
"v_cvt_pk_bf16_f32 v159, v254, v255\n"
"v_mfma_f32_16x16x32_bf16 v[248:251], a[128:131], a[32:35], -4.0\n"
"v_exp_f32 v192, v192\n"
"v_mfma_f32_16x16x32_bf16 v[248:251], a[132:135], a[36:39], v[248:251]\n"
"ds_read_b128 a[156:159], v216 offset:21248\n"
"v_mul_f32 v96, v96, v208\n"
"v_mfma_f32_16x16x32_bf16 v[248:251], a[136:139], a[40:43], v[248:251]\n"
"v_exp_f32 v193, v193\n"
"v_mfma_f32_16x16x32_bf16 v[248:251], a[140:143], a[44:47], v[248:251]\n"
"ds_read_b64_tr_b16 a[176:177], v217 offset:256\n"
"ds_read_b128 a[244:247], v226 offset:3072\n"
"v_cvt_pk_bf16_f32 v160, v192, v193\n"
"v_mfma_f32_16x16x32_bf16 v[252:255], a[140:143], a[60:63], -4.0\n"
"v_exp_f32 v194, v194\n"
"v_mfma_f32_16x16x32_bf16 v[252:255], a[136:139], a[56:59], v[252:255]\n"
"ds_read_b64_tr_b16 a[178:179], v217 offset:4352\n"
"v_mul_f32 v97, v97, v208\n"
"v_mfma_f32_16x16x32_bf16 v[252:255], a[132:135], a[52:55], v[252:255]\n"
"s_add_u32 m0, s65, 32768\n"
"v_exp_f32 v195, v195\n"
"v_mfma_f32_16x16x32_bf16 v[252:255], a[128:131], a[48:51], v[252:255]\n"
"global_load_lds_dwordx4 v219, s[50:51] offset:3072\n"
"v_cvt_pk_bf16_f32 v161, v194, v195\n"
"v_smfmac_f32_16x16x64_bf16 a[192:195], a[208:211], v[128:135], v215\n"
"buffer_store_dwordx4 a[220:223], v223, s[56:59], s99 offen offset:2048 sc1 nt\n"
"v_mul_f32 v98, v98, v208\n"
"v_mul_f32 v99, v99, v208\n"
"v_smfmac_f32_16x16x64_bf16 a[196:199], a[208:211], v[136:143], v215\n"
"ds_read_b64_tr_b16 a[180:181], v217 offset:8448\n"
"v_smfmac_f32_16x16x64_bf16 a[200:203], a[208:211], v[144:151], v215\n"
"v_mul_f32 v100, v100, v208\n"
"v_smfmac_f32_16x16x64_bf16 a[204:207], a[208:211], v[152:159], v215\n"
"ds_read_b64_tr_b16 a[182:183], v217 offset:12544\n"
"v_mfma_f32_16x16x32_bf16 v[0:3], a[160:163], v[128:131], 0\n"
"v_exp_f32 v196, v196\n"
"v_mfma_f32_16x16x32_bf16 v[0:3], a[164:167], v[132:135], v[0:3]\n"
"s_waitcnt lgkmcnt(6)\n"
"ds_read_b64_tr_b16 a[184:185], v217 offset:264\n"
"ds_write_b128 v224, v[64:67] offset:0\n"
"v_mul_f32 v101, v101, v208\n"
"v_pk_mul_f32 v[102:103], v[102:103], v[208:209] op_sel_hi:[1,0]\n"
"v_cvt_pk_bf16_f32 v96, v96, v97\n"
"v_cvt_pk_bf16_f32 v97, v98, v99\n"
"v_mfma_f32_16x16x32_bf16 v[4:7], a[172:175], v[132:135], 0\n"
"v_exp_f32 v197, v197\n"
"v_mfma_f32_16x16x32_bf16 v[4:7], a[168:171], v[128:131], v[4:7]\n"
"v_cvt_pk_bf16_f32 v168, v196, v197\n"
"v_cvt_pk_bf16_f32 v98, v100, v101\n"
"v_mfma_f32_16x16x32_bf16 v[12:15], a[168:171], v[136:139], 0\n"
"v_exp_f32 v198, v198\n"
"v_mfma_f32_16x16x32_bf16 v[12:15], a[172:175], v[140:143], v[12:15]\n"
"s_bfe_u32 s48, s38, 0x3000c\n"
"ds_read_b64_tr_b16 a[186:187], v217 offset:4360\n"
"v_cvt_pk_bf16_f32 v99, v102, v103\n"
"v_mfma_f32_16x16x32_bf16 v[8:11], a[164:167], v[140:143], 0\n"
"s_lshl_b32 s48, s48, 14\n"
"v_exp_f32 v199, v199\n"
"v_mfma_f32_16x16x32_bf16 v[8:11], a[160:163], v[136:139], v[8:11]\n"
"s_add_u32 s36, s79, s48\n"
"ds_read_b64_tr_b16 a[188:189], v217 offset:8456\n"
"v_cvt_pk_bf16_f32 v169, v198, v199\n"
"v_mfma_f32_16x16x32_bf16 v[16:19], a[160:163], v[144:147], 0\n"
"s_add_u32 s48, s42, s36\n"
"v_exp_f32 v248, v248\n"
"v_mfma_f32_16x16x32_bf16 v[16:19], a[164:167], v[148:151], v[16:19]\n"
"s_addc_u32 s49, s43, 0\n"
"ds_read_b64_tr_b16 a[190:191], v217 offset:12552\n"
"v_mul_f32 v72, v72, v210\n"
"v_mfma_f32_16x16x32_bf16 v[20:23], a[172:175], v[148:151], 0\n"
"s_add_u32 m0, s64, 49152\n"
"v_exp_f32 v249, v249\n"
"v_mfma_f32_16x16x32_bf16 v[20:23], a[168:171], v[144:147], v[20:23]\n"
"global_load_lds_dwordx4 v219, s[48:49] offset:0\n"
"v_cvt_pk_bf16_f32 v176, v248, v249\n"
"v_mfma_f32_16x16x32_bf16 v[28:31], a[168:171], v[152:155], 0\n"
"v_exp_f32 v250, v250\n"
"v_mfma_f32_16x16x32_bf16 v[28:31], a[172:175], v[156:159], v[28:31]\n"
"ds_write_b128 v225, v[96:99] offset:0\n"
"v_mul_f32 v73, v73, v210\n"
"v_pk_mul_f32 v[74:75], v[74:75], v[210:211] op_sel_hi:[1,0]\n"
"v_pk_mul_f32 v[76:77], v[76:77], v[210:211] op_sel_hi:[1,0]\n"
"v_pk_mul_f32 v[78:79], v[78:79], v[210:211] op_sel_hi:[1,0]\n"
"v_cvt_pk_bf16_f32 v72, v72, v73\n"
"v_cvt_pk_bf16_f32 v73, v74, v75\n"
"v_mfma_f32_16x16x32_bf16 v[24:27], a[164:167], v[156:159], 0\n"
"v_exp_f32 v251, v251\n"
"v_mfma_f32_16x16x32_bf16 v[24:27], a[160:163], v[152:155], v[24:27]\n"
"v_cvt_pk_bf16_f32 v177, v250, v251\n"
"v_cvt_pk_bf16_f32 v74, v76, v77\n"
"v_mfma_f32_16x16x32_bf16 v[192:195], a[144:147], a[0:3], -4.0\n"
"v_exp_f32 v252, v252\n"
"v_mfma_f32_16x16x32_bf16 v[192:195], a[148:151], a[4:7], v[192:195]\n"
"ds_read_b128 a[128:131], v216 offset:24576\n"
"v_cvt_pk_bf16_f32 v75, v78, v79\n"
"v_mfma_f32_16x16x32_bf16 v[192:195], a[152:155], a[8:11], v[192:195]\n"
"s_waitcnt lgkmcnt(5)\n"
"v_exp_f32 v253, v253\n"
"v_mfma_f32_16x16x32_bf16 v[192:195], a[156:159], a[12:15], v[192:195]\n"
"ds_read_b128 a[132:135], v216 offset:24832\n"
"ds_read_b128 a[248:251], v226 offset:0\n"
"v_cvt_pk_bf16_f32 v184, v252, v253\n"
"v_mfma_f32_16x16x32_bf16 v[196:199], a[156:159], a[28:31], -4.0\n"
"v_exp_f32 v254, v254\n"
"v_mfma_f32_16x16x32_bf16 v[196:199], a[152:155], a[24:27], v[196:199]\n"
"ds_read_b128 a[136:139], v216 offset:25088\n"
"v_mul_f32 v104, v104, v210\n"
"v_mfma_f32_16x16x32_bf16 v[196:199], a[148:151], a[20:23], v[196:199]\n"
"s_add_u32 m0, s64, 49152\n"
"v_exp_f32 v255, v255\n"
"v_mfma_f32_16x16x32_bf16 v[196:199], a[144:147], a[16:19], v[196:199]\n"
"global_load_lds_dwordx4 v219, s[48:49] offset:1024\n"
"v_cvt_pk_bf16_f32 v185, v254, v255\n"
"v_mfma_f32_16x16x32_bf16 v[248:251], a[144:147], a[32:35], -4.0\n"
"v_exp_f32 v192, v192\n"
"v_mfma_f32_16x16x32_bf16 v[248:251], a[148:151], a[36:39], v[248:251]\n"
"ds_read_b128 a[140:143], v216 offset:25344\n"
"v_mul_f32 v105, v105, v210\n"
"v_mfma_f32_16x16x32_bf16 v[248:251], a[152:155], a[40:43], v[248:251]\n"
"v_exp_f32 v193, v193\n"
"v_mfma_f32_16x16x32_bf16 v[248:251], a[156:159], a[44:47], v[248:251]\n"
"ds_read_b64_tr_b16 a[160:161], v217 offset:512\n"
"ds_write_b128 v224, v[72:75] offset:2048\n"
"v_cvt_pk_bf16_f32 v162, v192, v193\n"
"v_pk_mul_f32 v[106:107], v[106:107], v[210:211] op_sel_hi:[1,0]\n"
"v_pk_mul_f32 v[108:109], v[108:109], v[210:211] op_sel_hi:[1,0]\n"
"v_pk_mul_f32 v[110:111], v[110:111], v[210:211] op_sel_hi:[1,0]\n"
"v_cvt_pk_bf16_f32 v104, v104, v105\n"
"v_cvt_pk_bf16_f32 v105, v106, v107\n"
"v_mfma_f32_16x16x32_bf16 v[252:255], a[156:159], a[60:63], -4.0\n"
"v_exp_f32 v194, v194\n"
"v_mfma_f32_16x16x32_bf16 v[252:255], a[152:155], a[56:59], v[252:255]\n"
"ds_read_b64_tr_b16 a[162:163], v217 offset:4608\n"
"v_cvt_pk_bf16_f32 v106, v108, v109\n"
"v_mfma_f32_16x16x32_bf16 v[252:255], a[148:151], a[52:55], v[252:255]\n"
"s_add_u32 m0, s64, 49152\n"
"v_exp_f32 v195, v195\n"
"v_mfma_f32_16x16x32_bf16 v[252:255], a[144:147], a[48:51], v[252:255]\n"
"global_load_lds_dwordx4 v220, s[48:49] offset:2048\n"
"v_cvt_pk_bf16_f32 v163, v194, v195\n"
"v_mfma_f32_16x16x32_bf16 v[32:35], a[176:179], v[128:131], 0\n"
"v_exp_f32 v196, v196\n"
"v_mfma_f32_16x16x32_bf16 v[32:35], a[180:183], v[132:135], v[32:35]\n"
"s_waitcnt lgkmcnt(5)\n"
"ds_read_b64_tr_b16 a[164:165], v217 offset:8704\n"
"ds_read_b128 a[252:255], v226 offset:1024\n"
"v_mfma_f32_16x16x32_bf16 v[36:39], a[188:191], v[132:135], 0\n"
"v_exp_f32 v197, v197\n"
"v_mfma_f32_16x16x32_bf16 v[36:39], a[184:187], v[128:131], v[36:39]\n"
"ds_read_b64_tr_b16 a[166:167], v217 offset:12800\n"
"buffer_store_dwordx4 a[224:227], v223, s[56:59], s70 offen offset:0 sc1 nt\n"
"v_cvt_pk_bf16_f32 v170, v196, v197\n"
"v_cvt_pk_bf16_f32 v107, v110, v111\n"
"v_mfma_f32_16x16x32_bf16 v[44:47], a[184:187], v[136:139], 0\n"
"v_exp_f32 v198, v198\n"
"v_mfma_f32_16x16x32_bf16 v[44:47], a[188:191], v[140:143], v[44:47]\n"
"ds_read_b64_tr_b16 a[168:169], v217 offset:520\n"
"v_mul_f32 v80, v80, v212\n"
"v_mfma_f32_16x16x32_bf16 v[40:43], a[180:183], v[140:143], 0\n"
"s_add_u32 m0, s66, 4096\n"
"v_exp_f32 v199, v199\n"
"v_mfma_f32_16x16x32_bf16 v[40:43], a[176:179], v[136:139], v[40:43]\n"
"global_load_lds_dwordx4 v219, s[52:53] offset:1024\n"
"v_cvt_pk_bf16_f32 v171, v198, v199\n"
"v_mfma_f32_16x16x32_bf16 v[48:51], a[176:179], v[144:147], 0\n"
"v_exp_f32 v248, v248\n"
"v_mfma_f32_16x16x32_bf16 v[48:51], a[180:183], v[148:151], v[48:51]\n"
"ds_read_b64_tr_b16 a[170:171], v217 offset:4616\n"
"v_mul_f32 v81, v81, v212\n"
"v_mfma_f32_16x16x32_bf16 v[52:55], a[188:191], v[148:151], 0\n"
"v_exp_f32 v249, v249\n"
"v_mfma_f32_16x16x32_bf16 v[52:55], a[184:187], v[144:147], v[52:55]\n"
"ds_read_b64_tr_b16 a[172:173], v217 offset:8712\n"
"v_cvt_pk_bf16_f32 v178, v248, v249\n"
"v_mfma_f32_16x16x32_bf16 v[60:63], a[184:187], v[152:155], 0\n"
"v_exp_f32 v250, v250\n"
"v_mfma_f32_16x16x32_bf16 v[60:63], a[188:191], v[156:159], v[60:63]\n"
"ds_read_b64_tr_b16 a[174:175], v217 offset:12808\n"
"ds_write_b128 v225, v[104:107] offset:2048\n"
"v_pk_mul_f32 v[82:83], v[82:83], v[212:213] op_sel_hi:[1,0]\n"
"v_pk_mul_f32 v[84:85], v[84:85], v[212:213] op_sel_hi:[1,0]\n"
"v_pk_mul_f32 v[86:87], v[86:87], v[212:213] op_sel_hi:[1,0]\n"
"v_cvt_pk_bf16_f32 v80, v80, v81\n"
"v_cvt_pk_bf16_f32 v81, v82, v83\n"
"v_mfma_f32_16x16x32_bf16 v[56:59], a[180:183], v[156:159], 0\n"
"s_add_u32 m0, s64, 49152\n"
"v_exp_f32 v251, v251\n"
"v_mfma_f32_16x16x32_bf16 v[56:59], a[176:179], v[152:155], v[56:59]\n"
"global_load_lds_dwordx4 v220, s[48:49] offset:3072\n"
"v_cvt_pk_bf16_f32 v179, v250, v251\n"
"v_mfma_f32_16x16x32_bf16 v[192:195], a[128:131], a[0:3], -4.0\n"
"v_exp_f32 v252, v252\n"
"v_mfma_f32_16x16x32_bf16 v[192:195], a[132:135], a[4:7], v[192:195]\n"
"s_waitcnt lgkmcnt(5)\n"
"ds_read_b128 a[144:147], v216 offset:28672\n"
"v_cvt_pk_bf16_f32 v82, v84, v85\n"
"v_mfma_f32_16x16x32_bf16 v[192:195], a[136:139], a[8:11], v[192:195]\n"
"v_exp_f32 v253, v253\n"
"v_mfma_f32_16x16x32_bf16 v[192:195], a[140:143], a[12:15], v[192:195]\n"
"ds_read_b128 a[148:151], v216 offset:28928\n"
"v_cvt_pk_bf16_f32 v186, v252, v253\n"
"v_mfma_f32_16x16x32_bf16 v[196:199], a[140:143], a[28:31], -4.0\n"
"v_exp_f32 v254, v254\n"
"v_mfma_f32_16x16x32_bf16 v[196:199], a[136:139], a[24:27], v[196:199]\n"
"ds_read_b128 a[152:155], v216 offset:29184\n"
"v_cvt_pk_bf16_f32 v83, v86, v87\n"
"v_mfma_f32_16x16x32_bf16 v[196:199], a[132:135], a[20:23], v[196:199]\n"
"v_exp_f32 v255, v255\n"
"v_mfma_f32_16x16x32_bf16 v[196:199], a[128:131], a[16:19], v[196:199]\n"
"ds_read_b128 v[236:239], v226 offset:2048\n"
"v_cvt_pk_bf16_f32 v187, v254, v255\n"
"v_mfma_f32_16x16x32_bf16 v[248:251], a[128:131], a[32:35], -4.0\n"
"v_exp_f32 v192, v192\n"
"v_mfma_f32_16x16x32_bf16 v[248:251], a[132:135], a[36:39], v[248:251]\n"
"ds_read_b128 a[156:159], v216 offset:29440\n"
"v_mul_f32 v88, v88, v214\n"
"v_mfma_f32_16x16x32_bf16 v[248:251], a[136:139], a[40:43], v[248:251]\n"
"v_exp_f32 v193, v193\n"
"v_mfma_f32_16x16x32_bf16 v[248:251], a[140:143], a[44:47], v[248:251]\n"
"ds_read_b64_tr_b16 a[176:177], v217 offset:768\n"
"ds_write_b128 v224, v[80:83] offset:0\n"
"v_cvt_pk_bf16_f32 v164, v192, v193\n"
"v_mul_f32 v89, v89, v214\n"
"v_pk_mul_f32 v[90:91], v[90:91], v[214:215] op_sel_hi:[1,0]\n"
"v_pk_mul_f32 v[92:93], v[92:93], v[214:215] op_sel_hi:[1,0]\n"
"v_pk_mul_f32 v[94:95], v[94:95], v[214:215] op_sel_hi:[1,0]\n"
"v_cvt_pk_bf16_f32 v88, v88, v89\n"
"v_cvt_pk_bf16_f32 v89, v90, v91\n"
"v_mfma_f32_16x16x32_bf16 v[252:255], a[140:143], a[60:63], -4.0\n"
"v_exp_f32 v194, v194\n"
"v_mfma_f32_16x16x32_bf16 v[252:255], a[136:139], a[56:59], v[252:255]\n"
"ds_read_b64_tr_b16 a[178:179], v217 offset:4864\n"
"v_cvt_pk_bf16_f32 v90, v92, v93\n"
"v_mfma_f32_16x16x32_bf16 v[252:255], a[132:135], a[52:55], v[252:255]\n"
"v_exp_f32 v195, v195\n"
"v_mfma_f32_16x16x32_bf16 v[252:255], a[128:131], a[48:51], v[252:255]\n"
"ds_read_b128 v[240:243], v226 offset:3072\n"
"v_cvt_pk_bf16_f32 v165, v194, v195\n"
"v_mfma_f32_16x16x32_bf16 v[64:67], a[160:163], v[128:131], 0\n"
"v_exp_f32 v196, v196\n"
"v_mfma_f32_16x16x32_bf16 v[64:67], a[164:167], v[132:135], v[64:67]\n"
"s_waitcnt lgkmcnt(7)\n"
"ds_read_b64_tr_b16 a[180:181], v217 offset:8960\n"
"v_cvt_pk_bf16_f32 v91, v94, v95\n"
"v_mfma_f32_16x16x32_bf16 v[68:71], a[172:175], v[132:135], 0\n"
"v_exp_f32 v197, v197\n"
"v_mfma_f32_16x16x32_bf16 v[68:71], a[168:171], v[128:131], v[68:71]\n"
"ds_read_b64_tr_b16 a[182:183], v217 offset:13056\n"
"v_cvt_pk_bf16_f32 v172, v196, v197\n"
"v_mfma_f32_16x16x32_bf16 v[76:79], a[168:171], v[136:139], 0\n"
"v_exp_f32 v198, v198\n"
"v_mfma_f32_16x16x32_bf16 v[76:79], a[172:175], v[140:143], v[76:79]\n"
"ds_read_b64_tr_b16 a[184:185], v217 offset:776\n"
"ds_write_b128 v224, v[88:91] offset:2048\n"
"v_pk_mul_f32 v[112:113], v[112:113], v[212:213] op_sel_hi:[1,0]\n"
"v_pk_mul_f32 v[114:115], v[114:115], v[212:213] op_sel_hi:[1,0]\n"
"v_pk_mul_f32 v[116:117], v[116:117], v[212:213] op_sel_hi:[1,0]\n"
"v_pk_mul_f32 v[118:119], v[118:119], v[212:213] op_sel_hi:[1,0]\n"
"v_cvt_pk_bf16_f32 v112, v112, v113\n"
"v_cvt_pk_bf16_f32 v113, v114, v115\n"
"v_mfma_f32_16x16x32_bf16 v[72:75], a[164:167], v[140:143], 0\n"
"s_add_u32 m0, s66, 4096\n"
"v_exp_f32 v199, v199\n"
"v_mfma_f32_16x16x32_bf16 v[72:75], a[160:163], v[136:139], v[72:75]\n"
"global_load_lds_dwordx4 v220, s[52:53] offset:2048\n"
"v_cvt_pk_bf16_f32 v173, v198, v199\n"
"v_mfma_f32_16x16x32_bf16 v[80:83], a[160:163], v[144:147], 0\n"
".Lattn_pad6:\n"
"s_cbranch_vccnz .Lattn_ret\n"
"v_exp_f32 v248, v248\n"
"v_mfma_f32_16x16x32_bf16 v[80:83], a[164:167], v[148:151], v[80:83]\n"
"ds_read_b64_tr_b16 a[186:187], v217 offset:4872\n"
"buffer_store_dwordx4 a[228:231], v223, s[56:59], s70 offen offset:2048 sc1 nt\n"
"v_cvt_pk_bf16_f32 v114, v116, v117\n"
"v_cvt_pk_bf16_f32 v115, v118, v119\n"
"v_mfma_f32_16x16x32_bf16 v[84:87], a[172:175], v[148:151], 0\n"
"v_exp_f32 v249, v249\n"
"v_mfma_f32_16x16x32_bf16 v[84:87], a[168:171], v[144:147], v[84:87]\n"
"ds_read_b64_tr_b16 a[188:189], v217 offset:8968\n"
"v_cvt_pk_bf16_f32 v180, v248, v249\n"
"v_mfma_f32_16x16x32_bf16 v[92:95], a[168:171], v[152:155], 0\n"
"v_exp_f32 v250, v250\n"
"v_mfma_f32_16x16x32_bf16 v[92:95], a[172:175], v[156:159], v[92:95]\n"
"ds_read_b64_tr_b16 a[190:191], v217 offset:13064\n"
"v_mul_f32 v120, v120, v214\n"
"v_mfma_f32_16x16x32_bf16 v[88:91], a[164:167], v[156:159], 0\n"
"v_exp_f32 v251, v251\n"
"v_mfma_f32_16x16x32_bf16 v[88:91], a[160:163], v[152:155], v[88:91]\n"
"s_waitcnt vmcnt(15) lgkmcnt(6)\n"
"v_cvt_pk_bf16_f32 v181, v250, v251\n"
"v_mul_f32 v121, v121, v214\n"
"v_mfma_f32_16x16x32_bf16 v[192:195], a[144:147], a[0:3], -4.0\n"
"s_barrier\n"
"v_exp_f32 v252, v252\n"
"v_mfma_f32_16x16x32_bf16 v[192:195], a[148:151], a[4:7], v[192:195]\n"
"s_bfe_u32 s50, s38, 0x3000c\n"
"ds_read_b128 a[128:131], v216 offset:32768\n"
"v_mul_f32 v122, v122, v214\n"
"v_mfma_f32_16x16x32_bf16 v[192:195], a[152:155], a[8:11], v[192:195]\n"
"s_lshl_b32 s50, s50, 14\n"
"v_exp_f32 v253, v253\n"
"v_mfma_f32_16x16x32_bf16 v[192:195], a[156:159], a[12:15], v[192:195]\n"
"s_add_u32 s39, s54, s50\n"
"ds_read_b128 a[132:135], v216 offset:33024\n"
"v_cvt_pk_bf16_f32 v188, v252, v253\n"
"v_mfma_f32_16x16x32_bf16 v[196:199], a[156:159], a[28:31], -4.0\n"
"s_add_u32 s50, s44, s39\n"
"v_exp_f32 v254, v254\n"
"v_mfma_f32_16x16x32_bf16 v[196:199], a[152:155], a[24:27], v[196:199]\n"
"s_addc_u32 s51, s45, 0\n"
"ds_read_b128 a[136:139], v216 offset:33280\n"
"v_mul_f32 v123, v123, v214\n"
"v_mfma_f32_16x16x32_bf16 v[196:199], a[148:151], a[20:23], v[196:199]\n"
"s_add_u32 m0, s65, 49152\n"
"v_exp_f32 v255, v255\n"
"v_mfma_f32_16x16x32_bf16 v[196:199], a[144:147], a[16:19], v[196:199]\n"
"global_load_lds_dwordx4 v219, s[50:51] offset:0\n"
"v_cvt_pk_bf16_f32 v189, v254, v255\n"
"v_mfma_f32_16x16x32_bf16 v[248:251], a[144:147], a[32:35], -4.0\n"
"v_exp_f32 v192, v192\n"
"v_mfma_f32_16x16x32_bf16 v[248:251], a[148:151], a[36:39], v[248:251]\n"
"ds_read_b128 a[140:143], v216 offset:33536\n"
"v_mul_f32 v124, v124, v214\n"
"v_mfma_f32_16x16x32_bf16 v[248:251], a[152:155], a[40:43], v[248:251]\n"
"v_exp_f32 v193, v193\n"
"v_mfma_f32_16x16x32_bf16 v[248:251], a[156:159], a[44:47], v[248:251]\n"
"ds_read_b64_tr_b16 a[160:161], v217 offset:16384\n"
"ds_write_b128 v225, v[112:115] offset:0\n"
"v_cvt_pk_bf16_f32 v166, v192, v193\n"
"v_mul_f32 v125, v125, v214\n"
"v_pk_mul_f32 v[126:127], v[126:127], v[214:215] op_sel_hi:[1,0]\n"
"v_cvt_pk_bf16_f32 v120, v120, v121\n"
"v_cvt_pk_bf16_f32 v121, v122, v123\n"
"v_mfma_f32_16x16x32_bf16 v[252:255], a[156:159], a[60:63], -4.0\n"
"v_exp_f32 v194, v194\n"
"v_mfma_f32_16x16x32_bf16 v[252:255], a[152:155], a[56:59], v[252:255]\n"
"ds_read_b64_tr_b16 a[162:163], v217 offset:20480\n"
"v_cvt_pk_bf16_f32 v122, v124, v125\n"
"v_mfma_f32_16x16x32_bf16 v[252:255], a[148:151], a[52:55], v[252:255]\n"
"v_exp_f32 v195, v195\n"
"v_mfma_f32_16x16x32_bf16 v[252:255], a[144:147], a[48:51], v[252:255]\n"
"ds_read_b128 a[216:219], v226 offset:0\n"
"v_cvt_pk_bf16_f32 v167, v194, v195\n"
"v_mfma_f32_16x16x32_bf16 v[96:99], a[176:179], v[128:131], 0\n"
"s_waitcnt lgkmcnt(7)\n"
"v_exp_f32 v196, v196\n"
"v_mfma_f32_16x16x32_bf16 v[96:99], a[180:183], v[132:135], v[96:99]\n"
"ds_read_b64_tr_b16 a[164:165], v217 offset:24576\n"
"ds_read_b128 a[220:223], v226 offset:1024\n"
"v_mfma_f32_16x16x32_bf16 v[100:103], a[188:191], v[132:135], 0\n"
"v_exp_f32 v197, v197\n"
"v_mfma_f32_16x16x32_bf16 v[100:103], a[184:187], v[128:131], v[100:103]\n"
"ds_read_b64_tr_b16 a[166:167], v217 offset:28672\n"
"v_cvt_pk_bf16_f32 v174, v196, v197\n"
"v_cvt_pk_bf16_f32 v123, v126, v127\n"
"v_mfma_f32_16x16x32_bf16 v[108:111], a[184:187], v[136:139], 0\n"
"v_exp_f32 v198, v198\n"
"v_mfma_f32_16x16x32_bf16 v[108:111], a[188:191], v[140:143], v[108:111]\n"
"ds_read_b64_tr_b16 a[168:169], v217 offset:16392\n"
"ds_write_b128 v225, v[120:123] offset:2048\n"
"v_mfma_f32_16x16x32_bf16 v[104:107], a[180:183], v[140:143], 0\n"
"s_add_u32 m0, s65, 49152\n"
"v_exp_f32 v199, v199\n"
"v_mfma_f32_16x16x32_bf16 v[104:107], a[176:179], v[136:139], v[104:107]\n"
"global_load_lds_dwordx4 v219, s[50:51] offset:1024\n"
"v_cvt_pk_bf16_f32 v175, v198, v199\n"
"v_mfma_f32_16x16x32_bf16 v[112:115], a[176:179], v[144:147], 0\n"
"v_exp_f32 v248, v248\n"
"v_mfma_f32_16x16x32_bf16 v[112:115], a[180:183], v[148:151], v[112:115]\n"
"ds_read_b64_tr_b16 a[170:171], v217 offset:20488\n"
"ds_read_b128 a[224:227], v226 offset:2048\n"
"v_mfma_f32_16x16x32_bf16 v[116:119], a[188:191], v[148:151], 0\n"
"v_exp_f32 v249, v249\n"
"v_mfma_f32_16x16x32_bf16 v[116:119], a[184:187], v[144:147], v[116:119]\n"
"ds_read_b64_tr_b16 a[172:173], v217 offset:24584\n"
"ds_read_b128 a[228:231], v226 offset:3072\n"
"v_cvt_pk_bf16_f32 v182, v248, v249\n"
"v_mfma_f32_16x16x32_bf16 v[124:127], a[184:187], v[152:155], 0\n"
"v_exp_f32 v250, v250\n"
"v_mfma_f32_16x16x32_bf16 v[124:127], a[188:191], v[156:159], v[124:127]\n"
"ds_read_b64_tr_b16 a[174:175], v217 offset:28680\n"
"v_mfma_f32_16x16x32_bf16 v[120:123], a[180:183], v[156:159], 0\n"
"s_add_u32 m0, s65, 49152\n"
"v_exp_f32 v251, v251\n"
"v_mfma_f32_16x16x32_bf16 v[120:123], a[176:179], v[152:155], v[120:123]\n"
"global_load_lds_dwordx4 v219, s[50:51] offset:2048\n"
"v_cvt_pk_bf16_f32 v183, v250, v251\n"
".Lattn_A2:\n"
"v_mfma_f32_16x16x32_bf16 v[192:195], a[128:131], a[0:3], -4.0\n"
"s_waitcnt lgkmcnt(9)\n"
"v_exp_f32 v252, v252\n"
"v_mfma_f32_16x16x32_bf16 v[192:195], a[132:135], a[4:7], v[192:195]\n"
"ds_read_b128 a[144:147], v216 offset:36864\n"
"v_mfma_f32_16x16x32_bf16 v[192:195], a[136:139], a[8:11], v[192:195]\n"
"v_exp_f32 v253, v253\n"
"v_mfma_f32_16x16x32_bf16 v[192:195], a[140:143], a[12:15], v[192:195]\n"
"s_waitcnt vmcnt(14)\n"
"ds_read_b128 a[148:151], v216 offset:37120\n"
"ds_read_b128 v[200:203], v218 offset:0\n"
"v_cvt_pk_bf16_f32 v190, v252, v253\n"
"v_mfma_f32_16x16x32_bf16 v[196:199], a[140:143], a[28:31], -4.0\n"
"v_exp_f32 v254, v254\n"
"v_mfma_f32_16x16x32_bf16 v[196:199], a[136:139], a[24:27], v[196:199]\n"
"ds_read_b128 a[152:155], v216 offset:37376\n"
"v_mfma_f32_16x16x32_bf16 v[196:199], a[132:135], a[20:23], v[196:199]\n"
"s_add_u32 m0, s66, 4096\n"
"v_exp_f32 v255, v255\n"
"v_mfma_f32_16x16x32_bf16 v[196:199], a[128:131], a[16:19], v[196:199]\n"
"global_load_lds_dwordx4 v220, s[52:53] offset:3072\n"
"v_cvt_pk_bf16_f32 v191, v254, v255\n"
"v_mfma_f32_16x16x32_bf16 v[248:251], a[128:131], a[32:35], -4.0\n"
"v_exp_f32 v192, v192\n"
"v_mfma_f32_16x16x32_bf16 v[248:251], a[132:135], a[36:39], v[248:251]\n"
"ds_read_b128 a[156:159], v216 offset:37632\n"
"v_mfma_f32_16x16x32_bf16 v[248:251], a[136:139], a[40:43], v[248:251]\n"
"v_exp_f32 v193, v193\n"
"v_mfma_f32_16x16x32_bf16 v[248:251], a[140:143], a[44:47], v[248:251]\n"
"ds_read_b64_tr_b16 a[176:177], v217 offset:16640\n"
"v_cvt_pk_bf16_f32 v128, v192, v193\n"
"v_mfma_f32_16x16x32_bf16 v[252:255], a[140:143], a[60:63], -4.0\n"
"v_exp_f32 v194, v194\n"
"v_mfma_f32_16x16x32_bf16 v[252:255], a[136:139], a[56:59], v[252:255]\n"
"ds_read_b64_tr_b16 a[178:179], v217 offset:20736\n"
"v_mfma_f32_16x16x32_bf16 v[252:255], a[132:135], a[52:55], v[252:255]\n"
"s_add_u32 m0, s65, 49152\n"
"v_exp_f32 v195, v195\n"
"v_mfma_f32_16x16x32_bf16 v[252:255], a[128:131], a[48:51], v[252:255]\n"
"s_waitcnt lgkmcnt(4)\n"
"global_load_lds_dwordx4 v219, s[50:51] offset:3072\n"
"v_cvt_pk_bf16_f32 v129, v194, v195\n"
"v_lshlrev_b32 v228, 16, v200\n"
"v_and_b32 v229, s67, v200\n"
"v_smfmac_f32_16x16x64_bf16 a[192:195], a[208:211], v[160:167], v215\n"
"ds_read_b128 v[204:207], v218 offset:256\n"
"v_smfmac_f32_16x16x64_bf16 a[196:199], a[208:211], v[168:175], v215\n"
"ds_read_b64_tr_b16 a[180:181], v217 offset:24832\n"
"v_smfmac_f32_16x16x64_bf16 a[200:203], a[208:211], v[176:183], v215\n"
"ds_read_b128 v[244:247], v218 offset:512\n"
"v_smfmac_f32_16x16x64_bf16 a[204:207], a[208:211], v[184:191], v215\n"
"ds_read_b64_tr_b16 a[182:183], v217 offset:28928\n"
"v_mfma_f32_16x16x32_bf16 v[0:3], a[160:163], v[160:163], v[0:3]\n"
"v_exp_f32 v196, v196\n"
"v_mfma_f32_16x16x32_bf16 v[0:3], a[164:167], v[164:167], v[0:3]\n"
"ds_read_b64_tr_b16 a[184:185], v217 offset:16648\n"
"v_lshlrev_b32 v230, 16, v201\n"
"v_mfma_f32_16x16x32_bf16 v[4:7], a[172:175], v[164:167], v[4:7]\n"
"v_exp_f32 v197, v197\n"
"v_mfma_f32_16x16x32_bf16 v[4:7], a[168:171], v[160:163], v[4:7]\n"
"v_cvt_pk_bf16_f32 v136, v196, v197\n"
"v_and_b32 v231, s67, v201\n"
"v_mfma_f32_16x16x32_bf16 v[12:15], a[168:171], v[168:171], v[12:15]\n"
"v_exp_f32 v198, v198\n"
"v_mfma_f32_16x16x32_bf16 v[12:15], a[172:175], v[172:175], v[12:15]\n"
"s_bfe_u32 s48, s38, 0x30010\n"
"ds_read_b64_tr_b16 a[186:187], v217 offset:20744\n"
"v_lshlrev_b32 v232, 16, v202\n"
"v_mfma_f32_16x16x32_bf16 v[8:11], a[164:167], v[172:175], v[8:11]\n"
"s_lshl_b32 s48, s48, 14\n"
"v_exp_f32 v199, v199\n"
"v_mfma_f32_16x16x32_bf16 v[8:11], a[160:163], v[168:171], v[8:11]\n"
"s_add_u32 s36, s79, s48\n"
"ds_read_b64_tr_b16 a[188:189], v217 offset:24840\n"
"v_cvt_pk_bf16_f32 v137, v198, v199\n"
"v_mfma_f32_16x16x32_bf16 v[16:19], a[160:163], v[176:179], v[16:19]\n"
"s_add_u32 s48, s42, s36\n"
"v_exp_f32 v248, v248\n"
"v_mfma_f32_16x16x32_bf16 v[16:19], a[164:167], v[180:183], v[16:19]\n"
"s_addc_u32 s49, s43, 0\n"
"ds_read_b64_tr_b16 a[190:191], v217 offset:28936\n"
"v_and_b32 v233, s67, v202\n"
"v_mfma_f32_16x16x32_bf16 v[20:23], a[172:175], v[180:183], v[20:23]\n"
"s_add_u32 m0, s64, 0\n"
"v_exp_f32 v249, v249\n"
"v_mfma_f32_16x16x32_bf16 v[20:23], a[168:171], v[176:179], v[20:23]\n"
"global_load_lds_dwordx4 v219, s[48:49] offset:0\n"
"v_cvt_pk_bf16_f32 v144, v248, v249\n"
"v_mfma_f32_16x16x32_bf16 v[28:31], a[168:171], v[184:187], v[28:31]\n"
"v_exp_f32 v250, v250\n"
"v_mfma_f32_16x16x32_bf16 v[28:31], a[172:175], v[188:191], v[28:31]\n"
"buffer_store_dwordx4 a[232:235], v223, s[56:59], s71 offen offset:0 sc1 nt\n"
"v_lshlrev_b32 v234, 16, v203\n"
"v_and_b32 v235, s67, v203\n"
"v_pk_mul_f32 v[228:229], v[228:229], s[68:69]\n"
"v_pk_mul_f32 v[230:231], v[230:231], s[68:69]\n"
"v_pk_mul_f32 v[232:233], v[232:233], s[68:69]\n"
"v_pk_mul_f32 v[234:235], v[234:235], s[68:69]\n"
"v_cvt_pk_bf16_f32 v228, v228, v229\n"
"v_cvt_pk_bf16_f32 v230, v230, v231\n"
"v_mfma_f32_16x16x32_bf16 v[24:27], a[164:167], v[188:191], v[24:27]\n"
"v_exp_f32 v251, v251\n"
"v_mfma_f32_16x16x32_bf16 v[24:27], a[160:163], v[184:187], v[24:27]\n"
"v_cvt_pk_bf16_f32 v145, v250, v251\n"
"v_cvt_pk_bf16_f32 v232, v232, v233\n"
"v_mfma_f32_16x16x32_bf16 v[192:195], a[144:147], a[0:3], -4.0\n"
"v_exp_f32 v252, v252\n"
"v_mfma_f32_16x16x32_bf16 v[192:195], a[148:151], a[4:7], v[192:195]\n"
"s_waitcnt lgkmcnt(3)\n"
"ds_read_b128 a[128:131], v216 offset:40960\n"
"v_cvt_pk_bf16_f32 v234, v234, v235\n"
"v_mfma_f32_16x16x32_bf16 v[192:195], a[152:155], a[8:11], v[192:195]\n"
"v_exp_f32 v253, v253\n"
"v_mfma_f32_16x16x32_bf16 v[192:195], a[156:159], a[12:15], v[192:195]\n"
"ds_read_b128 a[132:135], v216 offset:41216\n"
"v_cvt_pk_bf16_f32 v152, v252, v253\n"
"v_mfma_f32_16x16x32_bf16 v[196:199], a[156:159], a[28:31], -4.0\n"
"v_exp_f32 v254, v254\n"
"v_mfma_f32_16x16x32_bf16 v[196:199], a[152:155], a[24:27], v[196:199]\n"
"ds_read_b128 a[136:139], v216 offset:41472\n"
"v_accvgpr_write_b32 a64, v228\n"
"v_mfma_f32_16x16x32_bf16 v[196:199], a[148:151], a[20:23], v[196:199]\n"
"s_add_u32 m0, s64, 0\n"
"v_exp_f32 v255, v255\n"
"v_mfma_f32_16x16x32_bf16 v[196:199], a[144:147], a[16:19], v[196:199]\n"
"global_load_lds_dwordx4 v219, s[48:49] offset:1024\n"
"v_cvt_pk_bf16_f32 v153, v254, v255\n"
"v_mfma_f32_16x16x32_bf16 v[248:251], a[144:147], a[32:35], -4.0\n"
"v_exp_f32 v192, v192\n"
"v_mfma_f32_16x16x32_bf16 v[248:251], a[148:151], a[36:39], v[248:251]\n"
"ds_read_b128 a[140:143], v216 offset:41728\n"
"v_accvgpr_write_b32 a65, v230\n"
"v_mfma_f32_16x16x32_bf16 v[248:251], a[152:155], a[40:43], v[248:251]\n"
"v_exp_f32 v193, v193\n"
"v_mfma_f32_16x16x32_bf16 v[248:251], a[156:159], a[44:47], v[248:251]\n"
"ds_read_b64_tr_b16 a[160:161], v217 offset:16896\n"
"v_cvt_pk_bf16_f32 v130, v192, v193\n"
"v_mfma_f32_16x16x32_bf16 v[252:255], a[156:159], a[60:63], -4.0\n"
"v_exp_f32 v194, v194\n"
"v_mfma_f32_16x16x32_bf16 v[252:255], a[152:155], a[56:59], v[252:255]\n"
"ds_read_b64_tr_b16 a[162:163], v217 offset:20992\n"
"v_accvgpr_write_b32 a66, v232\n"
"v_mfma_f32_16x16x32_bf16 v[252:255], a[148:151], a[52:55], v[252:255]\n"
"s_add_u32 m0, s64, 0\n"
"v_exp_f32 v195, v195\n"
"v_mfma_f32_16x16x32_bf16 v[252:255], a[144:147], a[48:51], v[252:255]\n"
"global_load_lds_dwordx4 v220, s[48:49] offset:2048\n"
"v_cvt_pk_bf16_f32 v131, v194, v195\n"
"v_mfma_f32_16x16x32_bf16 v[32:35], a[176:179], v[160:163], v[32:35]\n"
"v_exp_f32 v196, v196\n"
"v_mfma_f32_16x16x32_bf16 v[32:35], a[180:183], v[164:167], v[32:35]\n"
"s_waitcnt lgkmcnt(5)\n"
"ds_read_b64_tr_b16 a[164:165], v217 offset:25088\n"
"v_accvgpr_write_b32 a67, v234\n"
"v_mfma_f32_16x16x32_bf16 v[36:39], a[188:191], v[164:167], v[36:39]\n"
"v_exp_f32 v197, v197\n"
"v_mfma_f32_16x16x32_bf16 v[36:39], a[184:187], v[160:163], v[36:39]\n"
"ds_read_b64_tr_b16 a[166:167], v217 offset:29184\n"
"v_cvt_pk_bf16_f32 v138, v196, v197\n"
"v_mfma_f32_16x16x32_bf16 v[44:47], a[184:187], v[168:171], v[44:47]\n"
"v_exp_f32 v198, v198\n"
"v_mfma_f32_16x16x32_bf16 v[44:47], a[188:191], v[172:175], v[44:47]\n"
"ds_read_b64_tr_b16 a[168:169], v217 offset:16904\n"
"v_lshlrev_b32 v228, 16, v204\n"
"v_and_b32 v229, s67, v204\n"
"v_lshlrev_b32 v230, 16, v205\n"
"v_mfma_f32_16x16x32_bf16 v[40:43], a[180:183], v[172:175], v[40:43]\n"
"v_exp_f32 v199, v199\n"
"v_mfma_f32_16x16x32_bf16 v[40:43], a[176:179], v[168:171], v[40:43]\n"
"ds_read_b128 v[200:203], v218 offset:768\n"
"v_cvt_pk_bf16_f32 v139, v198, v199\n"
"v_mfma_f32_16x16x32_bf16 v[48:51], a[176:179], v[176:179], v[48:51]\n"
"v_exp_f32 v248, v248\n"
"v_mfma_f32_16x16x32_bf16 v[48:51], a[180:183], v[180:183], v[48:51]\n"
"ds_read_b64_tr_b16 a[170:171], v217 offset:21000\n"
"v_and_b32 v231, s67, v205\n"
"v_mfma_f32_16x16x32_bf16 v[52:55], a[188:191], v[180:183], v[52:55]\n"
"v_exp_f32 v249, v249\n"
"v_mfma_f32_16x16x32_bf16 v[52:55], a[184:187], v[176:179], v[52:55]\n"
"ds_read_b64_tr_b16 a[172:173], v217 offset:25096\n"
"v_cvt_pk_bf16_f32 v146, v248, v249\n"
"v_mfma_f32_16x16x32_bf16 v[60:63], a[184:187], v[184:187], v[60:63]\n"
"v_exp_f32 v250, v250\n"
"v_mfma_f32_16x16x32_bf16 v[60:63], a[188:191], v[188:191], v[60:63]\n"
"ds_read_b64_tr_b16 a[174:175], v217 offset:29192\n"
"v_lshlrev_b32 v232, 16, v206\n"
"v_and_b32 v233, s67, v206\n"
"v_lshlrev_b32 v234, 16, v207\n"
"v_and_b32 v235, s67, v207\n"
"v_pk_mul_f32 v[228:229], v[228:229], s[68:69]\n"
"v_pk_mul_f32 v[230:231], v[230:231], s[68:69]\n"
"v_pk_mul_f32 v[232:233], v[232:233], s[68:69]\n"
"v_pk_mul_f32 v[234:235], v[234:235], s[68:69]\n"
"v_cvt_pk_bf16_f32 v228, v228, v229\n"
"v_cvt_pk_bf16_f32 v230, v230, v231\n"
"v_mfma_f32_16x16x32_bf16 v[56:59], a[180:183], v[188:191], v[56:59]\n"
"s_add_u32 m0, s64, 0\n"
"v_exp_f32 v251, v251\n"
"v_mfma_f32_16x16x32_bf16 v[56:59], a[176:179], v[184:187], v[56:59]\n"
"global_load_lds_dwordx4 v220, s[48:49] offset:3072\n"
"v_cvt_pk_bf16_f32 v147, v250, v251\n"
"v_mfma_f32_16x16x32_bf16 v[192:195], a[128:131], a[0:3], -4.0\n"
"s_waitcnt lgkmcnt(5)\n"
"v_exp_f32 v252, v252\n"
"v_mfma_f32_16x16x32_bf16 v[192:195], a[132:135], a[4:7], v[192:195]\n"
"ds_read_b128 a[144:147], v216 offset:45056\n"
"v_cvt_pk_bf16_f32 v232, v232, v233\n"
"v_mfma_f32_16x16x32_bf16 v[192:195], a[136:139], a[8:11], v[192:195]\n"
"v_exp_f32 v253, v253\n"
"v_mfma_f32_16x16x32_bf16 v[192:195], a[140:143], a[12:15], v[192:195]\n"
"s_add_u32 s35, s55, s71\n"
"ds_read_b128 a[148:151], v216 offset:45312\n"
"v_cvt_pk_bf16_f32 v154, v252, v253\n"
"v_mfma_f32_16x16x32_bf16 v[196:199], a[140:143], a[28:31], -4.0\n"
"s_add_u32 s52, s40, s35\n"
"v_exp_f32 v254, v254\n"
"v_mfma_f32_16x16x32_bf16 v[196:199], a[136:139], a[24:27], v[196:199]\n"
"s_addc_u32 s53, s41, 0\n"
"ds_read_b128 a[152:155], v216 offset:45568\n"
"v_cvt_pk_bf16_f32 v234, v234, v235\n"
"v_mfma_f32_16x16x32_bf16 v[196:199], a[132:135], a[20:23], v[196:199]\n"
"s_add_u32 m0, s66, 0\n"
"v_exp_f32 v255, v255\n"
"v_mfma_f32_16x16x32_bf16 v[196:199], a[128:131], a[16:19], v[196:199]\n"
"s_waitcnt lgkmcnt(5)\n"
"global_load_lds_dwordx4 v219, s[52:53] offset:0\n"
"v_cvt_pk_bf16_f32 v155, v254, v255\n"
"v_mfma_f32_16x16x32_bf16 v[248:251], a[128:131], a[32:35], -4.0\n"
"v_exp_f32 v192, v192\n"
"v_mfma_f32_16x16x32_bf16 v[248:251], a[132:135], a[36:39], v[248:251]\n"
"ds_read_b128 a[156:159], v216 offset:45824\n"
"v_accvgpr_write_b32 a68, v228\n"
"v_mfma_f32_16x16x32_bf16 v[248:251], a[136:139], a[40:43], v[248:251]\n"
"v_exp_f32 v193, v193\n"
"v_mfma_f32_16x16x32_bf16 v[248:251], a[140:143], a[44:47], v[248:251]\n"
"ds_read_b64_tr_b16 a[176:177], v217 offset:17152\n"
"v_cvt_pk_bf16_f32 v132, v192, v193\n"
"v_mfma_f32_16x16x32_bf16 v[252:255], a[140:143], a[60:63], -4.0\n"
"v_exp_f32 v194, v194\n"
"v_mfma_f32_16x16x32_bf16 v[252:255], a[136:139], a[56:59], v[252:255]\n"
"ds_read_b64_tr_b16 a[178:179], v217 offset:21248\n"
"v_accvgpr_write_b32 a69, v230\n"
"v_mfma_f32_16x16x32_bf16 v[252:255], a[132:135], a[52:55], v[252:255]\n"
"v_exp_f32 v195, v195\n"
"v_mfma_f32_16x16x32_bf16 v[252:255], a[128:131], a[48:51], v[252:255]\n"
"v_cvt_pk_bf16_f32 v133, v194, v195\n"
"v_accvgpr_write_b32 a70, v232\n"
"v_mfma_f32_16x16x32_bf16 v[64:67], a[160:163], v[160:163], v[64:67]\n"
"v_exp_f32 v196, v196\n"
"v_mfma_f32_16x16x32_bf16 v[64:67], a[164:167], v[164:167], v[64:67]\n"
"s_waitcnt lgkmcnt(5)\n"
"ds_read_b64_tr_b16 a[180:181], v217 offset:25344\n"
"v_accvgpr_write_b32 a71, v234\n"
"v_mfma_f32_16x16x32_bf16 v[68:71], a[172:175], v[164:167], v[68:71]\n"
"v_exp_f32 v197, v197\n"
"v_mfma_f32_16x16x32_bf16 v[68:71], a[168:171], v[160:163], v[68:71]\n"
"ds_read_b64_tr_b16 a[182:183], v217 offset:29440\n"
"v_cvt_pk_bf16_f32 v140, v196, v197\n"
"v_mfma_f32_16x16x32_bf16 v[76:79], a[168:171], v[168:171], v[76:79]\n"
"v_exp_f32 v198, v198\n"
"v_mfma_f32_16x16x32_bf16 v[76:79], a[172:175], v[172:175], v[76:79]\n"
"ds_read_b64_tr_b16 a[184:185], v217 offset:17160\n"
"v_lshlrev_b32 v228, 16, v244\n"
"v_mfma_f32_16x16x32_bf16 v[72:75], a[164:167], v[172:175], v[72:75]\n"
"v_exp_f32 v199, v199\n"
"v_mfma_f32_16x16x32_bf16 v[72:75], a[160:163], v[168:171], v[72:75]\n"
"v_cvt_pk_bf16_f32 v141, v198, v199\n"
"v_and_b32 v229, s67, v244\n"
"v_mfma_f32_16x16x32_bf16 v[80:83], a[160:163], v[176:179], v[80:83]\n"
"v_exp_f32 v248, v248\n"
"v_mfma_f32_16x16x32_bf16 v[80:83], a[164:167], v[180:183], v[80:83]\n"
"ds_read_b64_tr_b16 a[186:187], v217 offset:21256\n"
"v_lshlrev_b32 v230, 16, v245\n"
"v_mfma_f32_16x16x32_bf16 v[84:87], a[172:175], v[180:183], v[84:87]\n"
"v_exp_f32 v249, v249\n"
"v_mfma_f32_16x16x32_bf16 v[84:87], a[168:171], v[176:179], v[84:87]\n"
"ds_read_b64_tr_b16 a[188:189], v217 offset:25352\n"
"v_cvt_pk_bf16_f32 v148, v248, v249\n"
"v_mfma_f32_16x16x32_bf16 v[92:95], a[168:171], v[184:187], v[92:95]\n"
"v_exp_f32 v250, v250\n"
"v_mfma_f32_16x16x32_bf16 v[92:95], a[172:175], v[188:191], v[92:95]\n"
"ds_read_b64_tr_b16 a[190:191], v217 offset:29448\n"
"v_and_b32 v231, s67, v245\n"
"v_mfma_f32_16x16x32_bf16 v[88:91], a[164:167], v[188:191], v[88:91]\n"
"v_exp_f32 v251, v251\n"
"v_mfma_f32_16x16x32_bf16 v[88:91], a[160:163], v[184:187], v[88:91]\n"
"s_waitcnt vmcnt(12) lgkmcnt(6)\n"
"v_cvt_pk_bf16_f32 v149, v250, v251\n"
"v_lshlrev_b32 v232, 16, v246\n"
"v_mfma_f32_16x16x32_bf16 v[192:195], a[144:147], a[0:3], -4.0\n"
"s_barrier\n"
"v_exp_f32 v252, v252\n"
"v_mfma_f32_16x16x32_bf16 v[192:195], a[148:151], a[4:7], v[192:195]\n"
"s_bfe_u32 s50, s38, 0x30010\n"
"ds_read_b128 a[128:131], v216 offset:49152\n"
"v_and_b32 v233, s67, v246\n"
"v_mfma_f32_16x16x32_bf16 v[192:195], a[152:155], a[8:11], v[192:195]\n"
"s_lshl_b32 s50, s50, 14\n"
"v_exp_f32 v253, v253\n"
"v_mfma_f32_16x16x32_bf16 v[192:195], a[156:159], a[12:15], v[192:195]\n"
"s_add_u32 s39, s54, s50\n"
"ds_read_b128 a[132:135], v216 offset:49408\n"
"v_cvt_pk_bf16_f32 v156, v252, v253\n"
"v_mfma_f32_16x16x32_bf16 v[196:199], a[156:159], a[28:31], -4.0\n"
"s_add_u32 s50, s44, s39\n"
"v_exp_f32 v254, v254\n"
"v_mfma_f32_16x16x32_bf16 v[196:199], a[152:155], a[24:27], v[196:199]\n"
"s_addc_u32 s51, s45, 0\n"
"ds_read_b128 a[136:139], v216 offset:49664\n"
"v_lshlrev_b32 v234, 16, v247\n"
"v_mfma_f32_16x16x32_bf16 v[196:199], a[148:151], a[20:23], v[196:199]\n"
"s_add_u32 m0, s65, 0\n"
"v_exp_f32 v255, v255\n"
"v_mfma_f32_16x16x32_bf16 v[196:199], a[144:147], a[16:19], v[196:199]\n"
"global_load_lds_dwordx4 v219, s[50:51] offset:0\n"
"v_cvt_pk_bf16_f32 v157, v254, v255\n"
"v_and_b32 v235, s67, v247\n"
"v_mul_f32 v228, s68, v228\n"
"v_mul_f32 v229, s68, v229\n"
"v_pk_mul_f32 v[230:231], v[230:231], s[68:69]\n"
"v_pk_mul_f32 v[232:233], v[232:233], s[68:69]\n"
"v_pk_mul_f32 v[234:235], v[234:235], s[68:69]\n"
"v_cvt_pk_bf16_f32 v228, v228, v229\n"
"v_cvt_pk_bf16_f32 v230, v230, v231\n"
"v_mfma_f32_16x16x32_bf16 v[248:251], a[144:147], a[32:35], -4.0\n"
"v_exp_f32 v192, v192\n"
"v_mfma_f32_16x16x32_bf16 v[248:251], a[148:151], a[36:39], v[248:251]\n"
"ds_read_b128 a[140:143], v216 offset:49920\n"
"v_cvt_pk_bf16_f32 v232, v232, v233\n"
"v_mfma_f32_16x16x32_bf16 v[248:251], a[152:155], a[40:43], v[248:251]\n"
"v_exp_f32 v193, v193\n"
"v_mfma_f32_16x16x32_bf16 v[248:251], a[156:159], a[44:47], v[248:251]\n"
"ds_read_b64_tr_b16 a[160:161], v217 offset:32768\n"
"v_cvt_pk_bf16_f32 v134, v192, v193\n"
"v_mfma_f32_16x16x32_bf16 v[252:255], a[156:159], a[60:63], -4.0\n"
"v_exp_f32 v194, v194\n"
"v_mfma_f32_16x16x32_bf16 v[252:255], a[152:155], a[56:59], v[252:255]\n"
"ds_read_b64_tr_b16 a[162:163], v217 offset:36864\n"
"v_cvt_pk_bf16_f32 v234, v234, v235\n"
"v_mfma_f32_16x16x32_bf16 v[252:255], a[148:151], a[52:55], v[252:255]\n"
"s_add_u32 m0, s66, 0\n"
"v_exp_f32 v195, v195\n"
"v_mfma_f32_16x16x32_bf16 v[252:255], a[144:147], a[48:51], v[252:255]\n"
"global_load_lds_dwordx4 v219, s[52:53] offset:1024\n"
"v_cvt_pk_bf16_f32 v135, v194, v195\n"
"v_mfma_f32_16x16x32_bf16 v[96:99], a[176:179], v[160:163], v[96:99]\n"
"s_waitcnt lgkmcnt(4)\n"
"v_exp_f32 v196, v196\n"
"v_mfma_f32_16x16x32_bf16 v[96:99], a[180:183], v[164:167], v[96:99]\n"
"ds_read_b64_tr_b16 a[164:165], v217 offset:40960\n"
"buffer_store_dwordx4 a[236:239], v223, s[56:59], s71 offen offset:2048 sc1 nt\n"
"v_accvgpr_write_b32 a72, v228\n"
"v_accvgpr_write_b32 a73, v230\n"
"v_mfma_f32_16x16x32_bf16 v[100:103], a[188:191], v[164:167], v[100:103]\n"
"v_exp_f32 v197, v197\n"
"v_mfma_f32_16x16x32_bf16 v[100:103], a[184:187], v[160:163], v[100:103]\n"
"ds_read_b64_tr_b16 a[166:167], v217 offset:45056\n"
"v_cvt_pk_bf16_f32 v142, v196, v197\n"
"v_mfma_f32_16x16x32_bf16 v[108:111], a[184:187], v[168:171], v[108:111]\n"
"v_exp_f32 v198, v198\n"
"v_mfma_f32_16x16x32_bf16 v[108:111], a[188:191], v[172:175], v[108:111]\n"
"ds_read_b64_tr_b16 a[168:169], v217 offset:32776\n"
"v_accvgpr_write_b32 a74, v232\n"
"v_mfma_f32_16x16x32_bf16 v[104:107], a[180:183], v[172:175], v[104:107]\n"
"s_add_u32 m0, s65, 0\n"
"v_exp_f32 v199, v199\n"
"v_mfma_f32_16x16x32_bf16 v[104:107], a[176:179], v[168:171], v[104:107]\n"
"global_load_lds_dwordx4 v219, s[50:51] offset:1024\n"
"v_cvt_pk_bf16_f32 v143, v198, v199\n"
"v_mfma_f32_16x16x32_bf16 v[112:115], a[176:179], v[176:179], v[112:115]\n"
"v_exp_f32 v248, v248\n"
"v_mfma_f32_16x16x32_bf16 v[112:115], a[180:183], v[180:183], v[112:115]\n"
"ds_read_b64_tr_b16 a[170:171], v217 offset:36872\n"
"v_accvgpr_write_b32 a75, v234\n"
"v_mfma_f32_16x16x32_bf16 v[116:119], a[188:191], v[180:183], v[116:119]\n"
"v_exp_f32 v249, v249\n"
"v_mfma_f32_16x16x32_bf16 v[116:119], a[184:187], v[176:179], v[116:119]\n"
"ds_read_b64_tr_b16 a[172:173], v217 offset:40968\n"
"v_cvt_pk_bf16_f32 v150, v248, v249\n"
"v_mfma_f32_16x16x32_bf16 v[124:127], a[184:187], v[184:187], v[124:127]\n"
"v_exp_f32 v250, v250\n"
"v_mfma_f32_16x16x32_bf16 v[124:127], a[188:191], v[188:191], v[124:127]\n"
"ds_read_b64_tr_b16 a[174:175], v217 offset:45064\n"
"v_lshlrev_b32 v228, 16, v200\n"
"v_mfma_f32_16x16x32_bf16 v[120:123], a[180:183], v[188:191], v[120:123]\n"
"s_add_u32 m0, s65, 0\n"
"v_exp_f32 v251, v251\n"
"v_mfma_f32_16x16x32_bf16 v[120:123], a[176:179], v[184:187], v[120:123]\n"
"global_load_lds_dwordx4 v219, s[50:51] offset:2048\n"
"v_cvt_pk_bf16_f32 v151, v250, v251\n"
"v_mfma_f32_16x16x32_bf16 v[192:195], a[128:131], a[0:3], -4.0\n"
"v_exp_f32 v252, v252\n"
"v_mfma_f32_16x16x32_bf16 v[192:195], a[132:135], a[4:7], v[192:195]\n"
"s_waitcnt lgkmcnt(5)\n"
"ds_read_b128 a[144:147], v216 offset:53248\n"
"v_and_b32 v229, s67, v200\n"
"v_mfma_f32_16x16x32_bf16 v[192:195], a[136:139], a[8:11], v[192:195]\n"
"v_exp_f32 v253, v253\n"
"v_mfma_f32_16x16x32_bf16 v[192:195], a[140:143], a[12:15], v[192:195]\n"
"ds_read_b128 a[148:151], v216 offset:53504\n"
"v_cvt_pk_bf16_f32 v158, v252, v253\n"
"v_mfma_f32_16x16x32_bf16 v[196:199], a[140:143], a[28:31], -4.0\n"
"v_exp_f32 v254, v254\n"
"v_mfma_f32_16x16x32_bf16 v[196:199], a[136:139], a[24:27], v[196:199]\n"
"ds_read_b128 a[152:155], v216 offset:53760\n"
"v_lshlrev_b32 v230, 16, v201\n"
"v_mfma_f32_16x16x32_bf16 v[196:199], a[132:135], a[20:23], v[196:199]\n"
"v_exp_f32 v255, v255\n"
"v_mfma_f32_16x16x32_bf16 v[196:199], a[128:131], a[16:19], v[196:199]\n"
"v_cvt_pk_bf16_f32 v159, v254, v255\n"
"v_and_b32 v231, s67, v201\n"
"v_mfma_f32_16x16x32_bf16 v[248:251], a[128:131], a[32:35], -4.0\n"
"v_exp_f32 v192, v192\n"
"v_mfma_f32_16x16x32_bf16 v[248:251], a[132:135], a[36:39], v[248:251]\n"
"ds_read_b128 a[156:159], v216 offset:54016\n"
"v_lshlrev_b32 v232, 16, v202\n"
"v_mfma_f32_16x16x32_bf16 v[248:251], a[136:139], a[40:43], v[248:251]\n"
"v_exp_f32 v193, v193\n"
"v_mfma_f32_16x16x32_bf16 v[248:251], a[140:143], a[44:47], v[248:251]\n"
"ds_read_b64_tr_b16 a[176:177], v217 offset:33024\n"
"v_cvt_pk_bf16_f32 v160, v192, v193\n"
"v_mfma_f32_16x16x32_bf16 v[252:255], a[140:143], a[60:63], -4.0\n"
"v_exp_f32 v194, v194\n"
"v_mfma_f32_16x16x32_bf16 v[252:255], a[136:139], a[56:59], v[252:255]\n"
"ds_read_b64_tr_b16 a[178:179], v217 offset:37120\n"
"v_and_b32 v233, s67, v202\n"
"v_mfma_f32_16x16x32_bf16 v[252:255], a[132:135], a[52:55], v[252:255]\n"
"s_add_u32 m0, s65, 0\n"
"v_exp_f32 v195, v195\n"
"v_mfma_f32_16x16x32_bf16 v[252:255], a[128:131], a[48:51], v[252:255]\n"
"global_load_lds_dwordx4 v219, s[50:51] offset:3072\n"
"v_cvt_pk_bf16_f32 v161, v194, v195\n"
"v_smfmac_f32_16x16x64_bf16 a[192:195], a[208:211], v[128:135], v215\n"
"v_lshlrev_b32 v234, 16, v203\n"
"v_smfmac_f32_16x16x64_bf16 a[196:199], a[208:211], v[136:143], v215\n"
"ds_read_b64_tr_b16 a[180:181], v217 offset:41216\n"
"v_smfmac_f32_16x16x64_bf16 a[200:203], a[208:211], v[144:151], v215\n"
"v_and_b32 v235, s67, v203\n"
"v_smfmac_f32_16x16x64_bf16 a[204:207], a[208:211], v[152:159], v215\n"
"ds_read_b64_tr_b16 a[182:183], v217 offset:45312\n"
"v_mfma_f32_16x16x32_bf16 v[0:3], a[160:163], v[128:131], v[0:3]\n"
"s_waitcnt lgkmcnt(6)\n"
"v_exp_f32 v196, v196\n"
"v_mfma_f32_16x16x32_bf16 v[0:3], a[164:167], v[132:135], v[0:3]\n"
"ds_read_b64_tr_b16 a[184:185], v217 offset:33032\n"
"v_mul_f32 v228, s68, v228\n"
"v_mfma_f32_16x16x32_bf16 v[4:7], a[172:175], v[132:135], v[4:7]\n"
"v_exp_f32 v197, v197\n"
"v_mfma_f32_16x16x32_bf16 v[4:7], a[168:171], v[128:131], v[4:7]\n"
"v_cvt_pk_bf16_f32 v168, v196, v197\n"
"v_mul_f32 v229, s68, v229\n"
"v_mfma_f32_16x16x32_bf16 v[12:15], a[168:171], v[136:139], v[12:15]\n"
"v_exp_f32 v198, v198\n"
"v_mfma_f32_16x16x32_bf16 v[12:15], a[172:175], v[140:143], v[12:15]\n"
"s_bfe_u32 s48, s38, 0x30014\n"
"ds_read_b64_tr_b16 a[186:187], v217 offset:37128\n"
"v_mul_f32 v230, s68, v230\n"
"v_mfma_f32_16x16x32_bf16 v[8:11], a[164:167], v[140:143], v[8:11]\n"
"s_lshl_b32 s48, s48, 14\n"
"v_exp_f32 v199, v199\n"
"v_mfma_f32_16x16x32_bf16 v[8:11], a[160:163], v[136:139], v[8:11]\n"
"s_add_u32 s36, s79, s48\n"
"ds_read_b64_tr_b16 a[188:189], v217 offset:41224\n"
"v_cvt_pk_bf16_f32 v169, v198, v199\n"
"v_mfma_f32_16x16x32_bf16 v[16:19], a[160:163], v[144:147], v[16:19]\n"
"s_add_u32 s48, s42, s36\n"
"v_exp_f32 v248, v248\n"
"v_mfma_f32_16x16x32_bf16 v[16:19], a[164:167], v[148:151], v[16:19]\n"
"s_addc_u32 s49, s43, 0\n"
"ds_read_b64_tr_b16 a[190:191], v217 offset:45320\n"
"v_mul_f32 v231, s68, v231\n"
"v_mfma_f32_16x16x32_bf16 v[20:23], a[172:175], v[148:151], v[20:23]\n"
"s_add_u32 m0, s64, 16384\n"
"v_exp_f32 v249, v249\n"
"v_mfma_f32_16x16x32_bf16 v[20:23], a[168:171], v[144:147], v[20:23]\n"
"global_load_lds_dwordx4 v219, s[48:49] offset:0\n"
"v_cvt_pk_bf16_f32 v176, v248, v249\n"
"v_mfma_f32_16x16x32_bf16 v[28:31], a[168:171], v[152:155], v[28:31]\n"
"v_exp_f32 v250, v250\n"
"v_mfma_f32_16x16x32_bf16 v[28:31], a[172:175], v[156:159], v[28:31]\n"
"v_mul_f32 v232, s68, v232\n"
"v_mul_f32 v233, s68, v233\n"
"v_mfma_f32_16x16x32_bf16 v[24:27], a[164:167], v[156:159], v[24:27]\n"
"s_add_u32 m0, s66, 0\n"
"v_exp_f32 v251, v251\n"
"v_mfma_f32_16x16x32_bf16 v[24:27], a[160:163], v[152:155], v[24:27]\n"
"global_load_lds_dwordx4 v220, s[52:53] offset:2048\n"
"v_cvt_pk_bf16_f32 v177, v250, v251\n"
"v_mfma_f32_16x16x32_bf16 v[192:195], a[144:147], a[0:3], -4.0\n"
".Lattn_pad7:\n"
"s_cbranch_vccnz .Lattn_ret\n"
"v_exp_f32 v252, v252\n"
"v_mfma_f32_16x16x32_bf16 v[192:195], a[148:151], a[4:7], v[192:195]\n"
"s_waitcnt lgkmcnt(3)\n"
"ds_read_b128 a[128:131], v216 offset:57344\n"
"v_mul_f32 v234, s68, v234\n"
"v_mfma_f32_16x16x32_bf16 v[192:195], a[152:155], a[8:11], v[192:195]\n"
"v_exp_f32 v253, v253\n"
"v_mfma_f32_16x16x32_bf16 v[192:195], a[156:159], a[12:15], v[192:195]\n"
"ds_read_b128 a[132:135], v216 offset:57600\n"
"v_cvt_pk_bf16_f32 v184, v252, v253\n"
"v_mfma_f32_16x16x32_bf16 v[196:199], a[156:159], a[28:31], -4.0\n"
"v_exp_f32 v254, v254\n"
"v_mfma_f32_16x16x32_bf16 v[196:199], a[152:155], a[24:27], v[196:199]\n"
"ds_read_b128 a[136:139], v216 offset:57856\n"
"v_mul_f32 v235, s68, v235\n"
"v_mfma_f32_16x16x32_bf16 v[196:199], a[148:151], a[20:23], v[196:199]\n"
"s_add_u32 m0, s64, 16384\n"
"v_exp_f32 v255, v255\n"
"v_mfma_f32_16x16x32_bf16 v[196:199], a[144:147], a[16:19], v[196:199]\n"
"global_load_lds_dwordx4 v219, s[48:49] offset:1024\n"
"v_cvt_pk_bf16_f32 v185, v254, v255\n"
"v_mfma_f32_16x16x32_bf16 v[248:251], a[144:147], a[32:35], -4.0\n"
"v_exp_f32 v192, v192\n"
"v_mfma_f32_16x16x32_bf16 v[248:251], a[148:151], a[36:39], v[248:251]\n"
"ds_read_b128 a[140:143], v216 offset:58112\n"
"v_cvt_pk_bf16_f32 v228, v228, v229\n"
"v_mfma_f32_16x16x32_bf16 v[248:251], a[152:155], a[40:43], v[248:251]\n"
"v_exp_f32 v193, v193\n"
"v_mfma_f32_16x16x32_bf16 v[248:251], a[156:159], a[44:47], v[248:251]\n"
"s_waitcnt vmcnt(16)\n"
"ds_read_b64_tr_b16 a[160:161], v217 offset:33280\n"
"ds_read_b128 v[204:207], v218 offset:4096\n"
"v_cvt_pk_bf16_f32 v162, v192, v193\n"
"v_mfma_f32_16x16x32_bf16 v[252:255], a[156:159], a[60:63], -4.0\n"
"v_exp_f32 v194, v194\n"
"v_mfma_f32_16x16x32_bf16 v[252:255], a[152:155], a[56:59], v[252:255]\n"
"ds_read_b64_tr_b16 a[162:163], v217 offset:37376\n"
"v_cvt_pk_bf16_f32 v230, v230, v231\n"
"v_mfma_f32_16x16x32_bf16 v[252:255], a[148:151], a[52:55], v[252:255]\n"
"s_add_u32 m0, s64, 16384\n"
"v_exp_f32 v195, v195\n"
"v_mfma_f32_16x16x32_bf16 v[252:255], a[144:147], a[48:51], v[252:255]\n"
"global_load_lds_dwordx4 v220, s[48:49] offset:2048\n"
"v_cvt_pk_bf16_f32 v163, v194, v195\n"
"v_mfma_f32_16x16x32_bf16 v[32:35], a[176:179], v[128:131], v[32:35]\n"
"v_exp_f32 v196, v196\n"
"v_mfma_f32_16x16x32_bf16 v[32:35], a[180:183], v[132:135], v[32:35]\n"
"s_waitcnt lgkmcnt(6)\n"
"ds_read_b64_tr_b16 a[164:165], v217 offset:41472\n"
"ds_read_b128 v[244:247], v218 offset:4352\n"
"v_mfma_f32_16x16x32_bf16 v[36:39], a[188:191], v[132:135], v[36:39]\n"
"v_exp_f32 v197, v197\n"
"v_mfma_f32_16x16x32_bf16 v[36:39], a[184:187], v[128:131], v[36:39]\n"
"ds_read_b64_tr_b16 a[166:167], v217 offset:45568\n"
"buffer_store_dwordx4 a[240:243], v223, s[56:59], s37 offen offset:0 sc1 nt\n"
"v_cvt_pk_bf16_f32 v170, v196, v197\n"
"v_cvt_pk_bf16_f32 v232, v232, v233\n"
"v_mfma_f32_16x16x32_bf16 v[44:47], a[184:187], v[136:139], v[44:47]\n"
"v_exp_f32 v198, v198\n"
"v_mfma_f32_16x16x32_bf16 v[44:47], a[188:191], v[140:143], v[44:47]\n"
"ds_read_b64_tr_b16 a[168:169], v217 offset:33288\n"
"v_cvt_pk_bf16_f32 v234, v234, v235\n"
"v_mfma_f32_16x16x32_bf16 v[40:43], a[180:183], v[140:143], v[40:43]\n"
"v_exp_f32 v199, v199\n"
"v_mfma_f32_16x16x32_bf16 v[40:43], a[176:179], v[136:139], v[40:43]\n"
"v_cvt_pk_bf16_f32 v171, v198, v199\n"
"v_accvgpr_write_b32 a76, v228\n"
"v_mfma_f32_16x16x32_bf16 v[48:51], a[176:179], v[144:147], v[48:51]\n"
"v_exp_f32 v248, v248\n"
"v_mfma_f32_16x16x32_bf16 v[48:51], a[180:183], v[148:151], v[48:51]\n"
"ds_read_b64_tr_b16 a[170:171], v217 offset:37384\n"
"v_accvgpr_write_b32 a77, v230\n"
"v_mfma_f32_16x16x32_bf16 v[52:55], a[188:191], v[148:151], v[52:55]\n"
"v_exp_f32 v249, v249\n"
"v_mfma_f32_16x16x32_bf16 v[52:55], a[184:187], v[144:147], v[52:55]\n"
"ds_read_b64_tr_b16 a[172:173], v217 offset:41480\n"
"v_cvt_pk_bf16_f32 v178, v248, v249\n"
"v_mfma_f32_16x16x32_bf16 v[60:63], a[184:187], v[152:155], v[60:63]\n"
"v_exp_f32 v250, v250\n"
"v_mfma_f32_16x16x32_bf16 v[60:63], a[188:191], v[156:159], v[60:63]\n"
"ds_read_b64_tr_b16 a[174:175], v217 offset:45576\n"
"v_accvgpr_write_b32 a78, v232\n"
"v_mfma_f32_16x16x32_bf16 v[56:59], a[180:183], v[156:159], v[56:59]\n"
"s_add_u32 m0, s64, 16384\n"
"v_exp_f32 v251, v251\n"
"v_mfma_f32_16x16x32_bf16 v[56:59], a[176:179], v[152:155], v[56:59]\n"
"global_load_lds_dwordx4 v220, s[48:49] offset:3072\n"
"v_cvt_pk_bf16_f32 v179, v250, v251\n"
"v_mfma_f32_16x16x32_bf16 v[192:195], a[128:131], a[0:3], -4.0\n"
"s_waitcnt lgkmcnt(5)\n"
"v_exp_f32 v252, v252\n"
"v_mfma_f32_16x16x32_bf16 v[192:195], a[132:135], a[4:7], v[192:195]\n"
"ds_read_b128 a[144:147], v216 offset:61440\n"
"v_accvgpr_write_b32 a79, v234\n"
"v_mfma_f32_16x16x32_bf16 v[192:195], a[136:139], a[8:11], v[192:195]\n"
"v_exp_f32 v253, v253\n"
"v_mfma_f32_16x16x32_bf16 v[192:195], a[140:143], a[12:15], v[192:195]\n"
"ds_read_b128 a[148:151], v216 offset:61696\n"
"v_cvt_pk_bf16_f32 v186, v252, v253\n"
"v_mfma_f32_16x16x32_bf16 v[196:199], a[140:143], a[28:31], -4.0\n"
"v_exp_f32 v254, v254\n"
"v_mfma_f32_16x16x32_bf16 v[196:199], a[136:139], a[24:27], v[196:199]\n"
"ds_read_b128 a[152:155], v216 offset:61952\n"
"v_lshlrev_b32 v228, 16, v204\n"
"v_mfma_f32_16x16x32_bf16 v[196:199], a[132:135], a[20:23], v[196:199]\n"
"s_add_u32 m0, s66, 0\n"
"v_exp_f32 v255, v255\n"
"v_mfma_f32_16x16x32_bf16 v[196:199], a[128:131], a[16:19], v[196:199]\n"
"global_load_lds_dwordx4 v220, s[52:53] offset:3072\n"
"v_cvt_pk_bf16_f32 v187, v254, v255\n"
"v_mfma_f32_16x16x32_bf16 v[248:251], a[128:131], a[32:35], -4.0\n"
"v_exp_f32 v192, v192\n"
"v_mfma_f32_16x16x32_bf16 v[248:251], a[132:135], a[36:39], v[248:251]\n"
"ds_read_b128 a[156:159], v216 offset:62208\n"
"v_and_b32 v229, s67, v204\n"
"v_mfma_f32_16x16x32_bf16 v[248:251], a[136:139], a[40:43], v[248:251]\n"
"v_exp_f32 v193, v193\n"
"v_mfma_f32_16x16x32_bf16 v[248:251], a[140:143], a[44:47], v[248:251]\n"
"ds_read_b64_tr_b16 a[176:177], v217 offset:33536\n"
"v_cvt_pk_bf16_f32 v164, v192, v193\n"
"v_mfma_f32_16x16x32_bf16 v[252:255], a[140:143], a[60:63], -4.0\n"
"v_exp_f32 v194, v194\n"
"v_mfma_f32_16x16x32_bf16 v[252:255], a[136:139], a[56:59], v[252:255]\n"
"ds_read_b64_tr_b16 a[178:179], v217 offset:37632\n"
"v_lshlrev_b32 v230, 16, v205\n"
"v_mfma_f32_16x16x32_bf16 v[252:255], a[132:135], a[52:55], v[252:255]\n"
"v_exp_f32 v195, v195\n"
"v_mfma_f32_16x16x32_bf16 v[252:255], a[128:131], a[48:51], v[252:255]\n"
"ds_read_b128 v[200:203], v218 offset:4608\n"
"v_cvt_pk_bf16_f32 v165, v194, v195\n"
"v_mfma_f32_16x16x32_bf16 v[64:67], a[160:163], v[128:131], v[64:67]\n"
"s_waitcnt lgkmcnt(7)\n"
"v_exp_f32 v196, v196\n"
"v_mfma_f32_16x16x32_bf16 v[64:67], a[164:167], v[132:135], v[64:67]\n"
"ds_read_b64_tr_b16 a[180:181], v217 offset:41728\n"
"v_and_b32 v231, s67, v205\n"
"v_mfma_f32_16x16x32_bf16 v[68:71], a[172:175], v[132:135], v[68:71]\n"
"v_exp_f32 v197, v197\n"
"v_mfma_f32_16x16x32_bf16 v[68:71], a[168:171], v[128:131], v[68:71]\n"
"ds_read_b64_tr_b16 a[182:183], v217 offset:45824\n"
"v_cvt_pk_bf16_f32 v172, v196, v197\n"
"v_mfma_f32_16x16x32_bf16 v[76:79], a[168:171], v[136:139], v[76:79]\n"
"v_exp_f32 v198, v198\n"
"v_mfma_f32_16x16x32_bf16 v[76:79], a[172:175], v[140:143], v[76:79]\n"
"ds_read_b64_tr_b16 a[184:185], v217 offset:33544\n"
"v_lshlrev_b32 v232, 16, v206\n"
"v_mfma_f32_16x16x32_bf16 v[72:75], a[164:167], v[140:143], v[72:75]\n"
"v_exp_f32 v199, v199\n"
"v_mfma_f32_16x16x32_bf16 v[72:75], a[160:163], v[136:139], v[72:75]\n"
"v_cvt_pk_bf16_f32 v173, v198, v199\n"
"v_and_b32 v233, s67, v206\n"
"v_mfma_f32_16x16x32_bf16 v[80:83], a[160:163], v[144:147], v[80:83]\n"
"v_exp_f32 v248, v248\n"
"v_mfma_f32_16x16x32_bf16 v[80:83], a[164:167], v[148:151], v[80:83]\n"
"ds_read_b64_tr_b16 a[186:187], v217 offset:37640\n"
"v_lshlrev_b32 v234, 16, v207\n"
"v_mfma_f32_16x16x32_bf16 v[84:87], a[172:175], v[148:151], v[84:87]\n"
"v_exp_f32 v249, v249\n"
"v_mfma_f32_16x16x32_bf16 v[84:87], a[168:171], v[144:147], v[84:87]\n"
"ds_read_b64_tr_b16 a[188:189], v217 offset:41736\n"
"v_cvt_pk_bf16_f32 v180, v248, v249\n"
"v_and_b32 v235, s67, v207\n"
"v_mul_f32 v228, s68, v228\n"
"v_mul_f32 v229, s68, v229\n"
"v_pk_mul_f32 v[230:231], v[230:231], s[68:69]\n"
"v_pk_mul_f32 v[232:233], v[232:233], s[68:69]\n"
"v_pk_mul_f32 v[234:235], v[234:235], s[68:69]\n"
"v_cvt_pk_bf16_f32 v228, v228, v229\n"
"v_cvt_pk_bf16_f32 v230, v230, v231\n"
"v_mfma_f32_16x16x32_bf16 v[92:95], a[168:171], v[152:155], v[92:95]\n"
"v_exp_f32 v250, v250\n"
"v_mfma_f32_16x16x32_bf16 v[92:95], a[172:175], v[156:159], v[92:95]\n"
"ds_read_b64_tr_b16 a[190:191], v217 offset:45832\n"
"v_cvt_pk_bf16_f32 v232, v232, v233\n"
"v_mfma_f32_16x16x32_bf16 v[88:91], a[164:167], v[156:159], v[88:91]\n"
"v_exp_f32 v251, v251\n"
"v_mfma_f32_16x16x32_bf16 v[88:91], a[160:163], v[152:155], v[88:91]\n"
"s_waitcnt vmcnt(14) lgkmcnt(5)\n"
"v_cvt_pk_bf16_f32 v181, v250, v251\n"
"v_cvt_pk_bf16_f32 v234, v234, v235\n"
"v_mfma_f32_16x16x32_bf16 v[192:195], a[144:147], a[0:3], -4.0\n"
"s_barrier\n"
"v_exp_f32 v252, v252\n"
"v_mfma_f32_16x16x32_bf16 v[192:195], a[148:151], a[4:7], v[192:195]\n"
"s_bfe_u32 s50, s38, 0x30014\n"
"ds_read_b128 a[128:131], v216 offset:0\n"
"v_accvgpr_write_b32 a80, v228\n"
"v_mfma_f32_16x16x32_bf16 v[192:195], a[152:155], a[8:11], v[192:195]\n"
"s_lshl_b32 s50, s50, 14\n"
"v_exp_f32 v253, v253\n"
"v_mfma_f32_16x16x32_bf16 v[192:195], a[156:159], a[12:15], v[192:195]\n"
"s_add_u32 s39, s54, s50\n"
"ds_read_b128 a[132:135], v216 offset:256\n"
"v_cvt_pk_bf16_f32 v188, v252, v253\n"
"v_mfma_f32_16x16x32_bf16 v[196:199], a[156:159], a[28:31], -4.0\n"
"s_add_u32 s50, s44, s39\n"
"v_exp_f32 v254, v254\n"
"v_mfma_f32_16x16x32_bf16 v[196:199], a[152:155], a[24:27], v[196:199]\n"
"s_addc_u32 s51, s45, 0\n"
"ds_read_b128 a[136:139], v216 offset:512\n"
"v_accvgpr_write_b32 a81, v230\n"
"v_mfma_f32_16x16x32_bf16 v[196:199], a[148:151], a[20:23], v[196:199]\n"
"s_add_u32 m0, s65, 16384\n"
"v_exp_f32 v255, v255\n"
"v_mfma_f32_16x16x32_bf16 v[196:199], a[144:147], a[16:19], v[196:199]\n"
"global_load_lds_dwordx4 v219, s[50:51] offset:0\n"
"v_cvt_pk_bf16_f32 v189, v254, v255\n"
"v_mfma_f32_16x16x32_bf16 v[248:251], a[144:147], a[32:35], -4.0\n"
"v_exp_f32 v192, v192\n"
"v_mfma_f32_16x16x32_bf16 v[248:251], a[148:151], a[36:39], v[248:251]\n"
"ds_read_b128 a[140:143], v216 offset:768\n"
"v_accvgpr_write_b32 a82, v232\n"
"v_mfma_f32_16x16x32_bf16 v[248:251], a[152:155], a[40:43], v[248:251]\n"
"v_exp_f32 v193, v193\n"
"v_mfma_f32_16x16x32_bf16 v[248:251], a[156:159], a[44:47], v[248:251]\n"
"ds_read_b64_tr_b16 a[160:161], v217 offset:49152\n"
"buffer_store_dwordx4 a[244:247], v223, s[56:59], s37 offen offset:2048 sc1 nt\n"
"v_cvt_pk_bf16_f32 v166, v192, v193\n"
"v_accvgpr_write_b32 a83, v234\n"
"v_mfma_f32_16x16x32_bf16 v[252:255], a[156:159], a[60:63], -4.0\n"
"v_exp_f32 v194, v194\n"
"v_mfma_f32_16x16x32_bf16 v[252:255], a[152:155], a[56:59], v[252:255]\n"
"ds_read_b64_tr_b16 a[162:163], v217 offset:53248\n"
"v_lshlrev_b32 v228, 16, v244\n"
"v_mfma_f32_16x16x32_bf16 v[252:255], a[148:151], a[52:55], v[252:255]\n"
"v_exp_f32 v195, v195\n"
"v_mfma_f32_16x16x32_bf16 v[252:255], a[144:147], a[48:51], v[252:255]\n"
"ds_read_b128 v[204:207], v218 offset:4864\n"
"v_cvt_pk_bf16_f32 v167, v194, v195\n"
"v_mfma_f32_16x16x32_bf16 v[96:99], a[176:179], v[128:131], v[96:99]\n"
"s_waitcnt lgkmcnt(6)\n"
"v_exp_f32 v196, v196\n"
"v_mfma_f32_16x16x32_bf16 v[96:99], a[180:183], v[132:135], v[96:99]\n"
"ds_read_b64_tr_b16 a[164:165], v217 offset:57344\n"
"v_and_b32 v229, s67, v244\n"
"v_mfma_f32_16x16x32_bf16 v[100:103], a[188:191], v[132:135], v[100:103]\n"
"v_exp_f32 v197, v197\n"
"v_mfma_f32_16x16x32_bf16 v[100:103], a[184:187], v[128:131], v[100:103]\n"
"ds_read_b64_tr_b16 a[166:167], v217 offset:61440\n"
"v_cvt_pk_bf16_f32 v174, v196, v197\n"
"v_mfma_f32_16x16x32_bf16 v[108:111], a[184:187], v[136:139], v[108:111]\n"
"v_exp_f32 v198, v198\n"
"v_mfma_f32_16x16x32_bf16 v[108:111], a[188:191], v[140:143], v[108:111]\n"
"ds_read_b64_tr_b16 a[168:169], v217 offset:49160\n"
"v_lshlrev_b32 v230, 16, v245\n"
"v_mfma_f32_16x16x32_bf16 v[104:107], a[180:183], v[140:143], v[104:107]\n"
"s_add_u32 m0, s65, 16384\n"
"v_exp_f32 v199, v199\n"
"v_mfma_f32_16x16x32_bf16 v[104:107], a[176:179], v[136:139], v[104:107]\n"
"global_load_lds_dwordx4 v219, s[50:51] offset:1024\n"
"v_cvt_pk_bf16_f32 v175, v198, v199\n"
"v_mfma_f32_16x16x32_bf16 v[112:115], a[176:179], v[144:147], v[112:115]\n"
"v_exp_f32 v248, v248\n"
"v_mfma_f32_16x16x32_bf16 v[112:115], a[180:183], v[148:151], v[112:115]\n"
"ds_read_b64_tr_b16 a[170:171], v217 offset:53256\n"
"v_and_b32 v231, s67, v245\n"
"v_mfma_f32_16x16x32_bf16 v[116:119], a[188:191], v[148:151], v[116:119]\n"
"v_exp_f32 v249, v249\n"
"v_mfma_f32_16x16x32_bf16 v[116:119], a[184:187], v[144:147], v[116:119]\n"
"ds_read_b64_tr_b16 a[172:173], v217 offset:57352\n"
"v_cvt_pk_bf16_f32 v182, v248, v249\n"
"v_mfma_f32_16x16x32_bf16 v[124:127], a[184:187], v[152:155], v[124:127]\n"
"v_exp_f32 v250, v250\n"
"v_mfma_f32_16x16x32_bf16 v[124:127], a[188:191], v[156:159], v[124:127]\n"
"ds_read_b64_tr_b16 a[174:175], v217 offset:61448\n"
"v_lshlrev_b32 v232, 16, v246\n"
"v_and_b32 v233, s67, v246\n"
"v_lshlrev_b32 v234, 16, v247\n"
"v_and_b32 v235, s67, v247\n"
"v_pk_mul_f32 v[228:229], v[228:229], s[68:69]\n"
"v_pk_mul_f32 v[230:231], v[230:231], s[68:69]\n"
"v_pk_mul_f32 v[232:233], v[232:233], s[68:69]\n"
"v_pk_mul_f32 v[234:235], v[234:235], s[68:69]\n"
"v_cvt_pk_bf16_f32 v228, v228, v229\n"
"v_cvt_pk_bf16_f32 v230, v230, v231\n"
"v_mfma_f32_16x16x32_bf16 v[120:123], a[180:183], v[156:159], v[120:123]\n"
"s_add_u32 m0, s65, 16384\n"
"v_exp_f32 v251, v251\n"
"v_mfma_f32_16x16x32_bf16 v[120:123], a[176:179], v[152:155], v[120:123]\n"
"global_load_lds_dwordx4 v219, s[50:51] offset:2048\n"
"v_cvt_pk_bf16_f32 v183, v250, v251\n"
"v_mfma_f32_16x16x32_bf16 v[192:195], a[128:131], a[0:3], -4.0\n"
"s_waitcnt lgkmcnt(4)\n"
"v_exp_f32 v252, v252\n"
"v_mfma_f32_16x16x32_bf16 v[192:195], a[132:135], a[4:7], v[192:195]\n"
"ds_read_b128 a[144:147], v216 offset:4096\n"
"v_cvt_pk_bf16_f32 v232, v232, v233\n"
"v_mfma_f32_16x16x32_bf16 v[192:195], a[136:139], a[8:11], v[192:195]\n"
"v_exp_f32 v253, v253\n"
"v_mfma_f32_16x16x32_bf16 v[192:195], a[140:143], a[12:15], v[192:195]\n"
"s_add_u32 s35, s55, s37\n"
"ds_read_b128 a[148:151], v216 offset:4352\n"
"v_cvt_pk_bf16_f32 v190, v252, v253\n"
"v_mfma_f32_16x16x32_bf16 v[196:199], a[140:143], a[28:31], -4.0\n"
"s_add_u32 s52, s40, s35\n"
"v_exp_f32 v254, v254\n"
"v_mfma_f32_16x16x32_bf16 v[196:199], a[136:139], a[24:27], v[196:199]\n"
"s_addc_u32 s53, s41, 0\n"
"ds_read_b128 a[152:155], v216 offset:4608\n"
"v_cvt_pk_bf16_f32 v234, v234, v235\n"
"v_mfma_f32_16x16x32_bf16 v[196:199], a[132:135], a[20:23], v[196:199]\n"
"s_add_u32 m0, s66, 4096\n"
"v_exp_f32 v255, v255\n"
"v_mfma_f32_16x16x32_bf16 v[196:199], a[128:131], a[16:19], v[196:199]\n"
"global_load_lds_dwordx4 v219, s[52:53] offset:0\n"
"v_cvt_pk_bf16_f32 v191, v254, v255\n"
"v_mfma_f32_16x16x32_bf16 v[248:251], a[128:131], a[32:35], -4.0\n"
"v_exp_f32 v192, v192\n"
"v_mfma_f32_16x16x32_bf16 v[248:251], a[132:135], a[36:39], v[248:251]\n"
"ds_read_b128 a[156:159], v216 offset:4864\n"
"v_accvgpr_write_b32 a84, v228\n"
"v_mfma_f32_16x16x32_bf16 v[248:251], a[136:139], a[40:43], v[248:251]\n"
"v_exp_f32 v193, v193\n"
"v_mfma_f32_16x16x32_bf16 v[248:251], a[140:143], a[44:47], v[248:251]\n"
"ds_read_b64_tr_b16 a[176:177], v217 offset:49408\n"
"v_cvt_pk_bf16_f32 v128, v192, v193\n"
"v_mfma_f32_16x16x32_bf16 v[252:255], a[140:143], a[60:63], -4.0\n"
"v_exp_f32 v194, v194\n"
"v_mfma_f32_16x16x32_bf16 v[252:255], a[136:139], a[56:59], v[252:255]\n"
"ds_read_b64_tr_b16 a[178:179], v217 offset:53504\n"
"v_accvgpr_write_b32 a85, v230\n"
"v_mfma_f32_16x16x32_bf16 v[252:255], a[132:135], a[52:55], v[252:255]\n"
"s_add_u32 m0, s65, 16384\n"
"v_exp_f32 v195, v195\n"
"v_mfma_f32_16x16x32_bf16 v[252:255], a[128:131], a[48:51], v[252:255]\n"
"global_load_lds_dwordx4 v219, s[50:51] offset:3072\n"
"v_cvt_pk_bf16_f32 v129, v194, v195\n"
"v_smfmac_f32_16x16x64_bf16 a[192:195], a[208:211], v[160:167], v215\n"
"v_accvgpr_write_b32 a86, v232\n"
"v_smfmac_f32_16x16x64_bf16 a[196:199], a[208:211], v[168:175], v215\n"
"ds_read_b64_tr_b16 a[180:181], v217 offset:57600\n"
"v_smfmac_f32_16x16x64_bf16 a[200:203], a[208:211], v[176:183], v215\n"
"v_accvgpr_write_b32 a87, v234\n"
"v_smfmac_f32_16x16x64_bf16 a[204:207], a[208:211], v[184:191], v215\n"
"ds_read_b64_tr_b16 a[182:183], v217 offset:61696\n"
"v_mfma_f32_16x16x32_bf16 v[0:3], a[160:163], v[160:163], v[0:3]\n"
"v_exp_f32 v196, v196\n"
"v_mfma_f32_16x16x32_bf16 v[0:3], a[164:167], v[164:167], v[0:3]\n"
"s_waitcnt lgkmcnt(5)\n"
"ds_read_b64_tr_b16 a[184:185], v217 offset:49416\n"
"v_lshlrev_b32 v228, 16, v200\n"
"v_mfma_f32_16x16x32_bf16 v[4:7], a[172:175], v[164:167], v[4:7]\n"
"v_exp_f32 v197, v197\n"
"v_mfma_f32_16x16x32_bf16 v[4:7], a[168:171], v[160:163], v[4:7]\n"
"v_cvt_pk_bf16_f32 v136, v196, v197\n"
"v_and_b32 v229, s67, v200\n"
"v_mfma_f32_16x16x32_bf16 v[12:15], a[168:171], v[168:171], v[12:15]\n"
"v_exp_f32 v198, v198\n"
"v_mfma_f32_16x16x32_bf16 v[12:15], a[172:175], v[172:175], v[12:15]\n"
"s_bfe_u32 s48, s38, 0x30018\n"
"ds_read_b64_tr_b16 a[186:187], v217 offset:53512\n"
"v_lshlrev_b32 v230, 16, v201\n"
"v_mfma_f32_16x16x32_bf16 v[8:11], a[164:167], v[172:175], v[8:11]\n"
"s_lshl_b32 s48, s48, 14\n"
"v_exp_f32 v199, v199\n"
"v_mfma_f32_16x16x32_bf16 v[8:11], a[160:163], v[168:171], v[8:11]\n"
"s_add_u32 s36, s79, s48\n"
"ds_read_b64_tr_b16 a[188:189], v217 offset:57608\n"
"v_cvt_pk_bf16_f32 v137, v198, v199\n"
"v_mfma_f32_16x16x32_bf16 v[16:19], a[160:163], v[176:179], v[16:19]\n"
"s_add_u32 s48, s42, s36\n"
"v_exp_f32 v248, v248\n"
"v_mfma_f32_16x16x32_bf16 v[16:19], a[164:167], v[180:183], v[16:19]\n"
"s_addc_u32 s49, s43, 0\n"
"ds_read_b64_tr_b16 a[190:191], v217 offset:61704\n"
"v_and_b32 v231, s67, v201\n"
"v_mfma_f32_16x16x32_bf16 v[20:23], a[172:175], v[180:183], v[20:23]\n"
"s_add_u32 m0, s64, 32768\n"
"v_exp_f32 v249, v249\n"
"v_mfma_f32_16x16x32_bf16 v[20:23], a[168:171], v[176:179], v[20:23]\n"
"global_load_lds_dwordx4 v219, s[48:49] offset:0\n"
"v_cvt_pk_bf16_f32 v144, v248, v249\n"
"v_mfma_f32_16x16x32_bf16 v[28:31], a[168:171], v[184:187], v[28:31]\n"
"v_exp_f32 v250, v250\n"
"v_mfma_f32_16x16x32_bf16 v[28:31], a[172:175], v[188:191], v[28:31]\n"
"buffer_store_dwordx4 a[248:251], v223, s[56:59], s99 offen offset:128 sc1 nt\n"
"v_lshlrev_b32 v232, 16, v202\n"
"v_and_b32 v233, s67, v202\n"
"v_lshlrev_b32 v234, 16, v203\n"
"v_mfma_f32_16x16x32_bf16 v[24:27], a[164:167], v[188:191], v[24:27]\n"
"v_exp_f32 v251, v251\n"
"v_mfma_f32_16x16x32_bf16 v[24:27], a[160:163], v[184:187], v[24:27]\n"
"v_cvt_pk_bf16_f32 v145, v250, v251\n"
"v_and_b32 v235, s67, v203\n"
"v_mfma_f32_16x16x32_bf16 v[192:195], a[144:147], a[0:3], -4.0\n"
"v_exp_f32 v252, v252\n"
"v_mfma_f32_16x16x32_bf16 v[192:195], a[148:151], a[4:7], v[192:195]\n"
"ds_read_b128 a[128:131], v216 offset:8192\n"
"v_mul_f32 v228, s68, v228\n"
"v_mfma_f32_16x16x32_bf16 v[192:195], a[152:155], a[8:11], v[192:195]\n"
"s_waitcnt lgkmcnt(4)\n"
"v_exp_f32 v253, v253\n"
"v_mfma_f32_16x16x32_bf16 v[192:195], a[156:159], a[12:15], v[192:195]\n"
"ds_read_b128 a[132:135], v216 offset:8448\n"
"v_cvt_pk_bf16_f32 v152, v252, v253\n"
"v_mfma_f32_16x16x32_bf16 v[196:199], a[156:159], a[28:31], -4.0\n"
"v_exp_f32 v254, v254\n"
"v_mfma_f32_16x16x32_bf16 v[196:199], a[152:155], a[24:27], v[196:199]\n"
"ds_read_b128 a[136:139], v216 offset:8704\n"
"v_mul_f32 v229, s68, v229\n"
"v_mfma_f32_16x16x32_bf16 v[196:199], a[148:151], a[20:23], v[196:199]\n"
"s_add_u32 m0, s64, 32768\n"
"v_exp_f32 v255, v255\n"
"v_mfma_f32_16x16x32_bf16 v[196:199], a[144:147], a[16:19], v[196:199]\n"
"global_load_lds_dwordx4 v219, s[48:49] offset:1024\n"
"v_cvt_pk_bf16_f32 v153, v254, v255\n"
"v_mfma_f32_16x16x32_bf16 v[248:251], a[144:147], a[32:35], -4.0\n"
"v_exp_f32 v192, v192\n"
"v_mfma_f32_16x16x32_bf16 v[248:251], a[148:151], a[36:39], v[248:251]\n"
"ds_read_b128 a[140:143], v216 offset:8960\n"
"v_mul_f32 v230, s68, v230\n"
"v_mfma_f32_16x16x32_bf16 v[248:251], a[152:155], a[40:43], v[248:251]\n"
"v_exp_f32 v193, v193\n"
"v_mfma_f32_16x16x32_bf16 v[248:251], a[156:159], a[44:47], v[248:251]\n"
"ds_read_b64_tr_b16 a[160:161], v217 offset:49664\n"
"v_cvt_pk_bf16_f32 v130, v192, v193\n"
"v_mfma_f32_16x16x32_bf16 v[252:255], a[156:159], a[60:63], -4.0\n"
"v_exp_f32 v194, v194\n"
"v_mfma_f32_16x16x32_bf16 v[252:255], a[152:155], a[56:59], v[252:255]\n"
"ds_read_b64_tr_b16 a[162:163], v217 offset:53760\n"
"v_mul_f32 v231, s68, v231\n"
"v_mfma_f32_16x16x32_bf16 v[252:255], a[148:151], a[52:55], v[252:255]\n"
"s_add_u32 m0, s64, 32768\n"
"v_exp_f32 v195, v195\n"
"v_mfma_f32_16x16x32_bf16 v[252:255], a[144:147], a[48:51], v[252:255]\n"
"global_load_lds_dwordx4 v220, s[48:49] offset:2048\n"
"v_cvt_pk_bf16_f32 v131, v194, v195\n"
"v_mfma_f32_16x16x32_bf16 v[32:35], a[176:179], v[160:163], v[32:35]\n"
"v_exp_f32 v196, v196\n"
"v_mfma_f32_16x16x32_bf16 v[32:35], a[180:183], v[164:167], v[32:35]\n"
"s_waitcnt lgkmcnt(5)\n"
"ds_read_b64_tr_b16 a[164:165], v217 offset:57856\n"
"v_mul_f32 v232, s68, v232\n"
"v_mul_f32 v233, s68, v233\n"
"v_mul_f32 v234, s68, v234\n"
"v_mul_f32 v235, s68, v235\n"
"v_mfma_f32_16x16x32_bf16 v[36:39], a[188:191], v[164:167], v[36:39]\n"
"v_exp_f32 v197, v197\n"
"v_mfma_f32_16x16x32_bf16 v[36:39], a[184:187], v[160:163], v[36:39]\n"
"ds_read_b64_tr_b16 a[166:167], v217 offset:61952\n"
"v_cvt_pk_bf16_f32 v138, v196, v197\n"
"v_mfma_f32_16x16x32_bf16 v[44:47], a[184:187], v[168:171], v[44:47]\n"
"v_exp_f32 v198, v198\n"
"v_mfma_f32_16x16x32_bf16 v[44:47], a[188:191], v[172:175], v[44:47]\n"
"ds_read_b64_tr_b16 a[168:169], v217 offset:49672\n"
"v_cvt_pk_bf16_f32 v228, v228, v229\n"
"v_mfma_f32_16x16x32_bf16 v[40:43], a[180:183], v[172:175], v[40:43]\n"
"s_add_u32 m0, s66, 4096\n"
"v_exp_f32 v199, v199\n"
"v_mfma_f32_16x16x32_bf16 v[40:43], a[176:179], v[168:171], v[40:43]\n"
"global_load_lds_dwordx4 v219, s[52:53] offset:1024\n"
"v_cvt_pk_bf16_f32 v139, v198, v199\n"
"v_mfma_f32_16x16x32_bf16 v[48:51], a[176:179], v[176:179], v[48:51]\n"
"v_exp_f32 v248, v248\n"
"v_mfma_f32_16x16x32_bf16 v[48:51], a[180:183], v[180:183], v[48:51]\n"
"ds_read_b64_tr_b16 a[170:171], v217 offset:53768\n"
"v_cvt_pk_bf16_f32 v230, v230, v231\n"
"v_mfma_f32_16x16x32_bf16 v[52:55], a[188:191], v[180:183], v[52:55]\n"
"v_exp_f32 v249, v249\n"
"v_mfma_f32_16x16x32_bf16 v[52:55], a[184:187], v[176:179], v[52:55]\n"
"ds_read_b64_tr_b16 a[172:173], v217 offset:57864\n"
"v_cvt_pk_bf16_f32 v146, v248, v249\n"
"v_mfma_f32_16x16x32_bf16 v[60:63], a[184:187], v[184:187], v[60:63]\n"
"v_exp_f32 v250, v250\n"
"v_mfma_f32_16x16x32_bf16 v[60:63], a[188:191], v[188:191], v[60:63]\n"
"ds_read_b64_tr_b16 a[174:175], v217 offset:61960\n"
"v_cvt_pk_bf16_f32 v232, v232, v233\n"
"v_mfma_f32_16x16x32_bf16 v[56:59], a[180:183], v[188:191], v[56:59]\n"
"s_add_u32 m0, s64, 32768\n"
"v_exp_f32 v251, v251\n"
"v_mfma_f32_16x16x32_bf16 v[56:59], a[176:179], v[184:187], v[56:59]\n"
"global_load_lds_dwordx4 v220, s[48:49] offset:3072\n"
"v_cvt_pk_bf16_f32 v147, v250, v251\n"
"v_mfma_f32_16x16x32_bf16 v[192:195], a[128:131], a[0:3], -4.0\n"
"s_waitcnt lgkmcnt(5)\n"
"v_exp_f32 v252, v252\n"
"v_mfma_f32_16x16x32_bf16 v[192:195], a[132:135], a[4:7], v[192:195]\n"
"ds_read_b128 a[144:147], v216 offset:12288\n"
"v_cvt_pk_bf16_f32 v234, v234, v235\n"
"v_mfma_f32_16x16x32_bf16 v[192:195], a[136:139], a[8:11], v[192:195]\n"
"v_exp_f32 v253, v253\n"
"v_mfma_f32_16x16x32_bf16 v[192:195], a[140:143], a[12:15], v[192:195]\n"
"ds_read_b128 a[148:151], v216 offset:12544\n"
"v_cvt_pk_bf16_f32 v154, v252, v253\n"
"v_mfma_f32_16x16x32_bf16 v[196:199], a[140:143], a[28:31], -4.0\n"
"v_exp_f32 v254, v254\n"
"v_mfma_f32_16x16x32_bf16 v[196:199], a[136:139], a[24:27], v[196:199]\n"
"ds_read_b128 a[152:155], v216 offset:12800\n"
"v_accvgpr_write_b32 a88, v228\n"
"v_mfma_f32_16x16x32_bf16 v[196:199], a[132:135], a[20:23], v[196:199]\n"
"v_exp_f32 v255, v255\n"
"v_mfma_f32_16x16x32_bf16 v[196:199], a[128:131], a[16:19], v[196:199]\n"
"v_cvt_pk_bf16_f32 v155, v254, v255\n"
"v_accvgpr_write_b32 a89, v230\n"
"v_mfma_f32_16x16x32_bf16 v[248:251], a[128:131], a[32:35], -4.0\n"
"v_exp_f32 v192, v192\n"
"v_mfma_f32_16x16x32_bf16 v[248:251], a[132:135], a[36:39], v[248:251]\n"
"ds_read_b128 a[156:159], v216 offset:13056\n"
"v_accvgpr_write_b32 a90, v232\n"
"v_mfma_f32_16x16x32_bf16 v[248:251], a[136:139], a[40:43], v[248:251]\n"
"v_exp_f32 v193, v193\n"
"v_mfma_f32_16x16x32_bf16 v[248:251], a[140:143], a[44:47], v[248:251]\n"
"ds_read_b64_tr_b16 a[176:177], v217 offset:49920\n"
"v_cvt_pk_bf16_f32 v132, v192, v193\n"
"v_mfma_f32_16x16x32_bf16 v[252:255], a[140:143], a[60:63], -4.0\n"
"v_exp_f32 v194, v194\n"
"v_mfma_f32_16x16x32_bf16 v[252:255], a[136:139], a[56:59], v[252:255]\n"
"ds_read_b64_tr_b16 a[178:179], v217 offset:54016\n"
"v_accvgpr_write_b32 a91, v234\n"
"v_mfma_f32_16x16x32_bf16 v[252:255], a[132:135], a[52:55], v[252:255]\n"
"v_exp_f32 v195, v195\n"
"v_mfma_f32_16x16x32_bf16 v[252:255], a[128:131], a[48:51], v[252:255]\n"
"v_cvt_pk_bf16_f32 v133, v194, v195\n"
"v_lshlrev_b32 v228, 16, v204\n"
"v_mfma_f32_16x16x32_bf16 v[64:67], a[160:163], v[160:163], v[64:67]\n"
"s_waitcnt lgkmcnt(6)\n"
"v_exp_f32 v196, v196\n"
"v_mfma_f32_16x16x32_bf16 v[64:67], a[164:167], v[164:167], v[64:67]\n"
"ds_read_b64_tr_b16 a[180:181], v217 offset:58112\n"
"v_and_b32 v229, s67, v204\n"
"v_mfma_f32_16x16x32_bf16 v[68:71], a[172:175], v[164:167], v[68:71]\n"
"v_exp_f32 v197, v197\n"
"v_mfma_f32_16x16x32_bf16 v[68:71], a[168:171], v[160:163], v[68:71]\n"
"ds_read_b64_tr_b16 a[182:183], v217 offset:62208\n"
"v_cvt_pk_bf16_f32 v140, v196, v197\n"
"v_mfma_f32_16x16x32_bf16 v[76:79], a[168:171], v[168:171], v[76:79]\n"
"v_exp_f32 v198, v198\n"
"v_mfma_f32_16x16x32_bf16 v[76:79], a[172:175], v[172:175], v[76:79]\n"
"ds_read_b64_tr_b16 a[184:185], v217 offset:49928\n"
"v_lshlrev_b32 v230, 16, v205\n"
"v_mfma_f32_16x16x32_bf16 v[72:75], a[164:167], v[172:175], v[72:75]\n"
"s_add_u32 m0, s66, 4096\n"
"v_exp_f32 v199, v199\n"
"v_mfma_f32_16x16x32_bf16 v[72:75], a[160:163], v[168:171], v[72:75]\n"
"global_load_lds_dwordx4 v220, s[52:53] offset:2048\n"
"v_cvt_pk_bf16_f32 v141, v198, v199\n"
"v_mfma_f32_16x16x32_bf16 v[80:83], a[160:163], v[176:179], v[80:83]\n"
"v_exp_f32 v248, v248\n"
"v_mfma_f32_16x16x32_bf16 v[80:83], a[164:167], v[180:183], v[80:83]\n"
"ds_read_b64_tr_b16 a[186:187], v217 offset:54024\n"
"v_and_b32 v231, s67, v205\n"
"v_mfma_f32_16x16x32_bf16 v[84:87], a[172:175], v[180:183], v[84:87]\n"
"v_exp_f32 v249, v249\n"
"v_mfma_f32_16x16x32_bf16 v[84:87], a[168:171], v[176:179], v[84:87]\n"
"ds_read_b64_tr_b16 a[188:189], v217 offset:58120\n"
"v_cvt_pk_bf16_f32 v148, v248, v249\n"
"v_mfma_f32_16x16x32_bf16 v[92:95], a[168:171], v[184:187], v[92:95]\n"
"v_exp_f32 v250, v250\n"
"v_mfma_f32_16x16x32_bf16 v[92:95], a[172:175], v[188:191], v[92:95]\n"
"ds_read_b64_tr_b16 a[190:191], v217 offset:62216\n"
"v_lshlrev_b32 v232, 16, v206\n"
"v_mfma_f32_16x16x32_bf16 v[88:91], a[164:167], v[188:191], v[88:91]\n"
"v_exp_f32 v251, v251\n"
"v_mfma_f32_16x16x32_bf16 v[88:91], a[160:163], v[184:187], v[88:91]\n"
"s_waitcnt vmcnt(14) lgkmcnt(6)\n"
"buffer_store_dwordx4 a[252:255], v223, s[56:59], s99 offen offset:2176 sc1 nt\n"
"v_cvt_pk_bf16_f32 v149, v250, v251\n"
"v_and_b32 v233, s67, v206\n"
"v_lshlrev_b32 v234, 16, v207\n"
"v_mfma_f32_16x16x32_bf16 v[192:195], a[144:147], a[0:3], -4.0\n"
"s_barrier\n"
"v_exp_f32 v252, v252\n"
"v_mfma_f32_16x16x32_bf16 v[192:195], a[148:151], a[4:7], v[192:195]\n"
"s_bfe_u32 s50, s38, 0x30018\n"
"ds_read_b128 a[128:131], v216 offset:16384\n"
"v_and_b32 v235, s67, v207\n"
"v_mfma_f32_16x16x32_bf16 v[192:195], a[152:155], a[8:11], v[192:195]\n"
"s_lshl_b32 s50, s50, 14\n"
"v_exp_f32 v253, v253\n"
"v_mfma_f32_16x16x32_bf16 v[192:195], a[156:159], a[12:15], v[192:195]\n"
"s_add_u32 s39, s54, s50\n"
"ds_read_b128 a[132:135], v216 offset:16640\n"
"v_cvt_pk_bf16_f32 v156, v252, v253\n"
"v_mul_f32 v228, s68, v228\n"
"v_mul_f32 v229, s68, v229\n"
"v_mul_f32 v230, s68, v230\n"
"v_mul_f32 v231, s68, v231\n"
"v_pk_mul_f32 v[232:233], v[232:233], s[68:69]\n"
"v_pk_mul_f32 v[234:235], v[234:235], s[68:69]\n"
"v_cvt_pk_bf16_f32 v228, v228, v229\n"
"v_cvt_pk_bf16_f32 v230, v230, v231\n"
"v_mfma_f32_16x16x32_bf16 v[196:199], a[156:159], a[28:31], -4.0\n"
"s_add_u32 s50, s44, s39\n"
"v_exp_f32 v254, v254\n"
"v_mfma_f32_16x16x32_bf16 v[196:199], a[152:155], a[24:27], v[196:199]\n"
"s_addc_u32 s51, s45, 0\n"
"ds_read_b128 a[136:139], v216 offset:16896\n"
"v_cvt_pk_bf16_f32 v232, v232, v233\n"
"v_mfma_f32_16x16x32_bf16 v[196:199], a[148:151], a[20:23], v[196:199]\n"
"s_add_u32 m0, s65, 32768\n"
"v_exp_f32 v255, v255\n"
"v_mfma_f32_16x16x32_bf16 v[196:199], a[144:147], a[16:19], v[196:199]\n"
"global_load_lds_dwordx4 v219, s[50:51] offset:0\n"
"v_cvt_pk_bf16_f32 v157, v254, v255\n"
"v_mfma_f32_16x16x32_bf16 v[248:251], a[144:147], a[32:35], -4.0\n"
"v_exp_f32 v192, v192\n"
"v_mfma_f32_16x16x32_bf16 v[248:251], a[148:151], a[36:39], v[248:251]\n"
"ds_read_b128 a[140:143], v216 offset:17152\n"
"v_cvt_pk_bf16_f32 v234, v234, v235\n"
"v_mfma_f32_16x16x32_bf16 v[248:251], a[152:155], a[40:43], v[248:251]\n"
"v_exp_f32 v193, v193\n"
"v_mfma_f32_16x16x32_bf16 v[248:251], a[156:159], a[44:47], v[248:251]\n"
".Lattn_pad8:\n"
"s_cbranch_vccnz .Lattn_ret\n"
"ds_read_b64_tr_b16 a[160:161], v217 offset:0\n"
"v_cvt_pk_bf16_f32 v134, v192, v193\n"
"v_mfma_f32_16x16x32_bf16 v[252:255], a[156:159], a[60:63], -4.0\n"
"v_exp_f32 v194, v194\n"
"v_mfma_f32_16x16x32_bf16 v[252:255], a[152:155], a[56:59], v[252:255]\n"
"ds_read_b64_tr_b16 a[162:163], v217 offset:4096\n"
"v_accvgpr_write_b32 a92, v228\n"
"v_mfma_f32_16x16x32_bf16 v[252:255], a[148:151], a[52:55], v[252:255]\n"
"v_exp_f32 v195, v195\n"
"v_mfma_f32_16x16x32_bf16 v[252:255], a[144:147], a[48:51], v[252:255]\n"
"s_waitcnt vmcnt(15)\n"
"ds_read_b128 v[244:247], v218 offset:0\n"
"v_cvt_pk_bf16_f32 v135, v194, v195\n"
"v_mfma_f32_16x16x32_bf16 v[96:99], a[176:179], v[160:163], v[96:99]\n"
"s_waitcnt lgkmcnt(5)\n"
"v_exp_f32 v196, v196\n"
"v_mfma_f32_16x16x32_bf16 v[96:99], a[180:183], v[164:167], v[96:99]\n"
"ds_read_b64_tr_b16 a[164:165], v217 offset:8192\n"
"ds_read_b128 v[200:203], v218 offset:256\n"
"v_mfma_f32_16x16x32_bf16 v[100:103], a[188:191], v[164:167], v[100:103]\n"
"v_exp_f32 v197, v197\n"
"v_mfma_f32_16x16x32_bf16 v[100:103], a[184:187], v[160:163], v[100:103]\n"
"ds_read_b64_tr_b16 a[166:167], v217 offset:12288\n"
"v_cvt_pk_bf16_f32 v142, v196, v197\n"
"v_mfma_f32_16x16x32_bf16 v[108:111], a[184:187], v[168:171], v[108:111]\n"
"v_exp_f32 v198, v198\n"
"v_mfma_f32_16x16x32_bf16 v[108:111], a[188:191], v[172:175], v[108:111]\n"
"ds_read_b64_tr_b16 a[168:169], v217 offset:8\n"
"v_accvgpr_write_b32 a93, v230\n"
"v_mfma_f32_16x16x32_bf16 v[104:107], a[180:183], v[172:175], v[104:107]\n"
"s_add_u32 m0, s65, 32768\n"
"v_exp_f32 v199, v199\n"
"v_mfma_f32_16x16x32_bf16 v[104:107], a[176:179], v[168:171], v[104:107]\n"
"global_load_lds_dwordx4 v219, s[50:51] offset:1024\n"
"v_cvt_pk_bf16_f32 v143, v198, v199\n"
"v_mfma_f32_16x16x32_bf16 v[112:115], a[176:179], v[176:179], v[112:115]\n"
"v_exp_f32 v248, v248\n"
"v_mfma_f32_16x16x32_bf16 v[112:115], a[180:183], v[180:183], v[112:115]\n"
"ds_read_b64_tr_b16 a[170:171], v217 offset:4104\n"
"v_accvgpr_write_b32 a94, v232\n"
"v_mfma_f32_16x16x32_bf16 v[116:119], a[188:191], v[180:183], v[116:119]\n"
"v_exp_f32 v249, v249\n"
"v_mfma_f32_16x16x32_bf16 v[116:119], a[184:187], v[176:179], v[116:119]\n"
"ds_read_b64_tr_b16 a[172:173], v217 offset:8200\n"
"v_cvt_pk_bf16_f32 v150, v248, v249\n"
"v_mfma_f32_16x16x32_bf16 v[124:127], a[184:187], v[184:187], v[124:127]\n"
"v_exp_f32 v250, v250\n"
"v_mfma_f32_16x16x32_bf16 v[124:127], a[188:191], v[188:191], v[124:127]\n"
"ds_read_b64_tr_b16 a[174:175], v217 offset:12296\n"
"v_accvgpr_write_b32 a95, v234\n"
"v_mfma_f32_16x16x32_bf16 v[120:123], a[180:183], v[188:191], v[120:123]\n"
"s_add_u32 m0, s65, 32768\n"
"v_exp_f32 v251, v251\n"
"v_mfma_f32_16x16x32_bf16 v[120:123], a[176:179], v[184:187], v[120:123]\n"
"global_load_lds_dwordx4 v219, s[50:51] offset:2048\n"
"v_cvt_pk_bf16_f32 v151, v250, v251\n"
"v_mfma_f32_16x16x32_bf16 v[192:195], a[128:131], a[0:3], -4.0\n"
"v_exp_f32 v252, v252\n"
"v_mfma_f32_16x16x32_bf16 v[192:195], a[132:135], a[4:7], v[192:195]\n"
"s_waitcnt lgkmcnt(5)\n"
"ds_read_b128 a[144:147], v216 offset:20480\n"
"v_lshlrev_b32 v228, 16, v244\n"
"v_mfma_f32_16x16x32_bf16 v[192:195], a[136:139], a[8:11], v[192:195]\n"
"v_exp_f32 v253, v253\n"
"v_mfma_f32_16x16x32_bf16 v[192:195], a[140:143], a[12:15], v[192:195]\n"
"ds_read_b128 a[148:151], v216 offset:20736\n"
"ds_read_b128 v[204:207], v218 offset:512\n"
"v_cvt_pk_bf16_f32 v158, v252, v253\n"
"v_mfma_f32_16x16x32_bf16 v[196:199], a[140:143], a[28:31], -4.0\n"
"v_exp_f32 v254, v254\n"
"v_mfma_f32_16x16x32_bf16 v[196:199], a[136:139], a[24:27], v[196:199]\n"
"ds_read_b128 a[152:155], v216 offset:20992\n"
"v_and_b32 v229, s67, v244\n"
"v_mfma_f32_16x16x32_bf16 v[196:199], a[132:135], a[20:23], v[196:199]\n"
"s_add_u32 m0, s66, 4096\n"
"v_exp_f32 v255, v255\n"
"v_mfma_f32_16x16x32_bf16 v[196:199], a[128:131], a[16:19], v[196:199]\n"
"global_load_lds_dwordx4 v220, s[52:53] offset:3072\n"
"v_cvt_pk_bf16_f32 v159, v254, v255\n"
"v_mfma_f32_16x16x32_bf16 v[248:251], a[128:131], a[32:35], -4.0\n"
"v_exp_f32 v192, v192\n"
"v_mfma_f32_16x16x32_bf16 v[248:251], a[132:135], a[36:39], v[248:251]\n"
"ds_read_b128 a[156:159], v216 offset:21248\n"
"v_lshlrev_b32 v230, 16, v245\n"
"v_mfma_f32_16x16x32_bf16 v[248:251], a[136:139], a[40:43], v[248:251]\n"
"v_exp_f32 v193, v193\n"
"v_mfma_f32_16x16x32_bf16 v[248:251], a[140:143], a[44:47], v[248:251]\n"
"ds_read_b64_tr_b16 a[176:177], v217 offset:256\n"
"v_cvt_pk_bf16_f32 v160, v192, v193\n"
"v_mfma_f32_16x16x32_bf16 v[252:255], a[140:143], a[60:63], -4.0\n"
"v_exp_f32 v194, v194\n"
"v_mfma_f32_16x16x32_bf16 v[252:255], a[136:139], a[56:59], v[252:255]\n"
"ds_read_b64_tr_b16 a[178:179], v217 offset:4352\n"
"v_and_b32 v231, s67, v245\n"
"v_mfma_f32_16x16x32_bf16 v[252:255], a[132:135], a[52:55], v[252:255]\n"
"s_add_u32 m0, s65, 32768\n"
"v_exp_f32 v195, v195\n"
"v_mfma_f32_16x16x32_bf16 v[252:255], a[128:131], a[48:51], v[252:255]\n"
"global_load_lds_dwordx4 v219, s[50:51] offset:3072\n"
"v_cvt_pk_bf16_f32 v161, v194, v195\n"
"v_smfmac_f32_16x16x64_bf16 a[192:195], a[208:211], v[128:135], v215\n"
"v_lshlrev_b32 v232, 16, v246\n"
"v_smfmac_f32_16x16x64_bf16 a[196:199], a[208:211], v[136:143], v215\n"
"ds_read_b64_tr_b16 a[180:181], v217 offset:8448\n"
"v_smfmac_f32_16x16x64_bf16 a[200:203], a[208:211], v[144:151], v215\n"
"v_and_b32 v233, s67, v246\n"
"v_smfmac_f32_16x16x64_bf16 a[204:207], a[208:211], v[152:159], v215\n"
"ds_read_b64_tr_b16 a[182:183], v217 offset:12544\n"
"v_mfma_f32_16x16x32_bf16 v[0:3], a[160:163], v[128:131], v[0:3]\n"
"s_waitcnt lgkmcnt(6)\n"
"v_exp_f32 v196, v196\n"
"v_mfma_f32_16x16x32_bf16 v[0:3], a[164:167], v[132:135], v[0:3]\n"
"ds_read_b64_tr_b16 a[184:185], v217 offset:264\n"
"v_lshlrev_b32 v234, 16, v247\n"
"v_mfma_f32_16x16x32_bf16 v[4:7], a[172:175], v[132:135], v[4:7]\n"
"v_exp_f32 v197, v197\n"
"v_mfma_f32_16x16x32_bf16 v[4:7], a[168:171], v[128:131], v[4:7]\n"
"buffer_store_dwordx4 v[236:239], v223, s[56:59], s70 offen offset:128 sc1 nt\n"
"v_cvt_pk_bf16_f32 v168, v196, v197\n"
"v_and_b32 v235, s67, v247\n"
"v_pk_mul_f32 v[228:229], v[228:229], s[68:69]\n"
"v_pk_mul_f32 v[230:231], v[230:231], s[68:69]\n"
"v_pk_mul_f32 v[232:233], v[232:233], s[68:69]\n"
"v_pk_mul_f32 v[234:235], v[234:235], s[68:69]\n"
"v_cvt_pk_bf16_f32 v228, v228, v229\n"
"v_cvt_pk_bf16_f32 v230, v230, v231\n"
"v_mfma_f32_16x16x32_bf16 v[12:15], a[168:171], v[136:139], v[12:15]\n"
"v_exp_f32 v198, v198\n"
"v_mfma_f32_16x16x32_bf16 v[12:15], a[172:175], v[140:143], v[12:15]\n"
"s_bfe_u32 s48, s38, 0x3001c\n"
"ds_read_b64_tr_b16 a[186:187], v217 offset:4360\n"
"v_cvt_pk_bf16_f32 v232, v232, v233\n"
"v_mfma_f32_16x16x32_bf16 v[8:11], a[164:167], v[140:143], v[8:11]\n"
"s_lshl_b32 s48, s48, 14\n"
"v_exp_f32 v199, v199\n"
"v_mfma_f32_16x16x32_bf16 v[8:11], a[160:163], v[136:139], v[8:11]\n"
"s_add_u32 s36, s79, s48\n"
"ds_read_b64_tr_b16 a[188:189], v217 offset:8456\n"
"v_cvt_pk_bf16_f32 v169, v198, v199\n"
"v_mfma_f32_16x16x32_bf16 v[16:19], a[160:163], v[144:147], v[16:19]\n"
"s_add_u32 s48, s42, s36\n"
"v_exp_f32 v248, v248\n"
"v_mfma_f32_16x16x32_bf16 v[16:19], a[164:167], v[148:151], v[16:19]\n"
"s_addc_u32 s49, s43, 0\n"
"ds_read_b64_tr_b16 a[190:191], v217 offset:12552\n"
"v_cvt_pk_bf16_f32 v234, v234, v235\n"
"v_mfma_f32_16x16x32_bf16 v[20:23], a[172:175], v[148:151], v[20:23]\n"
"s_add_u32 m0, s64, 49152\n"
"v_exp_f32 v249, v249\n"
"v_mfma_f32_16x16x32_bf16 v[20:23], a[168:171], v[144:147], v[20:23]\n"
"global_load_lds_dwordx4 v219, s[48:49] offset:0\n"
"v_cvt_pk_bf16_f32 v176, v248, v249\n"
"v_mfma_f32_16x16x32_bf16 v[28:31], a[168:171], v[152:155], v[28:31]\n"
"v_exp_f32 v250, v250\n"
"v_mfma_f32_16x16x32_bf16 v[28:31], a[172:175], v[156:159], v[28:31]\n"
"v_accvgpr_write_b32 a96, v228\n"
"v_accvgpr_write_b32 a97, v230\n"
"v_mfma_f32_16x16x32_bf16 v[24:27], a[164:167], v[156:159], v[24:27]\n"
"v_exp_f32 v251, v251\n"
"v_mfma_f32_16x16x32_bf16 v[24:27], a[160:163], v[152:155], v[24:27]\n"
"v_cvt_pk_bf16_f32 v177, v250, v251\n"
"v_accvgpr_write_b32 a98, v232\n"
"v_mfma_f32_16x16x32_bf16 v[192:195], a[144:147], a[0:3], -4.0\n"
"s_waitcnt lgkmcnt(3)\n"
"v_exp_f32 v252, v252\n"
"v_mfma_f32_16x16x32_bf16 v[192:195], a[148:151], a[4:7], v[192:195]\n"
"s_mov_b32 s98, s61\n"
"ds_read_b128 a[128:131], v216 offset:24576\n"
"v_accvgpr_write_b32 a99, v234\n"
"v_mfma_f32_16x16x32_bf16 v[192:195], a[152:155], a[8:11], v[192:195]\n"
"v_exp_f32 v253, v253\n"
"v_mfma_f32_16x16x32_bf16 v[192:195], a[156:159], a[12:15], v[192:195]\n"
"s_mov_b32 s61, s62\n"
"ds_read_b128 a[132:135], v216 offset:24832\n"
"v_cvt_pk_bf16_f32 v184, v252, v253\n"
"v_mfma_f32_16x16x32_bf16 v[196:199], a[156:159], a[28:31], -4.0\n"
"v_exp_f32 v254, v254\n"
"v_mfma_f32_16x16x32_bf16 v[196:199], a[152:155], a[24:27], v[196:199]\n"
"s_add_u32 s74, s60, 32\n"
"ds_read_b128 a[136:139], v216 offset:25088\n"
"v_lshlrev_b32 v228, 16, v200\n"
"v_mfma_f32_16x16x32_bf16 v[196:199], a[148:151], a[20:23], v[196:199]\n"
"s_add_u32 m0, s64, 49152\n"
"v_exp_f32 v255, v255\n"
"v_mfma_f32_16x16x32_bf16 v[196:199], a[144:147], a[16:19], v[196:199]\n"
"s_sub_u32 s60, s60, 160\n"
"global_load_lds_dwordx4 v219, s[48:49] offset:1024\n"
"v_cvt_pk_bf16_f32 v185, v254, v255\n"
"v_mfma_f32_16x16x32_bf16 v[248:251], a[144:147], a[32:35], -4.0\n"
"s_cselect_b32 s60, s74, s60\n"
"v_exp_f32 v192, v192\n"
"v_mfma_f32_16x16x32_bf16 v[248:251], a[148:151], a[36:39], v[248:251]\n"
"ds_read_b128 a[140:143], v216 offset:25344\n"
"v_and_b32 v229, s67, v200\n"
"v_mfma_f32_16x16x32_bf16 v[248:251], a[152:155], a[40:43], v[248:251]\n"
"s_lshr_b32 s80, s80, 1\n"
"v_exp_f32 v193, v193\n"
"v_mfma_f32_16x16x32_bf16 v[248:251], a[156:159], a[44:47], v[248:251]\n"
"s_cselect_b32 s73, -1, 0\n"
"ds_read_b64_tr_b16 a[160:161], v217 offset:512\n"
"ds_read_b128 v[244:247], v218 offset:768\n"
"v_cvt_pk_bf16_f32 v162, v192, v193\n"
"v_mfma_f32_16x16x32_bf16 v[252:255], a[156:159], a[60:63], -4.0\n"
"v_exp_f32 v194, v194\n"
"v_mfma_f32_16x16x32_bf16 v[252:255], a[152:155], a[56:59], v[252:255]\n"
"s_mul_i32 s74, s76, 3\n"
"ds_read_b64_tr_b16 a[162:163], v217 offset:4608\n"
"v_lshlrev_b32 v230, 16, v201\n"
"v_mfma_f32_16x16x32_bf16 v[252:255], a[148:151], a[52:55], v[252:255]\n"
"s_add_u32 m0, s64, 49152\n"
"v_exp_f32 v195, v195\n"
"v_mfma_f32_16x16x32_bf16 v[252:255], a[144:147], a[48:51], v[252:255]\n"
"s_add_u32 s74, s74, s60\n"
"global_load_lds_dwordx4 v220, s[48:49] offset:2048\n"
"v_cvt_pk_bf16_f32 v163, v194, v195\n"
"v_mfma_f32_16x16x32_bf16 v[32:35], a[176:179], v[128:131], v[32:35]\n"
"s_waitcnt lgkmcnt(6)\n"
"v_exp_f32 v196, v196\n"
"v_mfma_f32_16x16x32_bf16 v[32:35], a[180:183], v[132:135], v[32:35]\n"
"s_lshl_b32 s74, s74, 16\n"
"ds_read_b64_tr_b16 a[164:165], v217 offset:8704\n"
"v_and_b32 v231, s67, v201\n"
"v_mfma_f32_16x16x32_bf16 v[36:39], a[188:191], v[132:135], v[36:39]\n"
"v_exp_f32 v197, v197\n"
"v_mfma_f32_16x16x32_bf16 v[36:39], a[184:187], v[128:131], v[36:39]\n"
"s_add_u32 s62, s74, s78\n"
"ds_read_b64_tr_b16 a[166:167], v217 offset:12800\n"
"v_cvt_pk_bf16_f32 v170, v196, v197\n"
"v_mfma_f32_16x16x32_bf16 v[44:47], a[184:187], v[136:139], v[44:47]\n"
"v_exp_f32 v198, v198\n"
"v_mfma_f32_16x16x32_bf16 v[44:47], a[188:191], v[140:143], v[44:47]\n"
"ds_read_b64_tr_b16 a[168:169], v217 offset:520\n"
"v_lshlrev_b32 v232, 16, v202\n"
"v_and_b32 v233, s67, v202\n"
"v_lshlrev_b32 v234, 16, v203\n"
"v_and_b32 v235, s67, v203\n"
"v_pk_mul_f32 v[228:229], v[228:229], s[68:69]\n"
"v_pk_mul_f32 v[230:231], v[230:231], s[68:69]\n"
"v_pk_mul_f32 v[232:233], v[232:233], s[68:69]\n"
"v_pk_mul_f32 v[234:235], v[234:235], s[68:69]\n"
"v_cvt_pk_bf16_f32 v228, v228, v229\n"
"v_cvt_pk_bf16_f32 v230, v230, v231\n"
"v_mfma_f32_16x16x32_bf16 v[40:43], a[180:183], v[140:143], v[40:43]\n"
"v_exp_f32 v199, v199\n"
"v_mfma_f32_16x16x32_bf16 v[40:43], a[176:179], v[136:139], v[40:43]\n"
"v_cvt_pk_bf16_f32 v171, v198, v199\n"
"v_cvt_pk_bf16_f32 v232, v232, v233\n"
"v_mfma_f32_16x16x32_bf16 v[48:51], a[176:179], v[144:147], v[48:51]\n"
"v_exp_f32 v248, v248\n"
"v_mfma_f32_16x16x32_bf16 v[48:51], a[180:183], v[148:151], v[48:51]\n"
"ds_read_b64_tr_b16 a[170:171], v217 offset:4616\n"
"v_cvt_pk_bf16_f32 v234, v234, v235\n"
"v_mfma_f32_16x16x32_bf16 v[52:55], a[188:191], v[148:151], v[52:55]\n"
"v_exp_f32 v249, v249\n"
"v_mfma_f32_16x16x32_bf16 v[52:55], a[184:187], v[144:147], v[52:55]\n"
"ds_read_b64_tr_b16 a[172:173], v217 offset:8712\n"
"v_cvt_pk_bf16_f32 v178, v248, v249\n"
"v_mfma_f32_16x16x32_bf16 v[60:63], a[184:187], v[152:155], v[60:63]\n"
"v_exp_f32 v250, v250\n"
"v_mfma_f32_16x16x32_bf16 v[60:63], a[188:191], v[156:159], v[60:63]\n"
"ds_read_b64_tr_b16 a[174:175], v217 offset:12808\n"
"v_accvgpr_write_b32 a100, v228\n"
"v_mfma_f32_16x16x32_bf16 v[56:59], a[180:183], v[156:159], v[56:59]\n"
"s_add_u32 m0, s64, 49152\n"
"v_exp_f32 v251, v251\n"
"v_mfma_f32_16x16x32_bf16 v[56:59], a[176:179], v[152:155], v[56:59]\n"
"global_load_lds_dwordx4 v220, s[48:49] offset:3072\n"
"v_cvt_pk_bf16_f32 v179, v250, v251\n"
"v_mfma_f32_16x16x32_bf16 v[192:195], a[128:131], a[0:3], -4.0\n"
"s_waitcnt lgkmcnt(4)\n"
"v_exp_f32 v252, v252\n"
"v_mfma_f32_16x16x32_bf16 v[192:195], a[132:135], a[4:7], v[192:195]\n"
"ds_read_b128 a[144:147], v216 offset:28672\n"
"v_and_b32 v221, s73, v219\n"
"v_mfma_f32_16x16x32_bf16 v[192:195], a[136:139], a[8:11], v[192:195]\n"
"v_exp_f32 v253, v253\n"
"v_mfma_f32_16x16x32_bf16 v[192:195], a[140:143], a[12:15], v[192:195]\n"
"ds_read_b128 a[148:151], v216 offset:28928\n"
"v_cvt_pk_bf16_f32 v186, v252, v253\n"
"v_mfma_f32_16x16x32_bf16 v[196:199], a[140:143], a[28:31], -4.0\n"
"v_exp_f32 v254, v254\n"
"v_mfma_f32_16x16x32_bf16 v[196:199], a[136:139], a[24:27], v[196:199]\n"
"ds_read_b128 a[152:155], v216 offset:29184\n"
"v_and_b32 v222, s73, v220\n"
"v_mfma_f32_16x16x32_bf16 v[196:199], a[132:135], a[20:23], v[196:199]\n"
"v_exp_f32 v255, v255\n"
"v_mfma_f32_16x16x32_bf16 v[196:199], a[128:131], a[16:19], v[196:199]\n"
"v_cvt_pk_bf16_f32 v187, v254, v255\n"
"v_accvgpr_write_b32 a101, v230\n"
"v_mfma_f32_16x16x32_bf16 v[248:251], a[128:131], a[32:35], -4.0\n"
"v_exp_f32 v192, v192\n"
"v_mfma_f32_16x16x32_bf16 v[248:251], a[132:135], a[36:39], v[248:251]\n"
"ds_read_b128 a[156:159], v216 offset:29440\n"
"v_accvgpr_write_b32 a102, v232\n"
"v_mfma_f32_16x16x32_bf16 v[248:251], a[136:139], a[40:43], v[248:251]\n"
"v_exp_f32 v193, v193\n"
"v_mfma_f32_16x16x32_bf16 v[248:251], a[140:143], a[44:47], v[248:251]\n"
"ds_read_b64_tr_b16 a[176:177], v217 offset:768\n"
"v_cvt_pk_bf16_f32 v164, v192, v193\n"
"v_mfma_f32_16x16x32_bf16 v[252:255], a[140:143], a[60:63], -4.0\n"
"v_exp_f32 v194, v194\n"
"v_mfma_f32_16x16x32_bf16 v[252:255], a[136:139], a[56:59], v[252:255]\n"
"ds_read_b64_tr_b16 a[178:179], v217 offset:4864\n"
"v_accvgpr_write_b32 a103, v234\n"
"v_mfma_f32_16x16x32_bf16 v[252:255], a[132:135], a[52:55], v[252:255]\n"
"v_exp_f32 v195, v195\n"
"v_mfma_f32_16x16x32_bf16 v[252:255], a[128:131], a[48:51], v[252:255]\n"
"v_cvt_pk_bf16_f32 v165, v194, v195\n"
"v_lshlrev_b32 v228, 16, v204\n"
"v_mfma_f32_16x16x32_bf16 v[64:67], a[160:163], v[128:131], v[64:67]\n"
"v_exp_f32 v196, v196\n"
"v_mfma_f32_16x16x32_bf16 v[64:67], a[164:167], v[132:135], v[64:67]\n"
"s_waitcnt lgkmcnt(5)\n"
"ds_read_b64_tr_b16 a[180:181], v217 offset:8960\n"
"v_and_b32 v229, s67, v204\n"
"v_mfma_f32_16x16x32_bf16 v[68:71], a[172:175], v[132:135], v[68:71]\n"
"s_mov_b32 s55, s62\n"
"v_exp_f32 v197, v197\n"
"v_mfma_f32_16x16x32_bf16 v[68:71], a[168:171], v[128:131], v[68:71]\n"
"s_add_u32 s35, s55, s99\n"
"ds_read_b64_tr_b16 a[182:183], v217 offset:13056\n"
"v_cvt_pk_bf16_f32 v172, v196, v197\n"
"v_mfma_f32_16x16x32_bf16 v[76:79], a[168:171], v[136:139], v[76:79]\n"
"s_add_u32 s52, s40, s35\n"
"v_exp_f32 v198, v198\n"
"v_mfma_f32_16x16x32_bf16 v[76:79], a[172:175], v[140:143], v[76:79]\n"
"s_addc_u32 s53, s41, 0\n"
"ds_read_b64_tr_b16 a[184:185], v217 offset:776\n"
"v_lshlrev_b32 v230, 16, v205\n"
"v_mfma_f32_16x16x32_bf16 v[72:75], a[164:167], v[140:143], v[72:75]\n"
"s_add_u32 m0, s66, 0\n"
"v_exp_f32 v199, v199\n"
"v_mfma_f32_16x16x32_bf16 v[72:75], a[160:163], v[136:139], v[72:75]\n"
"global_load_lds_dwordx4 v221, s[52:53] offset:0\n"
"v_cvt_pk_bf16_f32 v173, v198, v199\n"
"v_mfma_f32_16x16x32_bf16 v[80:83], a[160:163], v[144:147], v[80:83]\n"
"v_exp_f32 v248, v248\n"
"v_mfma_f32_16x16x32_bf16 v[80:83], a[164:167], v[148:151], v[80:83]\n"
"ds_read_b64_tr_b16 a[186:187], v217 offset:4872\n"
"v_and_b32 v231, s67, v205\n"
"v_mfma_f32_16x16x32_bf16 v[84:87], a[172:175], v[148:151], v[84:87]\n"
"v_exp_f32 v249, v249\n"
"v_mfma_f32_16x16x32_bf16 v[84:87], a[168:171], v[144:147], v[84:87]\n"
"ds_read_b64_tr_b16 a[188:189], v217 offset:8968\n"
"v_cvt_pk_bf16_f32 v180, v248, v249\n"
"v_mfma_f32_16x16x32_bf16 v[92:95], a[168:171], v[152:155], v[92:95]\n"
"v_exp_f32 v250, v250\n"
"v_mfma_f32_16x16x32_bf16 v[92:95], a[172:175], v[156:159], v[92:95]\n"
"ds_read_b64_tr_b16 a[190:191], v217 offset:13064\n"
"v_lshlrev_b32 v232, 16, v206\n"
"v_mfma_f32_16x16x32_bf16 v[88:91], a[164:167], v[156:159], v[88:91]\n"
"v_exp_f32 v251, v251\n"
"v_mfma_f32_16x16x32_bf16 v[88:91], a[160:163], v[152:155], v[88:91]\n"
"s_waitcnt vmcnt(13) lgkmcnt(6)\n"
"buffer_store_dwordx4 v[240:243], v223, s[56:59], s70 offen offset:2176 sc1 nt\n"
"v_cvt_pk_bf16_f32 v181, v250, v251\n"
"v_and_b32 v233, s67, v206\n"
"v_lshlrev_b32 v234, 16, v207\n"
"v_mfma_f32_16x16x32_bf16 v[192:195], a[144:147], a[0:3], -4.0\n"
"s_barrier\n"
"v_exp_f32 v252, v252\n"
"v_mfma_f32_16x16x32_bf16 v[192:195], a[148:151], a[4:7], v[192:195]\n"
"s_bfe_u32 s50, s38, 0x3001c\n"
"ds_read_b128 a[128:131], v216 offset:32768\n"
"v_and_b32 v235, s67, v207\n"
"v_mfma_f32_16x16x32_bf16 v[192:195], a[152:155], a[8:11], v[192:195]\n"
"s_lshl_b32 s50, s50, 14\n"
"v_exp_f32 v253, v253\n"
"v_mfma_f32_16x16x32_bf16 v[192:195], a[156:159], a[12:15], v[192:195]\n"
"s_add_u32 s39, s54, s50\n"
"ds_read_b128 a[132:135], v216 offset:33024\n"
"v_cvt_pk_bf16_f32 v188, v252, v253\n"
"v_mfma_f32_16x16x32_bf16 v[196:199], a[156:159], a[28:31], -4.0\n"
"s_add_u32 s50, s44, s39\n"
"v_exp_f32 v254, v254\n"
"v_mfma_f32_16x16x32_bf16 v[196:199], a[152:155], a[24:27], v[196:199]\n"
"s_addc_u32 s51, s45, 0\n"
"ds_read_b128 a[136:139], v216 offset:33280\n"
"v_mul_f32 v228, s68, v228\n"
"v_mfma_f32_16x16x32_bf16 v[196:199], a[148:151], a[20:23], v[196:199]\n"
"s_add_u32 m0, s65, 49152\n"
"v_exp_f32 v255, v255\n"
"v_mfma_f32_16x16x32_bf16 v[196:199], a[144:147], a[16:19], v[196:199]\n"
"global_load_lds_dwordx4 v219, s[50:51] offset:0\n"
"v_cvt_pk_bf16_f32 v189, v254, v255\n"
"v_mfma_f32_16x16x32_bf16 v[248:251], a[144:147], a[32:35], -4.0\n"
"v_exp_f32 v192, v192\n"
"v_mfma_f32_16x16x32_bf16 v[248:251], a[148:151], a[36:39], v[248:251]\n"
"ds_read_b128 a[140:143], v216 offset:33536\n"
"v_mul_f32 v229, s68, v229\n"
"v_mfma_f32_16x16x32_bf16 v[248:251], a[152:155], a[40:43], v[248:251]\n"
"v_exp_f32 v193, v193\n"
"v_mfma_f32_16x16x32_bf16 v[248:251], a[156:159], a[44:47], v[248:251]\n"
"ds_read_b64_tr_b16 a[160:161], v217 offset:16384\n"
"v_cvt_pk_bf16_f32 v166, v192, v193\n"
"v_mfma_f32_16x16x32_bf16 v[252:255], a[156:159], a[60:63], -4.0\n"
"v_exp_f32 v194, v194\n"
"v_mfma_f32_16x16x32_bf16 v[252:255], a[152:155], a[56:59], v[252:255]\n"
"ds_read_b64_tr_b16 a[162:163], v217 offset:20480\n"
"v_mul_f32 v230, s68, v230\n"
"v_mfma_f32_16x16x32_bf16 v[252:255], a[148:151], a[52:55], v[252:255]\n"
"v_exp_f32 v195, v195\n"
"v_mfma_f32_16x16x32_bf16 v[252:255], a[144:147], a[48:51], v[252:255]\n"
"v_cvt_pk_bf16_f32 v167, v194, v195\n"
"v_mul_f32 v231, s68, v231\n"
"v_mfma_f32_16x16x32_bf16 v[96:99], a[176:179], v[128:131], v[96:99]\n"
"s_waitcnt lgkmcnt(6)\n"
"v_exp_f32 v196, v196\n"
"v_mfma_f32_16x16x32_bf16 v[96:99], a[180:183], v[132:135], v[96:99]\n"
"ds_read_b64_tr_b16 a[164:165], v217 offset:24576\n"
"v_mul_f32 v232, s68, v232\n"
"v_mfma_f32_16x16x32_bf16 v[100:103], a[188:191], v[132:135], v[100:103]\n"
"v_exp_f32 v197, v197\n"
"v_mfma_f32_16x16x32_bf16 v[100:103], a[184:187], v[128:131], v[100:103]\n"
"ds_read_b64_tr_b16 a[166:167], v217 offset:28672\n"
"v_cvt_pk_bf16_f32 v174, v196, v197\n"
"v_mfma_f32_16x16x32_bf16 v[108:111], a[184:187], v[136:139], v[108:111]\n"
"v_exp_f32 v198, v198\n"
"v_mfma_f32_16x16x32_bf16 v[108:111], a[188:191], v[140:143], v[108:111]\n"
"ds_read_b64_tr_b16 a[168:169], v217 offset:16392\n"
"v_mul_f32 v233, s68, v233\n"
"v_mfma_f32_16x16x32_bf16 v[104:107], a[180:183], v[140:143], v[104:107]\n"
"s_add_u32 m0, s65, 49152\n"
"v_exp_f32 v199, v199\n"
"v_mfma_f32_16x16x32_bf16 v[104:107], a[176:179], v[136:139], v[104:107]\n"
"global_load_lds_dwordx4 v219, s[50:51] offset:1024\n"
"v_cvt_pk_bf16_f32 v175, v198, v199\n"
"v_mfma_f32_16x16x32_bf16 v[112:115], a[176:179], v[144:147], v[112:115]\n"
"v_exp_f32 v248, v248\n"
"v_mfma_f32_16x16x32_bf16 v[112:115], a[180:183], v[148:151], v[112:115]\n"
"ds_read_b64_tr_b16 a[170:171], v217 offset:20488\n"
"v_mul_f32 v234, s68, v234\n"
"v_mul_f32 v235, s68, v235\n"
"v_cvt_pk_bf16_f32 v228, v228, v229\n"
"v_cvt_pk_bf16_f32 v230, v230, v231\n"
"v_mfma_f32_16x16x32_bf16 v[116:119], a[188:191], v[148:151], v[116:119]\n"
"v_exp_f32 v249, v249\n"
"v_mfma_f32_16x16x32_bf16 v[116:119], a[184:187], v[144:147], v[116:119]\n"
"ds_read_b64_tr_b16 a[172:173], v217 offset:24584\n"
"v_cvt_pk_bf16_f32 v182, v248, v249\n"
"v_mfma_f32_16x16x32_bf16 v[124:127], a[184:187], v[152:155], v[124:127]\n"
"v_exp_f32 v250, v250\n"
"v_mfma_f32_16x16x32_bf16 v[124:127], a[188:191], v[156:159], v[124:127]\n"
"ds_read_b64_tr_b16 a[174:175], v217 offset:28680\n"
"v_cvt_pk_bf16_f32 v232, v232, v233\n"
"v_mfma_f32_16x16x32_bf16 v[120:123], a[180:183], v[156:159], v[120:123]\n"
"s_add_u32 m0, s65, 49152\n"
"v_exp_f32 v251, v251\n"
"v_mfma_f32_16x16x32_bf16 v[120:123], a[176:179], v[152:155], v[120:123]\n"
"s_waitcnt lgkmcnt(6)\n"
"global_load_lds_dwordx4 v219, s[50:51] offset:2048\n"
"v_cvt_pk_bf16_f32 v183, v250, v251\n"
"v_mfma_f32_16x16x32_bf16 v[192:195], a[128:131], a[0:3], -4.0\n"
"v_exp_f32 v252, v252\n"
"v_mfma_f32_16x16x32_bf16 v[192:195], a[132:135], a[4:7], v[192:195]\n"
"ds_read_b128 a[144:147], v216 offset:36864\n"
"v_cvt_pk_bf16_f32 v234, v234, v235\n"
"v_mfma_f32_16x16x32_bf16 v[192:195], a[136:139], a[8:11], v[192:195]\n"
"v_exp_f32 v253, v253\n"
"v_mfma_f32_16x16x32_bf16 v[192:195], a[140:143], a[12:15], v[192:195]\n"
"ds_read_b128 a[148:151], v216 offset:37120\n"
"v_cvt_pk_bf16_f32 v190, v252, v253\n"
"v_mfma_f32_16x16x32_bf16 v[196:199], a[140:143], a[28:31], -4.0\n"
"v_exp_f32 v254, v254\n"
"v_mfma_f32_16x16x32_bf16 v[196:199], a[136:139], a[24:27], v[196:199]\n"
"ds_read_b128 a[152:155], v216 offset:37376\n"
"v_accvgpr_write_b32 a104, v228\n"
"v_mfma_f32_16x16x32_bf16 v[196:199], a[132:135], a[20:23], v[196:199]\n"
"v_exp_f32 v255, v255\n"
"v_mfma_f32_16x16x32_bf16 v[196:199], a[128:131], a[16:19], v[196:199]\n"
"v_cvt_pk_bf16_f32 v191, v254, v255\n"
"v_accvgpr_write_b32 a105, v230\n"
"v_mfma_f32_16x16x32_bf16 v[248:251], a[128:131], a[32:35], -4.0\n"
"v_exp_f32 v192, v192\n"
"v_mfma_f32_16x16x32_bf16 v[248:251], a[132:135], a[36:39], v[248:251]\n"
"ds_read_b128 a[156:159], v216 offset:37632\n"
"v_accvgpr_write_b32 a106, v232\n"
"v_mfma_f32_16x16x32_bf16 v[248:251], a[136:139], a[40:43], v[248:251]\n"
"v_exp_f32 v193, v193\n"
"v_mfma_f32_16x16x32_bf16 v[248:251], a[140:143], a[44:47], v[248:251]\n"
"ds_read_b64_tr_b16 a[176:177], v217 offset:16640\n"
"v_cvt_pk_bf16_f32 v128, v192, v193\n"
"v_mfma_f32_16x16x32_bf16 v[252:255], a[140:143], a[60:63], -4.0\n"
"v_exp_f32 v194, v194\n"
"v_mfma_f32_16x16x32_bf16 v[252:255], a[136:139], a[56:59], v[252:255]\n"
"ds_read_b64_tr_b16 a[178:179], v217 offset:20736\n"
"v_accvgpr_write_b32 a107, v234\n"
"v_mfma_f32_16x16x32_bf16 v[252:255], a[132:135], a[52:55], v[252:255]\n"
"s_add_u32 m0, s65, 49152\n"
"v_exp_f32 v195, v195\n"
"v_mfma_f32_16x16x32_bf16 v[252:255], a[128:131], a[48:51], v[252:255]\n"
"global_load_lds_dwordx4 v219, s[50:51] offset:3072\n"
"v_cvt_pk_bf16_f32 v129, v194, v195\n"
"v_smfmac_f32_16x16x64_bf16 a[192:195], a[208:211], v[160:167], v215\n"
"v_lshlrev_b32 v228, 16, v244\n"
"v_smfmac_f32_16x16x64_bf16 a[196:199], a[208:211], v[168:175], v215\n"
"ds_read_b64_tr_b16 a[180:181], v217 offset:24832\n"
"v_smfmac_f32_16x16x64_bf16 a[200:203], a[208:211], v[176:183], v215\n"
"v_and_b32 v229, s67, v244\n"
"v_smfmac_f32_16x16x64_bf16 a[204:207], a[208:211], v[184:191], v215\n"
"ds_read_b64_tr_b16 a[182:183], v217 offset:28928\n"
"v_mfma_f32_16x16x32_bf16 v[0:3], a[160:163], v[160:163], v[0:3]\n"
"s_waitcnt lgkmcnt(6)\n"
"v_exp_f32 v196, v196\n"
"v_mfma_f32_16x16x32_bf16 v[0:3], a[164:167], v[164:167], v[0:3]\n"
"ds_read_b64_tr_b16 a[184:185], v217 offset:16648\n"
"v_lshlrev_b32 v230, 16, v245\n"
"v_mfma_f32_16x16x32_bf16 v[4:7], a[172:175], v[164:167], v[4:7]\n"
"v_exp_f32 v197, v197\n"
"v_mfma_f32_16x16x32_bf16 v[4:7], a[168:171], v[160:163], v[4:7]\n"
"v_cvt_pk_bf16_f32 v136, v196, v197\n"
"v_and_b32 v231, s67, v245\n"
"v_mfma_f32_16x16x32_bf16 v[12:15], a[168:171], v[168:171], v[12:15]\n"
"s_mov_b32 s79, s63\n"
"v_exp_f32 v198, v198\n"
"v_mfma_f32_16x16x32_bf16 v[12:15], a[172:175], v[172:175], v[12:15]\n"
"s_bfe_u32 s48, s38, 0x30000\n"
"ds_read_b64_tr_b16 a[186:187], v217 offset:20744\n"
"v_lshlrev_b32 v232, 16, v246\n"
"v_mfma_f32_16x16x32_bf16 v[8:11], a[164:167], v[172:175], v[8:11]\n"
"s_lshl_b32 s48, s48, 14\n"
"v_exp_f32 v199, v199\n"
"v_mfma_f32_16x16x32_bf16 v[8:11], a[160:163], v[168:171], v[8:11]\n"
"s_add_u32 s36, s79, s48\n"
"ds_read_b64_tr_b16 a[188:189], v217 offset:24840\n"
"v_cvt_pk_bf16_f32 v137, v198, v199\n"
"v_mfma_f32_16x16x32_bf16 v[16:19], a[160:163], v[176:179], v[16:19]\n"
"s_add_u32 s48, s42, s36\n"
"v_exp_f32 v248, v248\n"
"v_mfma_f32_16x16x32_bf16 v[16:19], a[164:167], v[180:183], v[16:19]\n"
"s_addc_u32 s49, s43, 0\n"
"ds_read_b64_tr_b16 a[190:191], v217 offset:28936\n"
"v_and_b32 v233, s67, v246\n"
"v_mfma_f32_16x16x32_bf16 v[20:23], a[172:175], v[180:183], v[20:23]\n"
"s_add_u32 m0, s64, 0\n"
"v_exp_f32 v249, v249\n"
"v_mfma_f32_16x16x32_bf16 v[20:23], a[168:171], v[176:179], v[20:23]\n"
"global_load_lds_dwordx4 v219, s[48:49] offset:0\n"
"v_cvt_pk_bf16_f32 v144, v248, v249\n"
"v_mfma_f32_16x16x32_bf16 v[28:31], a[168:171], v[184:187], v[28:31]\n"
"v_exp_f32 v250, v250\n"
"v_mfma_f32_16x16x32_bf16 v[28:31], a[172:175], v[188:191], v[28:31]\n"
"buffer_store_dwordx4 a[216:219], v223, s[56:59], s71 offen offset:128 sc1 nt\n"
"v_lshlrev_b32 v234, 16, v247\n"
"v_and_b32 v235, s67, v247\n"
"v_pk_mul_f32 v[228:229], v[228:229], s[68:69]\n"
"v_pk_mul_f32 v[230:231], v[230:231], s[68:69]\n"
"v_pk_mul_f32 v[232:233], v[232:233], s[68:69]\n"
"v_pk_mul_f32 v[234:235], v[234:235], s[68:69]\n"
"v_cvt_pk_bf16_f32 v228, v228, v229\n"
"v_cvt_pk_bf16_f32 v230, v230, v231\n"
"v_mfma_f32_16x16x32_bf16 v[24:27], a[164:167], v[188:191], v[24:27]\n"
"s_add_u32 m0, s66, 0\n"
"v_exp_f32 v251, v251\n"
"v_mfma_f32_16x16x32_bf16 v[24:27], a[160:163], v[184:187], v[24:27]\n"
"global_load_lds_dwordx4 v221, s[52:53] offset:1024\n"
"v_cvt_pk_bf16_f32 v145, v250, v251\n"
"v_mfma_f32_16x16x32_bf16 v[192:195], a[144:147], a[0:3], -4.0\n"
"v_exp_f32 v252, v252\n"
"v_mfma_f32_16x16x32_bf16 v[192:195], a[148:151], a[4:7], v[192:195]\n"
"s_waitcnt lgkmcnt(3)\n"
"ds_read_b128 a[128:131], v216 offset:40960\n"
"v_cvt_pk_bf16_f32 v232, v232, v233\n"
"v_mfma_f32_16x16x32_bf16 v[192:195], a[152:155], a[8:11], v[192:195]\n"
"s_mov_b32 s93, s63\n"
"v_exp_f32 v253, v253\n"
"v_mfma_f32_16x16x32_bf16 v[192:195], a[156:159], a[12:15], v[192:195]\n"
"ds_read_b128 a[132:135], v216 offset:41216\n"
"v_cvt_pk_bf16_f32 v152, v252, v253\n"
"v_mfma_f32_16x16x32_bf16 v[196:199], a[156:159], a[28:31], -4.0\n"
"s_mul_i32 s74, s60, 43691\n"
"v_exp_f32 v254, v254\n"
"v_mfma_f32_16x16x32_bf16 v[196:199], a[152:155], a[24:27], v[196:199]\n"
"ds_read_b128 a[136:139], v216 offset:41472\n"
"v_cvt_pk_bf16_f32 v234, v234, v235\n"
"v_mfma_f32_16x16x32_bf16 v[196:199], a[148:151], a[20:23], v[196:199]\n"
"s_add_u32 m0, s64, 0\n"
"v_exp_f32 v255, v255\n"
"v_mfma_f32_16x16x32_bf16 v[196:199], a[144:147], a[16:19], v[196:199]\n"
"s_lshr_b32 s74, s74, 17\n"
"global_load_lds_dwordx4 v219, s[48:49] offset:1024\n"
"v_cvt_pk_bf16_f32 v153, v254, v255\n"
"v_mfma_f32_16x16x32_bf16 v[248:251], a[144:147], a[32:35], -4.0\n"
"v_exp_f32 v192, v192\n"
"v_mfma_f32_16x16x32_bf16 v[248:251], a[148:151], a[36:39], v[248:251]\n"
"s_add_u32 s74, s74, s76\n"
"ds_read_b128 a[140:143], v216 offset:41728\n"
"v_accvgpr_write_b32 a108, v228\n"
"v_mfma_f32_16x16x32_bf16 v[248:251], a[152:155], a[40:43], v[248:251]\n"
"s_waitcnt vmcnt(16)\n"
"v_exp_f32 v193, v193\n"
"v_mfma_f32_16x16x32_bf16 v[248:251], a[156:159], a[44:47], v[248:251]\n"
"s_lshl_b32 s74, s74, 17\n"
"ds_read_b64_tr_b16 a[160:161], v217 offset:16896\n"
"ds_read_b128 v[200:203], v218 offset:4096\n"
"v_cvt_pk_bf16_f32 v130, v192, v193\n"
"v_mfma_f32_16x16x32_bf16 v[252:255], a[156:159], a[60:63], -4.0\n"
"v_exp_f32 v194, v194\n"
"v_mfma_f32_16x16x32_bf16 v[252:255], a[152:155], a[56:59], v[252:255]\n"
"s_add_u32 s63, s74, s77\n"
"ds_read_b64_tr_b16 a[162:163], v217 offset:20992\n"
"v_accvgpr_write_b32 a109, v230\n"
"v_mfma_f32_16x16x32_bf16 v[252:255], a[148:151], a[52:55], v[252:255]\n"
"s_add_u32 m0, s64, 0\n"
"v_exp_f32 v195, v195\n"
"v_mfma_f32_16x16x32_bf16 v[252:255], a[144:147], a[48:51], v[252:255]\n"
"global_load_lds_dwordx4 v220, s[48:49] offset:2048\n"
"v_cvt_pk_bf16_f32 v131, v194, v195\n"
"v_mfma_f32_16x16x32_bf16 v[32:35], a[176:179], v[160:163], v[32:35]\n"
".Lattn_pad9:\n"
"s_cbranch_vccnz .Lattn_ret\n"
"v_exp_f32 v196, v196\n"
"v_mfma_f32_16x16x32_bf16 v[32:35], a[180:183], v[164:167], v[32:35]\n"
"s_waitcnt lgkmcnt(6)\n"
"ds_read_b64_tr_b16 a[164:165], v217 offset:25088\n"
"ds_read_b128 v[204:207], v218 offset:4352\n"
"v_mfma_f32_16x16x32_bf16 v[36:39], a[188:191], v[164:167], v[36:39]\n"
"v_exp_f32 v197, v197\n"
"v_mfma_f32_16x16x32_bf16 v[36:39], a[184:187], v[160:163], v[36:39]\n"
"ds_read_b64_tr_b16 a[166:167], v217 offset:29184\n"
"v_cvt_pk_bf16_f32 v138, v196, v197\n"
"v_mfma_f32_16x16x32_bf16 v[44:47], a[184:187], v[168:171], v[44:47]\n"
"v_exp_f32 v198, v198\n"
"v_mfma_f32_16x16x32_bf16 v[44:47], a[188:191], v[172:175], v[44:47]\n"
"ds_read_b64_tr_b16 a[168:169], v217 offset:16904\n"
"v_accvgpr_write_b32 a110, v232\n"
"v_mfma_f32_16x16x32_bf16 v[40:43], a[180:183], v[172:175], v[40:43]\n"
"v_exp_f32 v199, v199\n"
"v_mfma_f32_16x16x32_bf16 v[40:43], a[176:179], v[168:171], v[40:43]\n"
"v_cvt_pk_bf16_f32 v139, v198, v199\n"
"v_accvgpr_write_b32 a111, v234\n"
"v_mfma_f32_16x16x32_bf16 v[48:51], a[176:179], v[176:179], v[48:51]\n"
"v_exp_f32 v248, v248\n"
"v_mfma_f32_16x16x32_bf16 v[48:51], a[180:183], v[180:183], v[48:51]\n"
"s_waitcnt lgkmcnt(5)\n"
"ds_read_b64_tr_b16 a[170:171], v217 offset:21000\n"
"v_lshlrev_b32 v228, 16, v200\n"
"v_mfma_f32_16x16x32_bf16 v[52:55], a[188:191], v[180:183], v[52:55]\n"
"v_exp_f32 v249, v249\n"
"v_mfma_f32_16x16x32_bf16 v[52:55], a[184:187], v[176:179], v[52:55]\n"
"ds_read_b64_tr_b16 a[172:173], v217 offset:25096\n"
"ds_read_b128 v[244:247], v218 offset:4608\n"
"v_cvt_pk_bf16_f32 v146, v248, v249\n"
"v_mfma_f32_16x16x32_bf16 v[60:63], a[184:187], v[184:187], v[60:63]\n"
"v_exp_f32 v250, v250\n"
"v_mfma_f32_16x16x32_bf16 v[60:63], a[188:191], v[188:191], v[60:63]\n"
"ds_read_b64_tr_b16 a[174:175], v217 offset:29192\n"
"v_and_b32 v229, s67, v200\n"
"v_mfma_f32_16x16x32_bf16 v[56:59], a[180:183], v[188:191], v[56:59]\n"
"s_add_u32 m0, s64, 0\n"
"v_exp_f32 v251, v251\n"
"v_mfma_f32_16x16x32_bf16 v[56:59], a[176:179], v[184:187], v[56:59]\n"
"global_load_lds_dwordx4 v220, s[48:49] offset:3072\n"
"v_cvt_pk_bf16_f32 v147, v250, v251\n"
"v_mfma_f32_16x16x32_bf16 v[192:195], a[128:131], a[0:3], -4.0\n"
"v_exp_f32 v252, v252\n"
"v_mfma_f32_16x16x32_bf16 v[192:195], a[132:135], a[4:7], v[192:195]\n"
"ds_read_b128 a[144:147], v216 offset:45056\n"
"v_lshlrev_b32 v230, 16, v201\n"
"v_mfma_f32_16x16x32_bf16 v[192:195], a[136:139], a[8:11], v[192:195]\n"
"v_exp_f32 v253, v253\n"
"v_mfma_f32_16x16x32_bf16 v[192:195], a[140:143], a[12:15], v[192:195]\n"
"ds_read_b128 a[148:151], v216 offset:45312\n"
"v_cvt_pk_bf16_f32 v154, v252, v253\n"
"v_mfma_f32_16x16x32_bf16 v[196:199], a[140:143], a[28:31], -4.0\n"
"v_exp_f32 v254, v254\n"
"v_mfma_f32_16x16x32_bf16 v[196:199], a[136:139], a[24:27], v[196:199]\n"
"ds_read_b128 a[152:155], v216 offset:45568\n"
"v_and_b32 v231, s67, v201\n"
"v_mfma_f32_16x16x32_bf16 v[196:199], a[132:135], a[20:23], v[196:199]\n"
"v_exp_f32 v255, v255\n"
"v_mfma_f32_16x16x32_bf16 v[196:199], a[128:131], a[16:19], v[196:199]\n"
"v_cvt_pk_bf16_f32 v155, v254, v255\n"
"v_lshlrev_b32 v232, 16, v202\n"
"v_mfma_f32_16x16x32_bf16 v[248:251], a[128:131], a[32:35], -4.0\n"
"v_exp_f32 v192, v192\n"
"v_mfma_f32_16x16x32_bf16 v[248:251], a[132:135], a[36:39], v[248:251]\n"
"ds_read_b128 a[156:159], v216 offset:45824\n"
"v_and_b32 v233, s67, v202\n"
"v_mfma_f32_16x16x32_bf16 v[248:251], a[136:139], a[40:43], v[248:251]\n"
"v_exp_f32 v193, v193\n"
"v_mfma_f32_16x16x32_bf16 v[248:251], a[140:143], a[44:47], v[248:251]\n"
"ds_read_b64_tr_b16 a[176:177], v217 offset:17152\n"
"v_cvt_pk_bf16_f32 v132, v192, v193\n"
"v_mfma_f32_16x16x32_bf16 v[252:255], a[140:143], a[60:63], -4.0\n"
"v_exp_f32 v194, v194\n"
"v_mfma_f32_16x16x32_bf16 v[252:255], a[136:139], a[56:59], v[252:255]\n"
"ds_read_b64_tr_b16 a[178:179], v217 offset:21248\n"
"v_lshlrev_b32 v234, 16, v203\n"
"v_mfma_f32_16x16x32_bf16 v[252:255], a[132:135], a[52:55], v[252:255]\n"
"v_exp_f32 v195, v195\n"
"v_mfma_f32_16x16x32_bf16 v[252:255], a[128:131], a[48:51], v[252:255]\n"
"s_waitcnt lgkmcnt(6)\n"
"buffer_store_dwordx4 a[220:223], v223, s[56:59], s71 offen offset:2176 sc1 nt\n"
"v_cvt_pk_bf16_f32 v133, v194, v195\n"
"v_and_b32 v235, s67, v203\n"
"v_pk_mul_f32 v[228:229], v[228:229], s[68:69]\n"
"v_pk_mul_f32 v[230:231], v[230:231], s[68:69]\n"
"v_pk_mul_f32 v[232:233], v[232:233], s[68:69]\n"
"v_pk_mul_f32 v[234:235], v[234:235], s[68:69]\n"
"v_cvt_pk_bf16_f32 v228, v228, v229\n"
"v_cvt_pk_bf16_f32 v230, v230, v231\n"
"v_mfma_f32_16x16x32_bf16 v[64:67], a[160:163], v[160:163], v[64:67]\n"
"v_exp_f32 v196, v196\n"
"v_mfma_f32_16x16x32_bf16 v[64:67], a[164:167], v[164:167], v[64:67]\n"
"ds_read_b64_tr_b16 a[180:181], v217 offset:25344\n"
"v_cvt_pk_bf16_f32 v232, v232, v233\n"
"v_mfma_f32_16x16x32_bf16 v[68:71], a[172:175], v[164:167], v[68:71]\n"
"v_exp_f32 v197, v197\n"
"v_mfma_f32_16x16x32_bf16 v[68:71], a[168:171], v[160:163], v[68:71]\n"
"ds_read_b64_tr_b16 a[182:183], v217 offset:29440\n"
"v_cvt_pk_bf16_f32 v140, v196, v197\n"
"v_mfma_f32_16x16x32_bf16 v[76:79], a[168:171], v[168:171], v[76:79]\n"
"v_exp_f32 v198, v198\n"
"v_mfma_f32_16x16x32_bf16 v[76:79], a[172:175], v[172:175], v[76:79]\n"
"ds_read_b64_tr_b16 a[184:185], v217 offset:17160\n"
"v_cvt_pk_bf16_f32 v234, v234, v235\n"
"v_mfma_f32_16x16x32_bf16 v[72:75], a[164:167], v[172:175], v[72:75]\n"
"v_exp_f32 v199, v199\n"
"v_mfma_f32_16x16x32_bf16 v[72:75], a[160:163], v[168:171], v[72:75]\n"
"v_cvt_pk_bf16_f32 v141, v198, v199\n"
"v_accvgpr_write_b32 a112, v228\n"
"v_mfma_f32_16x16x32_bf16 v[80:83], a[160:163], v[176:179], v[80:83]\n"
"v_exp_f32 v248, v248\n"
"v_mfma_f32_16x16x32_bf16 v[80:83], a[164:167], v[180:183], v[80:83]\n"
"ds_read_b64_tr_b16 a[186:187], v217 offset:21256\n"
"v_accvgpr_write_b32 a113, v230\n"
"v_mfma_f32_16x16x32_bf16 v[84:87], a[172:175], v[180:183], v[84:87]\n"
"v_exp_f32 v249, v249\n"
"v_mfma_f32_16x16x32_bf16 v[84:87], a[168:171], v[176:179], v[84:87]\n"
"ds_read_b64_tr_b16 a[188:189], v217 offset:25352\n"
"v_cvt_pk_bf16_f32 v148, v248, v249\n"
"v_mfma_f32_16x16x32_bf16 v[92:95], a[168:171], v[184:187], v[92:95]\n"
"v_exp_f32 v250, v250\n"
"v_mfma_f32_16x16x32_bf16 v[92:95], a[172:175], v[188:191], v[92:95]\n"
"ds_read_b64_tr_b16 a[190:191], v217 offset:29448\n"
"v_accvgpr_write_b32 a114, v232\n"
"v_mfma_f32_16x16x32_bf16 v[88:91], a[164:167], v[188:191], v[88:91]\n"
"s_mov_b32 s54, s93\n"
"v_exp_f32 v251, v251\n"
"v_mfma_f32_16x16x32_bf16 v[88:91], a[160:163], v[184:187], v[88:91]\n"
"s_waitcnt vmcnt(13) lgkmcnt(6)\n"
"v_cvt_pk_bf16_f32 v149, v250, v251\n"
"v_accvgpr_write_b32 a115, v234\n"
"v_mfma_f32_16x16x32_bf16 v[192:195], a[144:147], a[0:3], -4.0\n"
"s_barrier\n"
"v_exp_f32 v252, v252\n"
"v_mfma_f32_16x16x32_bf16 v[192:195], a[148:151], a[4:7], v[192:195]\n"
"s_bfe_u32 s50, s38, 0x30000\n"
"ds_read_b128 a[128:131], v216 offset:49152\n"
"v_lshlrev_b32 v228, 16, v204\n"
"v_mfma_f32_16x16x32_bf16 v[192:195], a[152:155], a[8:11], v[192:195]\n"
"s_lshl_b32 s50, s50, 14\n"
"v_exp_f32 v253, v253\n"
"v_mfma_f32_16x16x32_bf16 v[192:195], a[156:159], a[12:15], v[192:195]\n"
"s_add_u32 s39, s54, s50\n"
"ds_read_b128 a[132:135], v216 offset:49408\n"
"ds_read_b128 v[200:203], v218 offset:4864\n"
"v_cvt_pk_bf16_f32 v156, v252, v253\n"
"v_mfma_f32_16x16x32_bf16 v[196:199], a[156:159], a[28:31], -4.0\n"
"s_add_u32 s50, s44, s39\n"
"v_exp_f32 v254, v254\n"
"v_mfma_f32_16x16x32_bf16 v[196:199], a[152:155], a[24:27], v[196:199]\n"
"s_addc_u32 s51, s45, 0\n"
"ds_read_b128 a[136:139], v216 offset:49664\n"
"v_and_b32 v229, s67, v204\n"
"v_mfma_f32_16x16x32_bf16 v[196:199], a[148:151], a[20:23], v[196:199]\n"
"s_add_u32 m0, s65, 0\n"
"v_exp_f32 v255, v255\n"
"v_mfma_f32_16x16x32_bf16 v[196:199], a[144:147], a[16:19], v[196:199]\n"
"global_load_lds_dwordx4 v219, s[50:51] offset:0\n"
"v_cvt_pk_bf16_f32 v157, v254, v255\n"
"v_mfma_f32_16x16x32_bf16 v[248:251], a[144:147], a[32:35], -4.0\n"
"v_exp_f32 v192, v192\n"
"v_mfma_f32_16x16x32_bf16 v[248:251], a[148:151], a[36:39], v[248:251]\n"
"ds_read_b128 a[140:143], v216 offset:49920\n"
"v_lshlrev_b32 v230, 16, v205\n"
"v_mfma_f32_16x16x32_bf16 v[248:251], a[152:155], a[40:43], v[248:251]\n"
"v_exp_f32 v193, v193\n"
"v_mfma_f32_16x16x32_bf16 v[248:251], a[156:159], a[44:47], v[248:251]\n"
"ds_read_b64_tr_b16 a[160:161], v217 offset:32768\n"
"v_cvt_pk_bf16_f32 v134, v192, v193\n"
"v_mfma_f32_16x16x32_bf16 v[252:255], a[156:159], a[60:63], -4.0\n"
"v_exp_f32 v194, v194\n"
"v_mfma_f32_16x16x32_bf16 v[252:255], a[152:155], a[56:59], v[252:255]\n"
"ds_read_b64_tr_b16 a[162:163], v217 offset:36864\n"
"v_and_b32 v231, s67, v205\n"
"v_mfma_f32_16x16x32_bf16 v[252:255], a[148:151], a[52:55], v[252:255]\n"
"s_add_u32 m0, s66, 0\n"
"v_exp_f32 v195, v195\n"
"v_mfma_f32_16x16x32_bf16 v[252:255], a[144:147], a[48:51], v[252:255]\n"
"global_load_lds_dwordx4 v222, s[52:53] offset:2048\n"
"v_cvt_pk_bf16_f32 v135, v194, v195\n"
"v_mfma_f32_16x16x32_bf16 v[96:99], a[176:179], v[160:163], v[96:99]\n"
"s_waitcnt lgkmcnt(6)\n"
"v_exp_f32 v196, v196\n"
"v_mfma_f32_16x16x32_bf16 v[96:99], a[180:183], v[164:167], v[96:99]\n"
"ds_read_b64_tr_b16 a[164:165], v217 offset:40960\n"
"v_lshlrev_b32 v232, 16, v206\n"
"v_mfma_f32_16x16x32_bf16 v[100:103], a[188:191], v[164:167], v[100:103]\n"
"v_exp_f32 v197, v197\n"
"v_mfma_f32_16x16x32_bf16 v[100:103], a[184:187], v[160:163], v[100:103]\n"
"ds_read_b64_tr_b16 a[166:167], v217 offset:45056\n"
"v_cvt_pk_bf16_f32 v142, v196, v197\n"
"v_mfma_f32_16x16x32_bf16 v[108:111], a[184:187], v[168:171], v[108:111]\n"
"v_exp_f32 v198, v198\n"
"v_mfma_f32_16x16x32_bf16 v[108:111], a[188:191], v[172:175], v[108:111]\n"
"ds_read_b64_tr_b16 a[168:169], v217 offset:32776\n"
"v_and_b32 v233, s67, v206\n"
"v_mfma_f32_16x16x32_bf16 v[104:107], a[180:183], v[172:175], v[104:107]\n"
"s_add_u32 m0, s65, 0\n"
"v_exp_f32 v199, v199\n"
"v_mfma_f32_16x16x32_bf16 v[104:107], a[176:179], v[168:171], v[104:107]\n"
"global_load_lds_dwordx4 v219, s[50:51] offset:1024\n"
"v_cvt_pk_bf16_f32 v143, v198, v199\n"
"v_mfma_f32_16x16x32_bf16 v[112:115], a[176:179], v[176:179], v[112:115]\n"
"v_exp_f32 v248, v248\n"
"v_mfma_f32_16x16x32_bf16 v[112:115], a[180:183], v[180:183], v[112:115]\n"
"ds_read_b64_tr_b16 a[170:171], v217 offset:36872\n"
"v_lshlrev_b32 v234, 16, v207\n"
"v_mfma_f32_16x16x32_bf16 v[116:119], a[188:191], v[180:183], v[116:119]\n"
"v_exp_f32 v249, v249\n"
"v_mfma_f32_16x16x32_bf16 v[116:119], a[184:187], v[176:179], v[116:119]\n"
"ds_read_b64_tr_b16 a[172:173], v217 offset:40968\n"
"v_cvt_pk_bf16_f32 v150, v248, v249\n"
"v_mfma_f32_16x16x32_bf16 v[124:127], a[184:187], v[184:187], v[124:127]\n"
"v_exp_f32 v250, v250\n"
"v_mfma_f32_16x16x32_bf16 v[124:127], a[188:191], v[188:191], v[124:127]\n"
"ds_read_b64_tr_b16 a[174:175], v217 offset:45064\n"
"v_and_b32 v235, s67, v207\n"
"v_mul_f32 v228, s68, v228\n"
"v_mul_f32 v229, s68, v229\n"
"v_mul_f32 v230, s68, v230\n"
"v_mul_f32 v231, s68, v231\n"
"v_pk_mul_f32 v[232:233], v[232:233], s[68:69]\n"
"v_pk_mul_f32 v[234:235], v[234:235], s[68:69]\n"
"v_cvt_pk_bf16_f32 v228, v228, v229\n"
"v_cvt_pk_bf16_f32 v230, v230, v231\n"
"v_mfma_f32_16x16x32_bf16 v[120:123], a[180:183], v[188:191], v[120:123]\n"
"s_add_u32 m0, s65, 0\n"
"v_exp_f32 v251, v251\n"
"v_mfma_f32_16x16x32_bf16 v[120:123], a[176:179], v[184:187], v[120:123]\n"
"global_load_lds_dwordx4 v219, s[50:51] offset:2048\n"
"v_cvt_pk_bf16_f32 v151, v250, v251\n"
"v_mfma_f32_16x16x32_bf16 v[192:195], a[128:131], a[0:3], -4.0\n"
"s_waitcnt lgkmcnt(4)\n"
"v_exp_f32 v252, v252\n"
"v_mfma_f32_16x16x32_bf16 v[192:195], a[132:135], a[4:7], v[192:195]\n"
"ds_read_b128 a[144:147], v216 offset:53248\n"
"v_cvt_pk_bf16_f32 v232, v232, v233\n"
"v_mfma_f32_16x16x32_bf16 v[192:195], a[136:139], a[8:11], v[192:195]\n"
"v_exp_f32 v253, v253\n"
"v_mfma_f32_16x16x32_bf16 v[192:195], a[140:143], a[12:15], v[192:195]\n"
"ds_read_b128 a[148:151], v216 offset:53504\n"
"v_cvt_pk_bf16_f32 v158, v252, v253\n"
"v_mfma_f32_16x16x32_bf16 v[196:199], a[140:143], a[28:31], -4.0\n"
"v_exp_f32 v254, v254\n"
"v_mfma_f32_16x16x32_bf16 v[196:199], a[136:139], a[24:27], v[196:199]\n"
"ds_read_b128 a[152:155], v216 offset:53760\n"
"v_cvt_pk_bf16_f32 v234, v234, v235\n"
"v_mfma_f32_16x16x32_bf16 v[196:199], a[132:135], a[20:23], v[196:199]\n"
"v_exp_f32 v255, v255\n"
"v_mfma_f32_16x16x32_bf16 v[196:199], a[128:131], a[16:19], v[196:199]\n"
"v_cvt_pk_bf16_f32 v159, v254, v255\n"
"v_accvgpr_write_b32 a116, v228\n"
"v_mfma_f32_16x16x32_bf16 v[248:251], a[128:131], a[32:35], -4.0\n"
"v_exp_f32 v192, v192\n"
"v_mfma_f32_16x16x32_bf16 v[248:251], a[132:135], a[36:39], v[248:251]\n"
"ds_read_b128 a[156:159], v216 offset:54016\n"
"v_accvgpr_write_b32 a117, v230\n"
"v_mfma_f32_16x16x32_bf16 v[248:251], a[136:139], a[40:43], v[248:251]\n"
"v_exp_f32 v193, v193\n"
"v_mfma_f32_16x16x32_bf16 v[248:251], a[140:143], a[44:47], v[248:251]\n"
"ds_read_b64_tr_b16 a[176:177], v217 offset:33024\n"
"v_cvt_pk_bf16_f32 v160, v192, v193\n"
"v_mfma_f32_16x16x32_bf16 v[252:255], a[140:143], a[60:63], -4.0\n"
"v_exp_f32 v194, v194\n"
"v_mfma_f32_16x16x32_bf16 v[252:255], a[136:139], a[56:59], v[252:255]\n"
"ds_read_b64_tr_b16 a[178:179], v217 offset:37120\n"
"v_accvgpr_write_b32 a118, v232\n"
"v_mfma_f32_16x16x32_bf16 v[252:255], a[132:135], a[52:55], v[252:255]\n"
"s_add_u32 m0, s65, 0\n"
"v_exp_f32 v195, v195\n"
"v_mfma_f32_16x16x32_bf16 v[252:255], a[128:131], a[48:51], v[252:255]\n"
"global_load_lds_dwordx4 v219, s[50:51] offset:3072\n"
"v_cvt_pk_bf16_f32 v161, v194, v195\n"
"v_smfmac_f32_16x16x64_bf16 a[192:195], a[208:211], v[128:135], v215\n"
"v_accvgpr_write_b32 a119, v234\n"
"v_smfmac_f32_16x16x64_bf16 a[196:199], a[208:211], v[136:143], v215\n"
"ds_read_b64_tr_b16 a[180:181], v217 offset:41216\n"
"v_smfmac_f32_16x16x64_bf16 a[200:203], a[208:211], v[144:151], v215\n"
"v_lshlrev_b32 v228, 16, v244\n"
"v_smfmac_f32_16x16x64_bf16 a[204:207], a[208:211], v[152:159], v215\n"
"ds_read_b64_tr_b16 a[182:183], v217 offset:45312\n"
"v_mfma_f32_16x16x32_bf16 v[0:3], a[160:163], v[128:131], v[0:3]\n"
"v_exp_f32 v196, v196\n"
"v_mfma_f32_16x16x32_bf16 v[0:3], a[164:167], v[132:135], v[0:3]\n"
"s_waitcnt lgkmcnt(5)\n"
"ds_read_b64_tr_b16 a[184:185], v217 offset:33032\n"
"v_and_b32 v229, s67, v244\n"
"v_mfma_f32_16x16x32_bf16 v[4:7], a[172:175], v[132:135], v[4:7]\n"
"v_exp_f32 v197, v197\n"
"v_mfma_f32_16x16x32_bf16 v[4:7], a[168:171], v[128:131], v[4:7]\n"
"buffer_store_dwordx4 a[224:227], v223, s[56:59], s37 offen offset:128 sc1 nt\n"
"v_cvt_pk_bf16_f32 v168, v196, v197\n"
"v_lshlrev_b32 v230, 16, v245\n"
"v_and_b32 v231, s67, v245\n"
"v_mfma_f32_16x16x32_bf16 v[12:15], a[168:171], v[136:139], v[12:15]\n"
"v_exp_f32 v198, v198\n"
"v_mfma_f32_16x16x32_bf16 v[12:15], a[172:175], v[140:143], v[12:15]\n"
"s_bfe_u32 s48, s38, 0x30004\n"
"ds_read_b64_tr_b16 a[186:187], v217 offset:37128\n"
"v_lshlrev_b32 v232, 16, v246\n"
"v_mfma_f32_16x16x32_bf16 v[8:11], a[164:167], v[140:143], v[8:11]\n"
"s_lshl_b32 s48, s48, 14\n"
"v_exp_f32 v199, v199\n"
"v_mfma_f32_16x16x32_bf16 v[8:11], a[160:163], v[136:139], v[8:11]\n"
"s_add_u32 s36, s79, s48\n"
"ds_read_b64_tr_b16 a[188:189], v217 offset:41224\n"
"v_cvt_pk_bf16_f32 v169, v198, v199\n"
"v_mfma_f32_16x16x32_bf16 v[16:19], a[160:163], v[144:147], v[16:19]\n"
"s_add_u32 s48, s42, s36\n"
"v_exp_f32 v248, v248\n"
"v_mfma_f32_16x16x32_bf16 v[16:19], a[164:167], v[148:151], v[16:19]\n"
"s_addc_u32 s49, s43, 0\n"
"ds_read_b64_tr_b16 a[190:191], v217 offset:45320\n"
"v_and_b32 v233, s67, v246\n"
"v_mfma_f32_16x16x32_bf16 v[20:23], a[172:175], v[148:151], v[20:23]\n"
"s_add_u32 m0, s64, 16384\n"
"v_exp_f32 v249, v249\n"
"v_mfma_f32_16x16x32_bf16 v[20:23], a[168:171], v[144:147], v[20:23]\n"
"global_load_lds_dwordx4 v219, s[48:49] offset:0\n"
"v_cvt_pk_bf16_f32 v176, v248, v249\n"
"v_mfma_f32_16x16x32_bf16 v[28:31], a[168:171], v[152:155], v[28:31]\n"
"v_exp_f32 v250, v250\n"
"v_mfma_f32_16x16x32_bf16 v[28:31], a[172:175], v[156:159], v[28:31]\n"
"v_lshlrev_b32 v234, 16, v247\n"
"v_and_b32 v235, s67, v247\n"
"v_mfma_f32_16x16x32_bf16 v[24:27], a[164:167], v[156:159], v[24:27]\n"
"v_exp_f32 v251, v251\n"
"v_mfma_f32_16x16x32_bf16 v[24:27], a[160:163], v[152:155], v[24:27]\n"
"v_cvt_pk_bf16_f32 v177, v250, v251\n"
"v_mul_f32 v228, s68, v228\n"
"v_mfma_f32_16x16x32_bf16 v[192:195], a[144:147], a[0:3], -4.0\n"
"v_exp_f32 v252, v252\n"
"v_mfma_f32_16x16x32_bf16 v[192:195], a[148:151], a[4:7], v[192:195]\n"
"ds_read_b128 a[128:131], v216 offset:57344\n"
"v_mul_f32 v229, s68, v229\n"
"v_mfma_f32_16x16x32_bf16 v[192:195], a[152:155], a[8:11], v[192:195]\n"
"s_waitcnt lgkmcnt(4)\n"
"v_exp_f32 v253, v253\n"
"v_mfma_f32_16x16x32_bf16 v[192:195], a[156:159], a[12:15], v[192:195]\n"
"ds_read_b128 a[132:135], v216 offset:57600\n"
"v_cvt_pk_bf16_f32 v184, v252, v253\n"
"v_mfma_f32_16x16x32_bf16 v[196:199], a[156:159], a[28:31], -4.0\n"
"v_exp_f32 v254, v254\n"
"v_mfma_f32_16x16x32_bf16 v[196:199], a[152:155], a[24:27], v[196:199]\n"
"ds_read_b128 a[136:139], v216 offset:57856\n"
"v_mul_f32 v230, s68, v230\n"
"v_mfma_f32_16x16x32_bf16 v[196:199], a[148:151], a[20:23], v[196:199]\n"
"s_add_u32 m0, s64, 16384\n"
"v_exp_f32 v255, v255\n"
"v_mfma_f32_16x16x32_bf16 v[196:199], a[144:147], a[16:19], v[196:199]\n"
"global_load_lds_dwordx4 v219, s[48:49] offset:1024\n"
"v_cvt_pk_bf16_f32 v185, v254, v255\n"
"v_mfma_f32_16x16x32_bf16 v[248:251], a[144:147], a[32:35], -4.0\n"
"v_exp_f32 v192, v192\n"
"v_mfma_f32_16x16x32_bf16 v[248:251], a[148:151], a[36:39], v[248:251]\n"
"ds_read_b128 a[140:143], v216 offset:58112\n"
"v_mul_f32 v231, s68, v231\n"
"v_mfma_f32_16x16x32_bf16 v[248:251], a[152:155], a[40:43], v[248:251]\n"
"v_exp_f32 v193, v193\n"
"v_mfma_f32_16x16x32_bf16 v[248:251], a[156:159], a[44:47], v[248:251]\n"
"ds_read_b64_tr_b16 a[160:161], v217 offset:33280\n"
"v_cvt_pk_bf16_f32 v162, v192, v193\n"
"v_mfma_f32_16x16x32_bf16 v[252:255], a[156:159], a[60:63], -4.0\n"
"v_exp_f32 v194, v194\n"
"v_mfma_f32_16x16x32_bf16 v[252:255], a[152:155], a[56:59], v[252:255]\n"
"ds_read_b64_tr_b16 a[162:163], v217 offset:37376\n"
"v_mul_f32 v232, s68, v232\n"
"v_mfma_f32_16x16x32_bf16 v[252:255], a[148:151], a[52:55], v[252:255]\n"
"s_add_u32 m0, s64, 16384\n"
"v_exp_f32 v195, v195\n"
"v_mfma_f32_16x16x32_bf16 v[252:255], a[144:147], a[48:51], v[252:255]\n"
"global_load_lds_dwordx4 v220, s[48:49] offset:2048\n"
"v_cvt_pk_bf16_f32 v163, v194, v195\n"
"v_mfma_f32_16x16x32_bf16 v[32:35], a[176:179], v[128:131], v[32:35]\n"
"v_exp_f32 v196, v196\n"
"v_mfma_f32_16x16x32_bf16 v[32:35], a[180:183], v[132:135], v[32:35]\n"
"s_waitcnt lgkmcnt(5)\n"
"ds_read_b64_tr_b16 a[164:165], v217 offset:41472\n"
"v_mul_f32 v233, s68, v233\n"
"v_mfma_f32_16x16x32_bf16 v[36:39], a[188:191], v[132:135], v[36:39]\n"
"v_exp_f32 v197, v197\n"
"v_mfma_f32_16x16x32_bf16 v[36:39], a[184:187], v[128:131], v[36:39]\n"
"ds_read_b64_tr_b16 a[166:167], v217 offset:45568\n"
"v_cvt_pk_bf16_f32 v170, v196, v197\n"
"v_mfma_f32_16x16x32_bf16 v[44:47], a[184:187], v[136:139], v[44:47]\n"
"v_exp_f32 v198, v198\n"
"v_mfma_f32_16x16x32_bf16 v[44:47], a[188:191], v[140:143], v[44:47]\n"
"ds_read_b64_tr_b16 a[168:169], v217 offset:33288\n"
"v_mul_f32 v234, s68, v234\n"
"v_mfma_f32_16x16x32_bf16 v[40:43], a[180:183], v[140:143], v[40:43]\n"
"s_add_u32 m0, s66, 0\n"
"v_exp_f32 v199, v199\n"
"v_mfma_f32_16x16x32_bf16 v[40:43], a[176:179], v[136:139], v[40:43]\n"
"global_load_lds_dwordx4 v222, s[52:53] offset:3072\n"
"v_cvt_pk_bf16_f32 v171, v198, v199\n"
"v_mfma_f32_16x16x32_bf16 v[48:51], a[176:179], v[144:147], v[48:51]\n"
"v_exp_f32 v248, v248\n"
"v_mfma_f32_16x16x32_bf16 v[48:51], a[180:183], v[148:151], v[48:51]\n"
"ds_read_b64_tr_b16 a[170:171], v217 offset:37384\n"
"v_mul_f32 v235, s68, v235\n"
"v_mfma_f32_16x16x32_bf16 v[52:55], a[188:191], v[148:151], v[52:55]\n"
"v_exp_f32 v249, v249\n"
"v_mfma_f32_16x16x32_bf16 v[52:55], a[184:187], v[144:147], v[52:55]\n"
"ds_read_b64_tr_b16 a[172:173], v217 offset:41480\n"
"v_cvt_pk_bf16_f32 v178, v248, v249\n"
"v_mfma_f32_16x16x32_bf16 v[60:63], a[184:187], v[152:155], v[60:63]\n"
"v_exp_f32 v250, v250\n"
"v_mfma_f32_16x16x32_bf16 v[60:63], a[188:191], v[156:159], v[60:63]\n"
"ds_read_b64_tr_b16 a[174:175], v217 offset:45576\n"
"v_cvt_pk_bf16_f32 v228, v228, v229\n"
"v_mfma_f32_16x16x32_bf16 v[56:59], a[180:183], v[156:159], v[56:59]\n"
"s_add_u32 m0, s64, 16384\n"
"v_exp_f32 v251, v251\n"
"v_mfma_f32_16x16x32_bf16 v[56:59], a[176:179], v[152:155], v[56:59]\n"
"global_load_lds_dwordx4 v220, s[48:49] offset:3072\n"
"v_cvt_pk_bf16_f32 v179, v250, v251\n"
"v_mfma_f32_16x16x32_bf16 v[192:195], a[128:131], a[0:3], -4.0\n"
"s_waitcnt lgkmcnt(6)\n"
"v_exp_f32 v252, v252\n"
"v_mfma_f32_16x16x32_bf16 v[192:195], a[132:135], a[4:7], v[192:195]\n"
"ds_read_b128 a[144:147], v216 offset:61440\n"
"v_cvt_pk_bf16_f32 v230, v230, v231\n"
"v_mfma_f32_16x16x32_bf16 v[192:195], a[136:139], a[8:11], v[192:195]\n"
"v_exp_f32 v253, v253\n"
"v_mfma_f32_16x16x32_bf16 v[192:195], a[140:143], a[12:15], v[192:195]\n"
"ds_read_b128 a[148:151], v216 offset:61696\n"
"v_cvt_pk_bf16_f32 v186, v252, v253\n"
"v_mfma_f32_16x16x32_bf16 v[196:199], a[140:143], a[28:31], -4.0\n"
"v_exp_f32 v254, v254\n"
"v_mfma_f32_16x16x32_bf16 v[196:199], a[136:139], a[24:27], v[196:199]\n"
"ds_read_b128 a[152:155], v216 offset:61952\n"
"v_cvt_pk_bf16_f32 v232, v232, v233\n"
"v_mfma_f32_16x16x32_bf16 v[196:199], a[132:135], a[20:23], v[196:199]\n"
"v_exp_f32 v255, v255\n"
"v_mfma_f32_16x16x32_bf16 v[196:199], a[128:131], a[16:19], v[196:199]\n"
"v_cvt_pk_bf16_f32 v187, v254, v255\n"
"v_cvt_pk_bf16_f32 v234, v234, v235\n"
"v_mfma_f32_16x16x32_bf16 v[248:251], a[128:131], a[32:35], -4.0\n"
"v_exp_f32 v192, v192\n"
"v_mfma_f32_16x16x32_bf16 v[248:251], a[132:135], a[36:39], v[248:251]\n"
"ds_read_b128 a[156:159], v216 offset:62208\n"
"v_accvgpr_write_b32 a120, v228\n"
"v_mfma_f32_16x16x32_bf16 v[248:251], a[136:139], a[40:43], v[248:251]\n"
"v_exp_f32 v193, v193\n"
"v_mfma_f32_16x16x32_bf16 v[248:251], a[140:143], a[44:47], v[248:251]\n"
"ds_read_b64_tr_b16 a[176:177], v217 offset:33536\n"
"v_cvt_pk_bf16_f32 v164, v192, v193\n"
"v_mfma_f32_16x16x32_bf16 v[252:255], a[140:143], a[60:63], -4.0\n"
"v_exp_f32 v194, v194\n"
"v_mfma_f32_16x16x32_bf16 v[252:255], a[136:139], a[56:59], v[252:255]\n"
"ds_read_b64_tr_b16 a[178:179], v217 offset:37632\n"
"v_accvgpr_write_b32 a121, v230\n"
"v_mfma_f32_16x16x32_bf16 v[252:255], a[132:135], a[52:55], v[252:255]\n"
"v_exp_f32 v195, v195\n"
"v_mfma_f32_16x16x32_bf16 v[252:255], a[128:131], a[48:51], v[252:255]\n"
"v_cvt_pk_bf16_f32 v165, v194, v195\n"
"v_accvgpr_write_b32 a122, v232\n"
"v_mfma_f32_16x16x32_bf16 v[64:67], a[160:163], v[128:131], v[64:67]\n"
"s_waitcnt lgkmcnt(6)\n"
"v_exp_f32 v196, v196\n"
"v_mfma_f32_16x16x32_bf16 v[64:67], a[164:167], v[132:135], v[64:67]\n"
"ds_read_b64_tr_b16 a[180:181], v217 offset:41728\n"
"v_accvgpr_write_b32 a123, v234\n"
"v_mfma_f32_16x16x32_bf16 v[68:71], a[172:175], v[132:135], v[68:71]\n"
"v_exp_f32 v197, v197\n"
"v_mfma_f32_16x16x32_bf16 v[68:71], a[168:171], v[128:131], v[68:71]\n"
"ds_read_b64_tr_b16 a[182:183], v217 offset:45824\n"
"v_cvt_pk_bf16_f32 v172, v196, v197\n"
"v_mfma_f32_16x16x32_bf16 v[76:79], a[168:171], v[136:139], v[76:79]\n"
"v_exp_f32 v198, v198\n"
"v_mfma_f32_16x16x32_bf16 v[76:79], a[172:175], v[140:143], v[76:79]\n"
"ds_read_b64_tr_b16 a[184:185], v217 offset:33544\n"
"v_lshlrev_b32 v228, 16, v200\n"
"v_mfma_f32_16x16x32_bf16 v[72:75], a[164:167], v[140:143], v[72:75]\n"
"v_exp_f32 v199, v199\n"
"v_mfma_f32_16x16x32_bf16 v[72:75], a[160:163], v[136:139], v[72:75]\n"
"buffer_store_dwordx4 a[228:231], v223, s[56:59], s37 offen offset:2176 sc1 nt\n"
"v_cvt_pk_bf16_f32 v173, v198, v199\n"
"v_and_b32 v229, s67, v200\n"
"v_lshlrev_b32 v230, 16, v201\n"
"v_mfma_f32_16x16x32_bf16 v[80:83], a[160:163], v[144:147], v[80:83]\n"
"v_exp_f32 v248, v248\n"
"v_mfma_f32_16x16x32_bf16 v[80:83], a[164:167], v[148:151], v[80:83]\n"
"ds_read_b64_tr_b16 a[186:187], v217 offset:37640\n"
"v_and_b32 v231, s67, v201\n"
"v_mfma_f32_16x16x32_bf16 v[84:87], a[172:175], v[148:151], v[84:87]\n"
"v_exp_f32 v249, v249\n"
"v_mfma_f32_16x16x32_bf16 v[84:87], a[168:171], v[144:147], v[84:87]\n"
"ds_read_b64_tr_b16 a[188:189], v217 offset:41736\n"
"v_cvt_pk_bf16_f32 v180, v248, v249\n"
"v_mfma_f32_16x16x32_bf16 v[92:95], a[168:171], v[152:155], v[92:95]\n"
"v_exp_f32 v250, v250\n"
"v_mfma_f32_16x16x32_bf16 v[92:95], a[172:175], v[156:159], v[92:95]\n"
"ds_read_b64_tr_b16 a[190:191], v217 offset:45832\n"
"v_lshlrev_b32 v232, 16, v202\n"
"v_and_b32 v233, s67, v202\n"
"v_lshlrev_b32 v234, 16, v203\n"
"v_and_b32 v235, s67, v203\n"
"v_pk_mul_f32 v[228:229], v[228:229], s[68:69]\n"
"v_pk_mul_f32 v[230:231], v[230:231], s[68:69]\n"
"v_pk_mul_f32 v[232:233], v[232:233], s[68:69]\n"
"v_pk_mul_f32 v[234:235], v[234:235], s[68:69]\n"
"v_cvt_pk_bf16_f32 v228, v228, v229\n"
"v_cvt_pk_bf16_f32 v230, v230, v231\n"
"v_mfma_f32_16x16x32_bf16 v[88:91], a[164:167], v[156:159], v[88:91]\n"
"v_exp_f32 v251, v251\n"
"v_mfma_f32_16x16x32_bf16 v[88:91], a[160:163], v[152:155], v[88:91]\n"
"s_waitcnt vmcnt(13) lgkmcnt(5)\n"
"v_cvt_pk_bf16_f32 v181, v250, v251\n"
"v_cvt_pk_bf16_f32 v232, v232, v233\n"
"v_mfma_f32_16x16x32_bf16 v[192:195], a[144:147], a[0:3], -4.0\n"
"s_barrier\n"
"v_exp_f32 v252, v252\n"
"v_mfma_f32_16x16x32_bf16 v[192:195], a[148:151], a[4:7], v[192:195]\n"
"s_bfe_u32 s50, s38, 0x30004\n"
"ds_read_b128 a[128:131], v216 offset:0\n"
"v_cvt_pk_bf16_f32 v234, v234, v235\n"
"v_mfma_f32_16x16x32_bf16 v[192:195], a[152:155], a[8:11], v[192:195]\n"
"s_lshl_b32 s50, s50, 14\n"
"v_exp_f32 v253, v253\n"
"v_mfma_f32_16x16x32_bf16 v[192:195], a[156:159], a[12:15], v[192:195]\n"
"s_add_u32 s39, s54, s50\n"
"ds_read_b128 a[132:135], v216 offset:256\n"
"v_cvt_pk_bf16_f32 v188, v252, v253\n"
"v_mfma_f32_16x16x32_bf16 v[196:199], a[156:159], a[28:31], -4.0\n"
"s_add_u32 s50, s44, s39\n"
"v_exp_f32 v254, v254\n"
"v_mfma_f32_16x16x32_bf16 v[196:199], a[152:155], a[24:27], v[196:199]\n"
"s_addc_u32 s51, s45, 0\n"
"ds_read_b128 a[136:139], v216 offset:512\n"
"v_accvgpr_write_b32 a124, v228\n"
"v_mfma_f32_16x16x32_bf16 v[196:199], a[148:151], a[20:23], v[196:199]\n"
"s_add_u32 m0, s65, 16384\n"
"v_exp_f32 v255, v255\n"
"v_mfma_f32_16x16x32_bf16 v[196:199], a[144:147], a[16:19], v[196:199]\n"
"global_load_lds_dwordx4 v219, s[50:51] offset:0\n"
"v_cvt_pk_bf16_f32 v189, v254, v255\n"
"v_mfma_f32_16x16x32_bf16 v[248:251], a[144:147], a[32:35], -4.0\n"
"v_exp_f32 v192, v192\n"
"v_mfma_f32_16x16x32_bf16 v[248:251], a[148:151], a[36:39], v[248:251]\n"
"ds_read_b128 a[140:143], v216 offset:768\n"
"v_accvgpr_write_b32 a125, v230\n"
"v_mfma_f32_16x16x32_bf16 v[248:251], a[152:155], a[40:43], v[248:251]\n"
"v_exp_f32 v193, v193\n"
"v_mfma_f32_16x16x32_bf16 v[248:251], a[156:159], a[44:47], v[248:251]\n"
"s_add_u32 s35, s55, s70\n"
"ds_read_b64_tr_b16 a[160:161], v217 offset:49152\n"
"v_cvt_pk_bf16_f32 v166, v192, v193\n"
"v_mfma_f32_16x16x32_bf16 v[252:255], a[156:159], a[60:63], -4.0\n"
"s_add_u32 s52, s40, s35\n"
"v_exp_f32 v194, v194\n"
"v_mfma_f32_16x16x32_bf16 v[252:255], a[152:155], a[56:59], v[252:255]\n"
"s_addc_u32 s53, s41, 0\n"
"ds_read_b64_tr_b16 a[162:163], v217 offset:53248\n"
"v_accvgpr_write_b32 a126, v232\n"
"v_mfma_f32_16x16x32_bf16 v[252:255], a[148:151], a[52:55], v[252:255]\n"
"s_add_u32 m0, s66, 4096\n"
"v_exp_f32 v195, v195\n"
"v_mfma_f32_16x16x32_bf16 v[252:255], a[144:147], a[48:51], v[252:255]\n"
"global_load_lds_dwordx4 v221, s[52:53] offset:0\n"
"v_cvt_pk_bf16_f32 v167, v194, v195\n"
"v_mfma_f32_16x16x32_bf16 v[96:99], a[176:179], v[128:131], v[96:99]\n"
"s_waitcnt lgkmcnt(6)\n"
"v_exp_f32 v196, v196\n"
"v_mfma_f32_16x16x32_bf16 v[96:99], a[180:183], v[132:135], v[96:99]\n"
"ds_read_b64_tr_b16 a[164:165], v217 offset:57344\n"
"v_accvgpr_write_b32 a127, v234\n"
"v_mfma_f32_16x16x32_bf16 v[100:103], a[188:191], v[132:135], v[100:103]\n"
"v_exp_f32 v197, v197\n"
"v_mfma_f32_16x16x32_bf16 v[100:103], a[184:187], v[128:131], v[100:103]\n"
"ds_read_b64_tr_b16 a[166:167], v217 offset:61440\n"
"v_cvt_pk_bf16_f32 v174, v196, v197\n"
"v_mfma_f32_16x16x32_bf16 v[108:111], a[184:187], v[136:139], v[108:111]\n"
"v_exp_f32 v198, v198\n"
"v_mfma_f32_16x16x32_bf16 v[108:111], a[188:191], v[140:143], v[108:111]\n"
"ds_read_b64_tr_b16 a[168:169], v217 offset:49160\n"
"v_mfma_f32_16x16x32_bf16 v[104:107], a[180:183], v[140:143], v[104:107]\n"
"s_add_u32 m0, s65, 16384\n"
"v_exp_f32 v199, v199\n"
"v_mfma_f32_16x16x32_bf16 v[104:107], a[176:179], v[136:139], v[104:107]\n"
"global_load_lds_dwordx4 v219, s[50:51] offset:1024\n"
"v_cvt_pk_bf16_f32 v175, v198, v199\n"
"v_mfma_f32_16x16x32_bf16 v[112:115], a[176:179], v[144:147], v[112:115]\n"
"s_add_u32 s56, s46, s98\n"
"v_exp_f32 v248, v248\n"
"v_mfma_f32_16x16x32_bf16 v[112:115], a[180:183], v[148:151], v[112:115]\n"
"s_addc_u32 s57, s47, 0\n"
"ds_read_b64_tr_b16 a[170:171], v217 offset:53256\n"
"v_mfma_f32_16x16x32_bf16 v[116:119], a[188:191], v[148:151], v[116:119]\n"
"s_mov_b32 s58, 16384\n"
"v_exp_f32 v249, v249\n"
"v_mfma_f32_16x16x32_bf16 v[116:119], a[184:187], v[144:147], v[116:119]\n"
"ds_read_b64_tr_b16 a[172:173], v217 offset:57352\n"
"v_cvt_pk_bf16_f32 v182, v248, v249\n"
"v_mfma_f32_16x16x32_bf16 v[124:127], a[184:187], v[152:155], v[124:127]\n"
"v_exp_f32 v250, v250\n"
"v_mfma_f32_16x16x32_bf16 v[124:127], a[188:191], v[156:159], v[124:127]\n"
"ds_read_b64_tr_b16 a[174:175], v217 offset:61448\n"
"v_mfma_f32_16x16x32_bf16 v[120:123], a[180:183], v[156:159], v[120:123]\n"
"s_add_u32 m0, s65, 16384\n"
"v_exp_f32 v251, v251\n"
"v_mfma_f32_16x16x32_bf16 v[120:123], a[176:179], v[152:155], v[120:123]\n"
"s_waitcnt lgkmcnt(6)\n"
"global_load_lds_dwordx4 v219, s[50:51] offset:2048\n"
"v_cvt_pk_bf16_f32 v183, v250, v251\n"
"v_mfma_f32_16x16x32_bf16 v[192:195], a[128:131], a[64:67], -4.0\n"
".Lattn_pad10:\n"
"s_cbranch_vccnz .Lattn_ret\n"
"v_exp_f32 v252, v252\n"
"v_mfma_f32_16x16x32_bf16 v[192:195], a[132:135], a[68:71], v[192:195]\n"
"ds_read_b128 a[144:147], v216 offset:4096\n"
"v_mfma_f32_16x16x32_bf16 v[192:195], a[136:139], a[72:75], v[192:195]\n"
"v_exp_f32 v253, v253\n"
"v_mfma_f32_16x16x32_bf16 v[192:195], a[140:143], a[76:79], v[192:195]\n"
"ds_read_b128 a[148:151], v216 offset:4352\n"
"v_cvt_pk_bf16_f32 v190, v252, v253\n"
"v_mfma_f32_16x16x32_bf16 v[196:199], a[140:143], a[92:95], -4.0\n"
"v_exp_f32 v254, v254\n"
"v_mfma_f32_16x16x32_bf16 v[196:199], a[136:139], a[88:91], v[196:199]\n"
"ds_read_b128 a[152:155], v216 offset:4608\n"
"v_mfma_f32_16x16x32_bf16 v[196:199], a[132:135], a[84:87], v[196:199]\n"
"v_exp_f32 v255, v255\n"
"v_mfma_f32_16x16x32_bf16 v[196:199], a[128:131], a[80:83], v[196:199]\n"
"v_cvt_pk_bf16_f32 v191, v254, v255\n"
"v_mfma_f32_16x16x32_bf16 v[248:251], a[128:131], a[96:99], -4.0\n"
"v_exp_f32 v192, v192\n"
"v_mfma_f32_16x16x32_bf16 v[248:251], a[132:135], a[100:103], v[248:251]\n"
"ds_read_b128 a[156:159], v216 offset:4864\n"
"v_mfma_f32_16x16x32_bf16 v[248:251], a[136:139], a[104:107], v[248:251]\n"
"v_exp_f32 v193, v193\n"
"v_mfma_f32_16x16x32_bf16 v[248:251], a[140:143], a[108:111], v[248:251]\n"
"ds_read_b64_tr_b16 a[176:177], v217 offset:49408\n"
"v_cvt_pk_bf16_f32 v128, v192, v193\n"
"v_mfma_f32_16x16x32_bf16 v[252:255], a[140:143], a[124:127], -4.0\n"
"v_exp_f32 v194, v194\n"
"v_mfma_f32_16x16x32_bf16 v[252:255], a[136:139], a[120:123], v[252:255]\n"
"ds_read_b64_tr_b16 a[178:179], v217 offset:53504\n"
"v_mfma_f32_16x16x32_bf16 v[252:255], a[132:135], a[116:119], v[252:255]\n"
"s_add_u32 m0, s65, 16384\n"
"v_exp_f32 v195, v195\n"
"v_mfma_f32_16x16x32_bf16 v[252:255], a[128:131], a[112:115], v[252:255]\n"
"global_load_lds_dwordx4 v219, s[50:51] offset:3072\n"
"v_cvt_pk_bf16_f32 v129, v194, v195\n"
"v_smfmac_f32_16x16x64_bf16 a[192:195], a[208:211], v[160:167], v215\n"
"v_smfmac_f32_16x16x64_bf16 a[196:199], a[208:211], v[168:175], v215\n"
"ds_read_b64_tr_b16 a[180:181], v217 offset:57600\n"
"v_smfmac_f32_16x16x64_bf16 a[200:203], a[208:211], v[176:183], v215\n"
"v_smfmac_f32_16x16x64_bf16 a[204:207], a[208:211], v[184:191], v215\n"
"ds_read_b64_tr_b16 a[182:183], v217 offset:61696\n"
"v_mfma_f32_16x16x32_bf16 v[0:3], a[160:163], v[160:163], v[0:3]\n"
"s_waitcnt lgkmcnt(7)\n"
"v_exp_f32 v196, v196\n"
"v_mfma_f32_16x16x32_bf16 v[0:3], a[164:167], v[164:167], v[0:3]\n"
"ds_read_b64_tr_b16 a[184:185], v217 offset:49416\n"
"v_accvgpr_read_b32 v208, a192\n"
"v_accvgpr_read_b32 v213, a193\n"
"v_mfma_f32_16x16x32_bf16 v[4:7], a[172:175], v[164:167], v[4:7]\n"
"v_exp_f32 v197, v197\n"
"v_mfma_f32_16x16x32_bf16 v[4:7], a[168:171], v[160:163], v[4:7]\n"
"v_cvt_pk_bf16_f32 v136, v196, v197\n"
"v_add_f32 v208, v208, v213\n"
"v_accvgpr_read_b32 v210, a196\n"
"v_mfma_f32_16x16x32_bf16 v[12:15], a[168:171], v[168:171], v[12:15]\n"
"v_exp_f32 v198, v198\n"
"v_mfma_f32_16x16x32_bf16 v[12:15], a[172:175], v[172:175], v[12:15]\n"
"s_bfe_u32 s48, s38, 0x30008\n"
"ds_read_b64_tr_b16 a[186:187], v217 offset:53512\n"
"v_accvgpr_read_b32 v213, a197\n"
"v_add_f32 v210, v210, v213\n"
"v_mfma_f32_16x16x32_bf16 v[8:11], a[164:167], v[172:175], v[8:11]\n"
"s_lshl_b32 s48, s48, 14\n"
"v_exp_f32 v199, v199\n"
"v_mfma_f32_16x16x32_bf16 v[8:11], a[160:163], v[168:171], v[8:11]\n"
"s_add_u32 s36, s79, s48\n"
"ds_read_b64_tr_b16 a[188:189], v217 offset:57608\n"
"v_cvt_pk_bf16_f32 v137, v198, v199\n"
"v_max3_u32 v209, v209, v208, v210\n"
"v_min3_u32 v211, v211, v208, v210\n"
"v_mfma_f32_16x16x32_bf16 v[16:19], a[160:163], v[176:179], v[16:19]\n"
"s_add_u32 s48, s42, s36\n"
"v_exp_f32 v248, v248\n"
"v_mfma_f32_16x16x32_bf16 v[16:19], a[164:167], v[180:183], v[16:19]\n"
"s_addc_u32 s49, s43, 0\n"
"ds_read_b64_tr_b16 a[190:191], v217 offset:61704\n"
"v_rcp_f32 v208, v208\n"
"v_rcp_f32 v210, v210\n"
"v_mfma_f32_16x16x32_bf16 v[20:23], a[172:175], v[180:183], v[20:23]\n"
"s_add_u32 m0, s64, 32768\n"
"v_exp_f32 v249, v249\n"
"v_mfma_f32_16x16x32_bf16 v[20:23], a[168:171], v[176:179], v[20:23]\n"
"global_load_lds_dwordx4 v219, s[48:49] offset:0\n"
"v_cvt_pk_bf16_f32 v144, v248, v249\n"
"v_accvgpr_read_b32 v212, a200\n"
"v_accvgpr_read_b32 v213, a201\n"
"v_mfma_f32_16x16x32_bf16 v[28:31], a[168:171], v[184:187], v[28:31]\n"
"v_exp_f32 v250, v250\n"
"v_mfma_f32_16x16x32_bf16 v[28:31], a[172:175], v[188:191], v[28:31]\n"
"v_add_f32 v212, v212, v213\n"
"v_accvgpr_read_b32 v214, a204\n"
"v_mfma_f32_16x16x32_bf16 v[24:27], a[164:167], v[188:191], v[24:27]\n"
"v_exp_f32 v251, v251\n"
"v_mfma_f32_16x16x32_bf16 v[24:27], a[160:163], v[184:187], v[24:27]\n"
"v_cvt_pk_bf16_f32 v145, v250, v251\n"
"v_accvgpr_read_b32 v213, a205\n"
"v_add_f32 v214, v214, v213\n"
"v_mfma_f32_16x16x32_bf16 v[192:195], a[144:147], a[64:67], -4.0\n"
"s_waitcnt lgkmcnt(3)\n"
"v_exp_f32 v252, v252\n"
"v_mfma_f32_16x16x32_bf16 v[192:195], a[148:151], a[68:71], v[192:195]\n"
"ds_read_b128 a[128:131], v216 offset:8192\n"
"v_max3_u32 v209, v209, v212, v214\n"
"v_mfma_f32_16x16x32_bf16 v[192:195], a[152:155], a[72:75], v[192:195]\n"
"v_exp_f32 v253, v253\n"
"v_mfma_f32_16x16x32_bf16 v[192:195], a[156:159], a[76:79], v[192:195]\n"
"ds_read_b128 a[132:135], v216 offset:8448\n"
"v_cvt_pk_bf16_f32 v152, v252, v253\n"
"v_min3_u32 v211, v211, v212, v214\n"
"v_rcp_f32 v212, v212\n"
"v_mfma_f32_16x16x32_bf16 v[196:199], a[156:159], a[92:95], -4.0\n"
"v_exp_f32 v254, v254\n"
"v_mfma_f32_16x16x32_bf16 v[196:199], a[152:155], a[88:91], v[196:199]\n"
"ds_read_b128 a[136:139], v216 offset:8704\n"
"v_accvgpr_write_b32 a192, 0\n"
"v_mfma_f32_16x16x32_bf16 v[196:199], a[148:151], a[84:87], v[196:199]\n"
"s_add_u32 m0, s64, 32768\n"
"v_exp_f32 v255, v255\n"
"v_mfma_f32_16x16x32_bf16 v[196:199], a[144:147], a[80:83], v[196:199]\n"
"global_load_lds_dwordx4 v219, s[48:49] offset:1024\n"
"v_cvt_pk_bf16_f32 v153, v254, v255\n"
"v_rcp_f32 v214, v214\n"
"v_mfma_f32_16x16x32_bf16 v[248:251], a[144:147], a[96:99], -4.0\n"
"v_exp_f32 v192, v192\n"
"v_mfma_f32_16x16x32_bf16 v[248:251], a[148:151], a[100:103], v[248:251]\n"
"ds_read_b128 a[140:143], v216 offset:8960\n"
"v_accvgpr_write_b32 a193, 0\n"
"v_mfma_f32_16x16x32_bf16 v[248:251], a[152:155], a[104:107], v[248:251]\n"
"v_exp_f32 v193, v193\n"
"v_mfma_f32_16x16x32_bf16 v[248:251], a[156:159], a[108:111], v[248:251]\n"
"ds_read_b64_tr_b16 a[160:161], v217 offset:49664\n"
"v_cvt_pk_bf16_f32 v130, v192, v193\n"
"v_accvgpr_write_b32 a196, 0\n"
"v_accvgpr_write_b32 a197, 0\n"
"v_accvgpr_write_b32 a200, 0\n"
"v_pk_mul_f32 v[0:1], v[0:1], v[208:209] op_sel_hi:[1,0]\n"
"v_pk_mul_f32 v[2:3], v[2:3], v[208:209] op_sel_hi:[1,0]\n"
"v_pk_mul_f32 v[4:5], v[4:5], v[208:209] op_sel_hi:[1,0]\n"
"v_pk_mul_f32 v[6:7], v[6:7], v[208:209] op_sel_hi:[1,0]\n"
"v_cvt_pk_bf16_f32 v0, v0, v1\n"
"v_cvt_pk_bf16_f32 v1, v2, v3\n"
"v_mfma_f32_16x16x32_bf16 v[252:255], a[156:159], a[124:127], -4.0\n"
"v_exp_f32 v194, v194\n"
"v_mfma_f32_16x16x32_bf16 v[252:255], a[152:155], a[120:123], v[252:255]\n"
"ds_read_b64_tr_b16 a[162:163], v217 offset:53760\n"
"v_cvt_pk_bf16_f32 v2, v4, v5\n"
"v_mfma_f32_16x16x32_bf16 v[252:255], a[148:151], a[116:119], v[252:255]\n"
"s_add_u32 m0, s64, 32768\n"
"v_exp_f32 v195, v195\n"
"v_mfma_f32_16x16x32_bf16 v[252:255], a[144:147], a[112:115], v[252:255]\n"
"global_load_lds_dwordx4 v220, s[48:49] offset:2048\n"
"v_cvt_pk_bf16_f32 v131, v194, v195\n"
"v_mfma_f32_16x16x32_bf16 v[32:35], a[176:179], v[160:163], v[32:35]\n"
"v_exp_f32 v196, v196\n"
"v_mfma_f32_16x16x32_bf16 v[32:35], a[180:183], v[164:167], v[32:35]\n"
"s_waitcnt lgkmcnt(4)\n"
"ds_read_b64_tr_b16 a[164:165], v217 offset:57856\n"
"v_cvt_pk_bf16_f32 v3, v6, v7\n"
"v_mfma_f32_16x16x32_bf16 v[36:39], a[188:191], v[164:167], v[36:39]\n"
"v_exp_f32 v197, v197\n"
"v_mfma_f32_16x16x32_bf16 v[36:39], a[184:187], v[160:163], v[36:39]\n"
"ds_read_b64_tr_b16 a[166:167], v217 offset:61952\n"
"v_cvt_pk_bf16_f32 v138, v196, v197\n"
"v_mfma_f32_16x16x32_bf16 v[44:47], a[184:187], v[168:171], v[44:47]\n"
"v_exp_f32 v198, v198\n"
"v_mfma_f32_16x16x32_bf16 v[44:47], a[188:191], v[172:175], v[44:47]\n"
"ds_read_b64_tr_b16 a[168:169], v217 offset:49672\n"
"ds_write_b128 v224, v[0:3] offset:49152\n"
"v_pk_mul_f32 v[8:9], v[8:9], v[210:211] op_sel_hi:[1,0]\n"
"v_pk_mul_f32 v[10:11], v[10:11], v[210:211] op_sel_hi:[1,0]\n"
"v_pk_mul_f32 v[12:13], v[12:13], v[210:211] op_sel_hi:[1,0]\n"
"v_pk_mul_f32 v[14:15], v[14:15], v[210:211] op_sel_hi:[1,0]\n"
"v_cvt_pk_bf16_f32 v8, v8, v9\n"
"v_cvt_pk_bf16_f32 v9, v10, v11\n"
"v_mfma_f32_16x16x32_bf16 v[40:43], a[180:183], v[172:175], v[40:43]\n"
"v_exp_f32 v199, v199\n"
"v_mfma_f32_16x16x32_bf16 v[40:43], a[176:179], v[168:171], v[40:43]\n"
"v_cvt_pk_bf16_f32 v139, v198, v199\n"
"v_cvt_pk_bf16_f32 v10, v12, v13\n"
"v_mfma_f32_16x16x32_bf16 v[48:51], a[176:179], v[176:179], v[48:51]\n"
"v_exp_f32 v248, v248\n"
"v_mfma_f32_16x16x32_bf16 v[48:51], a[180:183], v[180:183], v[48:51]\n"
"ds_read_b64_tr_b16 a[170:171], v217 offset:53768\n"
"v_cvt_pk_bf16_f32 v11, v14, v15\n"
"v_mfma_f32_16x16x32_bf16 v[52:55], a[188:191], v[180:183], v[52:55]\n"
"v_exp_f32 v249, v249\n"
"v_mfma_f32_16x16x32_bf16 v[52:55], a[184:187], v[176:179], v[52:55]\n"
"ds_read_b64_tr_b16 a[172:173], v217 offset:57864\n"
"v_cvt_pk_bf16_f32 v146, v248, v249\n"
"v_mfma_f32_16x16x32_bf16 v[60:63], a[184:187], v[184:187], v[60:63]\n"
"v_exp_f32 v250, v250\n"
"v_mfma_f32_16x16x32_bf16 v[60:63], a[188:191], v[188:191], v[60:63]\n"
"ds_read_b64_tr_b16 a[174:175], v217 offset:61960\n"
"ds_write_b128 v224, v[8:11] offset:51200\n"
"v_pk_mul_f32 v[32:33], v[32:33], v[208:209] op_sel_hi:[1,0]\n"
"v_pk_mul_f32 v[34:35], v[34:35], v[208:209] op_sel_hi:[1,0]\n"
"v_pk_mul_f32 v[36:37], v[36:37], v[208:209] op_sel_hi:[1,0]\n"
"v_pk_mul_f32 v[38:39], v[38:39], v[208:209] op_sel_hi:[1,0]\n"
"v_cvt_pk_bf16_f32 v32, v32, v33\n"
"v_cvt_pk_bf16_f32 v33, v34, v35\n"
"v_mfma_f32_16x16x32_bf16 v[56:59], a[180:183], v[188:191], v[56:59]\n"
"s_add_u32 m0, s64, 32768\n"
"v_exp_f32 v251, v251\n"
"v_mfma_f32_16x16x32_bf16 v[56:59], a[176:179], v[184:187], v[56:59]\n"
"global_load_lds_dwordx4 v220, s[48:49] offset:3072\n"
"v_cvt_pk_bf16_f32 v147, v250, v251\n"
"v_cvt_pk_bf16_f32 v34, v36, v37\n"
"v_cvt_pk_bf16_f32 v35, v38, v39\n"
"v_mfma_f32_16x16x32_bf16 v[192:195], a[128:131], a[64:67], -4.0\n"
"v_exp_f32 v252, v252\n"
"v_mfma_f32_16x16x32_bf16 v[192:195], a[132:135], a[68:71], v[192:195]\n"
"s_waitcnt lgkmcnt(4)\n"
"ds_read_b128 a[144:147], v216 offset:12288\n"
"v_accvgpr_write_b32 a201, 0\n"
"v_mfma_f32_16x16x32_bf16 v[192:195], a[136:139], a[72:75], v[192:195]\n"
"v_exp_f32 v253, v253\n"
"v_mfma_f32_16x16x32_bf16 v[192:195], a[140:143], a[76:79], v[192:195]\n"
"ds_read_b128 a[148:151], v216 offset:12544\n"
"ds_write_b128 v225, v[32:35] offset:49152\n"
"v_cvt_pk_bf16_f32 v154, v252, v253\n"
"v_pk_mul_f32 v[40:41], v[40:41], v[210:211] op_sel_hi:[1,0]\n"
"v_pk_mul_f32 v[42:43], v[42:43], v[210:211] op_sel_hi:[1,0]\n"
"v_pk_mul_f32 v[44:45], v[44:45], v[210:211] op_sel_hi:[1,0]\n"
"v_pk_mul_f32 v[46:47], v[46:47], v[210:211] op_sel_hi:[1,0]\n"
"v_cvt_pk_bf16_f32 v40, v40, v41\n"
"v_cvt_pk_bf16_f32 v41, v42, v43\n"
"v_mfma_f32_16x16x32_bf16 v[196:199], a[140:143], a[92:95], -4.0\n"
"v_exp_f32 v254, v254\n"
"v_mfma_f32_16x16x32_bf16 v[196:199], a[136:139], a[88:91], v[196:199]\n"
"ds_read_b128 a[152:155], v216 offset:12800\n"
"v_cvt_pk_bf16_f32 v42, v44, v45\n"
"v_mfma_f32_16x16x32_bf16 v[196:199], a[132:135], a[84:87], v[196:199]\n"
"s_add_u32 m0, s66, 4096\n"
"v_exp_f32 v255, v255\n"
"v_mfma_f32_16x16x32_bf16 v[196:199], a[128:131], a[80:83], v[196:199]\n"
"global_load_lds_dwordx4 v221, s[52:53] offset:1024\n"
"v_cvt_pk_bf16_f32 v155, v254, v255\n"
"v_cvt_pk_bf16_f32 v43, v46, v47\n"
"v_mfma_f32_16x16x32_bf16 v[248:251], a[128:131], a[96:99], -4.0\n"
"v_exp_f32 v192, v192\n"
"v_mfma_f32_16x16x32_bf16 v[248:251], a[132:135], a[100:103], v[248:251]\n"
"ds_read_b128 a[156:159], v216 offset:13056\n"
"v_accvgpr_write_b32 a204, 0\n"
"v_mfma_f32_16x16x32_bf16 v[248:251], a[136:139], a[104:107], v[248:251]\n"
"v_exp_f32 v193, v193\n"
"v_mfma_f32_16x16x32_bf16 v[248:251], a[140:143], a[108:111], v[248:251]\n"
"ds_read_b64_tr_b16 a[176:177], v217 offset:49920\n"
"ds_write_b128 v225, v[40:43] offset:51200\n"
"v_cvt_pk_bf16_f32 v132, v192, v193\n"
"v_pk_mul_f32 v[16:17], v[16:17], v[212:213] op_sel_hi:[1,0]\n"
"v_pk_mul_f32 v[18:19], v[18:19], v[212:213] op_sel_hi:[1,0]\n"
"v_pk_mul_f32 v[20:21], v[20:21], v[212:213] op_sel_hi:[1,0]\n"
"v_pk_mul_f32 v[22:23], v[22:23], v[212:213] op_sel_hi:[1,0]\n"
"v_cvt_pk_bf16_f32 v16, v16, v17\n"
"v_cvt_pk_bf16_f32 v17, v18, v19\n"
"v_mfma_f32_16x16x32_bf16 v[252:255], a[140:143], a[124:127], -4.0\n"
"v_exp_f32 v194, v194\n"
"v_mfma_f32_16x16x32_bf16 v[252:255], a[136:139], a[120:123], v[252:255]\n"
"ds_read_b64_tr_b16 a[178:179], v217 offset:54016\n"
"v_cvt_pk_bf16_f32 v18, v20, v21\n"
"v_mfma_f32_16x16x32_bf16 v[252:255], a[132:135], a[116:119], v[252:255]\n"
"v_exp_f32 v195, v195\n"
"v_mfma_f32_16x16x32_bf16 v[252:255], a[128:131], a[112:115], v[252:255]\n"
"ds_read_b128 a[216:219], v226 offset:49152\n"
"ds_read_b128 a[224:227], v226 offset:51200\n"
"v_cvt_pk_bf16_f32 v133, v194, v195\n"
"v_mfma_f32_16x16x32_bf16 v[64:67], a[160:163], v[160:163], v[64:67]\n"
"v_exp_f32 v196, v196\n"
"v_mfma_f32_16x16x32_bf16 v[64:67], a[164:167], v[164:167], v[64:67]\n"
"s_waitcnt lgkmcnt(7)\n"
"ds_read_b64_tr_b16 a[180:181], v217 offset:58112\n"
"ds_read_b128 a[220:223], v226 offset:50176\n"
"v_mfma_f32_16x16x32_bf16 v[68:71], a[172:175], v[164:167], v[68:71]\n"
"v_exp_f32 v197, v197\n"
"v_mfma_f32_16x16x32_bf16 v[68:71], a[168:171], v[160:163], v[68:71]\n"
"ds_read_b64_tr_b16 a[182:183], v217 offset:62208\n"
"ds_read_b128 a[228:231], v226 offset:52224\n"
"v_cvt_pk_bf16_f32 v140, v196, v197\n"
"v_cvt_pk_bf16_f32 v19, v22, v23\n"
"v_mfma_f32_16x16x32_bf16 v[76:79], a[168:171], v[168:171], v[76:79]\n"
"v_exp_f32 v198, v198\n"
"v_mfma_f32_16x16x32_bf16 v[76:79], a[172:175], v[172:175], v[76:79]\n"
"ds_read_b64_tr_b16 a[184:185], v217 offset:49928\n"
"ds_write_b128 v224, v[16:19] offset:49152\n"
"v_pk_mul_f32 v[48:49], v[48:49], v[212:213] op_sel_hi:[1,0]\n"
"v_pk_mul_f32 v[50:51], v[50:51], v[212:213] op_sel_hi:[1,0]\n"
"v_pk_mul_f32 v[52:53], v[52:53], v[212:213] op_sel_hi:[1,0]\n"
"v_pk_mul_f32 v[54:55], v[54:55], v[212:213] op_sel_hi:[1,0]\n"
"v_cvt_pk_bf16_f32 v48, v48, v49\n"
"v_cvt_pk_bf16_f32 v49, v50, v51\n"
"v_mfma_f32_16x16x32_bf16 v[72:75], a[164:167], v[172:175], v[72:75]\n"
"v_exp_f32 v199, v199\n"
"v_mfma_f32_16x16x32_bf16 v[72:75], a[160:163], v[168:171], v[72:75]\n"
"v_cvt_pk_bf16_f32 v141, v198, v199\n"
"v_cvt_pk_bf16_f32 v50, v52, v53\n"
"v_mfma_f32_16x16x32_bf16 v[80:83], a[160:163], v[176:179], v[80:83]\n"
"v_exp_f32 v248, v248\n"
"v_mfma_f32_16x16x32_bf16 v[80:83], a[164:167], v[180:183], v[80:83]\n"
"ds_read_b64_tr_b16 a[186:187], v217 offset:54024\n"
"v_cvt_pk_bf16_f32 v51, v54, v55\n"
"v_mfma_f32_16x16x32_bf16 v[84:87], a[172:175], v[180:183], v[84:87]\n"
"v_exp_f32 v249, v249\n"
"v_mfma_f32_16x16x32_bf16 v[84:87], a[168:171], v[176:179], v[84:87]\n"
"ds_read_b64_tr_b16 a[188:189], v217 offset:58120\n"
"v_cvt_pk_bf16_f32 v148, v248, v249\n"
"v_mfma_f32_16x16x32_bf16 v[92:95], a[168:171], v[184:187], v[92:95]\n"
"v_exp_f32 v250, v250\n"
"v_mfma_f32_16x16x32_bf16 v[92:95], a[172:175], v[188:191], v[92:95]\n"
"ds_read_b64_tr_b16 a[190:191], v217 offset:62216\n"
"ds_write_b128 v225, v[48:51] offset:49152\n"
"v_pk_mul_f32 v[24:25], v[24:25], v[214:215] op_sel_hi:[1,0]\n"
"v_pk_mul_f32 v[26:27], v[26:27], v[214:215] op_sel_hi:[1,0]\n"
"v_pk_mul_f32 v[28:29], v[28:29], v[214:215] op_sel_hi:[1,0]\n"
"v_pk_mul_f32 v[30:31], v[30:31], v[214:215] op_sel_hi:[1,0]\n"
"v_cvt_pk_bf16_f32 v24, v24, v25\n"
"v_cvt_pk_bf16_f32 v25, v26, v27\n"
"v_mfma_f32_16x16x32_bf16 v[88:91], a[164:167], v[188:191], v[88:91]\n"
"v_exp_f32 v251, v251\n"
"v_mfma_f32_16x16x32_bf16 v[88:91], a[160:163], v[184:187], v[88:91]\n"
"s_waitcnt vmcnt(11) lgkmcnt(6)\n"
"buffer_store_dwordx4 a[216:219], v223, s[56:59], s99 offen offset:0 sc1 nt\n"
"v_cvt_pk_bf16_f32 v149, v250, v251\n"
"v_cvt_pk_bf16_f32 v26, v28, v29\n"
"v_cvt_pk_bf16_f32 v27, v30, v31\n"
"v_mfma_f32_16x16x32_bf16 v[192:195], a[144:147], a[64:67], -4.0\n"
"s_barrier\n"
"v_exp_f32 v252, v252\n"
"v_mfma_f32_16x16x32_bf16 v[192:195], a[148:151], a[68:71], v[192:195]\n"
"s_bfe_u32 s50, s38, 0x30008\n"
"ds_read_b128 a[128:131], v216 offset:16384\n"
"v_accvgpr_write_b32 a205, 0\n"
"v_mfma_f32_16x16x32_bf16 v[192:195], a[152:155], a[72:75], v[192:195]\n"
"s_lshl_b32 s50, s50, 14\n"
"v_exp_f32 v253, v253\n"
"v_mfma_f32_16x16x32_bf16 v[192:195], a[156:159], a[76:79], v[192:195]\n"
"s_add_u32 s39, s54, s50\n"
"ds_read_b128 a[132:135], v216 offset:16640\n"
"ds_read_b128 a[232:235], v226 offset:49152\n"
"v_cvt_pk_bf16_f32 v156, v252, v253\n"
"v_mfma_f32_16x16x32_bf16 v[196:199], a[156:159], a[92:95], -4.0\n"
"s_add_u32 s50, s44, s39\n"
"v_exp_f32 v254, v254\n"
"v_mfma_f32_16x16x32_bf16 v[196:199], a[152:155], a[88:91], v[196:199]\n"
"s_addc_u32 s51, s45, 0\n"
"ds_read_b128 a[136:139], v216 offset:16896\n"
"v_mul_f32 v56, v56, v214\n"
"v_mfma_f32_16x16x32_bf16 v[196:199], a[148:151], a[84:87], v[196:199]\n"
"s_add_u32 m0, s65, 32768\n"
"v_exp_f32 v255, v255\n"
"v_mfma_f32_16x16x32_bf16 v[196:199], a[144:147], a[80:83], v[196:199]\n"
"global_load_lds_dwordx4 v219, s[50:51] offset:0\n"
"v_cvt_pk_bf16_f32 v157, v254, v255\n"
"v_mfma_f32_16x16x32_bf16 v[248:251], a[144:147], a[96:99], -4.0\n"
"v_exp_f32 v192, v192\n"
"v_mfma_f32_16x16x32_bf16 v[248:251], a[148:151], a[100:103], v[248:251]\n"
"ds_read_b128 a[140:143], v216 offset:17152\n"
"v_mul_f32 v57, v57, v214\n"
"v_mfma_f32_16x16x32_bf16 v[248:251], a[152:155], a[104:107], v[248:251]\n"
"v_exp_f32 v193, v193\n"
"v_mfma_f32_16x16x32_bf16 v[248:251], a[156:159], a[108:111], v[248:251]\n"
"ds_read_b64_tr_b16 a[160:161], v217 offset:0\n"
"ds_write_b128 v224, v[24:27] offset:2048\n"
"v_cvt_pk_bf16_f32 v134, v192, v193\n"
"v_pk_mul_f32 v[58:59], v[58:59], v[214:215] op_sel_hi:[1,0]\n"
"v_pk_mul_f32 v[60:61], v[60:61], v[214:215] op_sel_hi:[1,0]\n"
"v_pk_mul_f32 v[62:63], v[62:63], v[214:215] op_sel_hi:[1,0]\n"
"v_cvt_pk_bf16_f32 v56, v56, v57\n"
"v_cvt_pk_bf16_f32 v57, v58, v59\n"
"v_mfma_f32_16x16x32_bf16 v[252:255], a[156:159], a[124:127], -4.0\n"
"v_exp_f32 v194, v194\n"
"v_mfma_f32_16x16x32_bf16 v[252:255], a[152:155], a[120:123], v[252:255]\n"
"ds_read_b64_tr_b16 a[162:163], v217 offset:4096\n"
"v_cvt_pk_bf16_f32 v58, v60, v61\n"
"v_mfma_f32_16x16x32_bf16 v[252:255], a[148:151], a[116:119], v[252:255]\n"
"v_exp_f32 v195, v195\n"
"v_mfma_f32_16x16x32_bf16 v[252:255], a[144:147], a[112:115], v[252:255]\n"
"ds_read_b128 a[236:239], v226 offset:50176\n"
"v_cvt_pk_bf16_f32 v135, v194, v195\n"
"v_mfma_f32_16x16x32_bf16 v[96:99], a[176:179], v[160:163], v[96:99]\n"
"v_exp_f32 v196, v196\n"
"v_mfma_f32_16x16x32_bf16 v[96:99], a[180:183], v[164:167], v[96:99]\n"
"s_waitcnt lgkmcnt(6)\n"
"ds_read_b64_tr_b16 a[164:165], v217 offset:8192\n"
"v_cvt_pk_bf16_f32 v59, v62, v63\n"
"v_mfma_f32_16x16x32_bf16 v[100:103], a[188:191], v[164:167], v[100:103]\n"
"v_exp_f32 v197, v197\n"
"v_mfma_f32_16x16x32_bf16 v[100:103], a[184:187], v[160:163], v[100:103]\n"
"ds_read_b64_tr_b16 a[166:167], v217 offset:12288\n"
"v_cvt_pk_bf16_f32 v142, v196, v197\n"
"v_mfma_f32_16x16x32_bf16 v[108:111], a[184:187], v[168:171], v[108:111]\n"
"v_exp_f32 v198, v198\n"
"v_mfma_f32_16x16x32_bf16 v[108:111], a[188:191], v[172:175], v[108:111]\n"
"ds_read_b64_tr_b16 a[168:169], v217 offset:8\n"
"v_mul_f32 v64, v64, v208\n"
"v_mfma_f32_16x16x32_bf16 v[104:107], a[180:183], v[172:175], v[104:107]\n"
"s_add_u32 m0, s65, 32768\n"
"v_exp_f32 v199, v199\n"
"v_mfma_f32_16x16x32_bf16 v[104:107], a[176:179], v[168:171], v[104:107]\n"
"global_load_lds_dwordx4 v219, s[50:51] offset:1024\n"
"v_cvt_pk_bf16_f32 v143, v198, v199\n"
"v_mfma_f32_16x16x32_bf16 v[112:115], a[176:179], v[176:179], v[112:115]\n"
"v_exp_f32 v248, v248\n"
"v_mfma_f32_16x16x32_bf16 v[112:115], a[180:183], v[180:183], v[112:115]\n"
"ds_read_b64_tr_b16 a[170:171], v217 offset:4104\n"
"v_mul_f32 v65, v65, v208\n"
"v_mfma_f32_16x16x32_bf16 v[116:119], a[188:191], v[180:183], v[116:119]\n"
"v_exp_f32 v249, v249\n"
"v_mfma_f32_16x16x32_bf16 v[116:119], a[184:187], v[176:179], v[116:119]\n"
"ds_read_b64_tr_b16 a[172:173], v217 offset:8200\n"
"v_cvt_pk_bf16_f32 v150, v248, v249\n"
"v_mfma_f32_16x16x32_bf16 v[124:127], a[184:187], v[184:187], v[124:127]\n"
"v_exp_f32 v250, v250\n"
"v_mfma_f32_16x16x32_bf16 v[124:127], a[188:191], v[188:191], v[124:127]\n"
"ds_read_b64_tr_b16 a[174:175], v217 offset:12296\n"
"ds_write_b128 v225, v[56:59] offset:2048\n"
"v_pk_mul_f32 v[66:67], v[66:67], v[208:209] op_sel_hi:[1,0]\n"
"v_pk_mul_f32 v[68:69], v[68:69], v[208:209] op_sel_hi:[1,0]\n"
"v_pk_mul_f32 v[70:71], v[70:71], v[208:209] op_sel_hi:[1,0]\n"
"v_cvt_pk_bf16_f32 v64, v64, v65\n"
"v_cvt_pk_bf16_f32 v65, v66, v67\n"
"v_mfma_f32_16x16x32_bf16 v[120:123], a[180:183], v[188:191], v[120:123]\n"
"s_add_u32 m0, s65, 32768\n"
"v_exp_f32 v251, v251\n"
"v_mfma_f32_16x16x32_bf16 v[120:123], a[176:179], v[184:187], v[120:123]\n"
"global_load_lds_dwordx4 v219, s[50:51] offset:2048\n"
"v_cvt_pk_bf16_f32 v151, v250, v251\n"
"v_mfma_f32_16x16x32_bf16 v[192:195], a[128:131], a[64:67], -4.0\n"
"v_exp_f32 v252, v252\n"
"v_mfma_f32_16x16x32_bf16 v[192:195], a[132:135], a[68:71], v[192:195]\n"
"s_waitcnt lgkmcnt(5)\n"
"ds_read_b128 a[144:147], v216 offset:20480\n"
"v_cvt_pk_bf16_f32 v66, v68, v69\n"
"v_mfma_f32_16x16x32_bf16 v[192:195], a[136:139], a[72:75], v[192:195]\n"
"v_exp_f32 v253, v253\n"
"v_mfma_f32_16x16x32_bf16 v[192:195], a[140:143], a[76:79], v[192:195]\n"
"ds_read_b128 a[148:151], v216 offset:20736\n"
"v_cvt_pk_bf16_f32 v158, v252, v253\n"
"v_mfma_f32_16x16x32_bf16 v[196:199], a[140:143], a[92:95], -4.0\n"
"v_exp_f32 v254, v254\n"
"v_mfma_f32_16x16x32_bf16 v[196:199], a[136:139], a[88:91], v[196:199]\n"
"ds_read_b128 a[152:155], v216 offset:20992\n"
"v_cvt_pk_bf16_f32 v67, v70, v71\n"
"v_mfma_f32_16x16x32_bf16 v[196:199], a[132:135], a[84:87], v[196:199]\n"
"v_exp_f32 v255, v255\n"
"v_mfma_f32_16x16x32_bf16 v[196:199], a[128:131], a[80:83], v[196:199]\n"
"ds_read_b128 a[240:243], v226 offset:2048\n"
"v_cvt_pk_bf16_f32 v159, v254, v255\n"
"v_mfma_f32_16x16x32_bf16 v[248:251], a[128:131], a[96:99], -4.0\n"
"v_exp_f32 v192, v192\n"
"v_mfma_f32_16x16x32_bf16 v[248:251], a[132:135], a[100:103], v[248:251]\n"
"ds_read_b128 a[156:159], v216 offset:21248\n"
"v_mul_f32 v96, v96, v208\n"
"v_mfma_f32_16x16x32_bf16 v[248:251], a[136:139], a[104:107], v[248:251]\n"
"v_exp_f32 v193, v193\n"
"v_mfma_f32_16x16x32_bf16 v[248:251], a[140:143], a[108:111], v[248:251]\n"
"ds_read_b64_tr_b16 a[176:177], v217 offset:256\n"
"v_cvt_pk_bf16_f32 v160, v192, v193\n"
"v_mfma_f32_16x16x32_bf16 v[252:255], a[140:143], a[124:127], -4.0\n"
"v_exp_f32 v194, v194\n"
"v_mfma_f32_16x16x32_bf16 v[252:255], a[136:139], a[120:123], v[252:255]\n"
"ds_read_b64_tr_b16 a[178:179], v217 offset:4352\n"
"v_mul_f32 v97, v97, v208\n"
"v_mfma_f32_16x16x32_bf16 v[252:255], a[132:135], a[116:119], v[252:255]\n"
"s_add_u32 m0, s65, 32768\n"
"v_exp_f32 v195, v195\n"
"v_mfma_f32_16x16x32_bf16 v[252:255], a[128:131], a[112:115], v[252:255]\n"
"global_load_lds_dwordx4 v219, s[50:51] offset:3072\n"
"v_cvt_pk_bf16_f32 v161, v194, v195\n"
"v_smfmac_f32_16x16x64_bf16 a[192:195], a[208:211], v[128:135], v215\n"
"ds_read_b128 a[244:247], v226 offset:3072\n"
"buffer_store_dwordx4 a[220:223], v223, s[56:59], s99 offen offset:2048 sc1 nt\n"
"v_mul_f32 v98, v98, v208\n"
"v_smfmac_f32_16x16x64_bf16 a[196:199], a[208:211], v[136:143], v215\n"
"ds_read_b64_tr_b16 a[180:181], v217 offset:8448\n"
"v_smfmac_f32_16x16x64_bf16 a[200:203], a[208:211], v[144:151], v215\n"
"v_mul_f32 v99, v99, v208\n"
"v_smfmac_f32_16x16x64_bf16 a[204:207], a[208:211], v[152:159], v215\n"
"ds_read_b64_tr_b16 a[182:183], v217 offset:12544\n"
"v_mfma_f32_16x16x32_bf16 v[0:3], a[160:163], v[128:131], 0\n"
"v_exp_f32 v196, v196\n"
"v_mfma_f32_16x16x32_bf16 v[0:3], a[164:167], v[132:135], v[0:3]\n"
"s_waitcnt lgkmcnt(7)\n"
"ds_read_b64_tr_b16 a[184:185], v217 offset:264\n"
"ds_write_b128 v224, v[64:67] offset:0\n"
"v_pk_mul_f32 v[100:101], v[100:101], v[208:209] op_sel_hi:[1,0]\n"
"v_pk_mul_f32 v[102:103], v[102:103], v[208:209] op_sel_hi:[1,0]\n"
"v_cvt_pk_bf16_f32 v96, v96, v97\n"
"v_cvt_pk_bf16_f32 v97, v98, v99\n"
"v_mfma_f32_16x16x32_bf16 v[4:7], a[172:175], v[132:135], 0\n"
"s_add_u32 m0, s66, 4096\n"
"v_exp_f32 v197, v197\n"
"v_mfma_f32_16x16x32_bf16 v[4:7], a[168:171], v[128:131], v[4:7]\n"
"global_load_lds_dwordx4 v222, s[52:53] offset:2048\n"
"v_cvt_pk_bf16_f32 v168, v196, v197\n"
"v_mfma_f32_16x16x32_bf16 v[12:15], a[168:171], v[136:139], 0\n"
"v_exp_f32 v198, v198\n"
"v_mfma_f32_16x16x32_bf16 v[12:15], a[172:175], v[140:143], v[12:15]\n"
"s_bfe_u32 s48, s38, 0x3000c\n"
"ds_read_b64_tr_b16 a[186:187], v217 offset:4360\n"
"v_cvt_pk_bf16_f32 v98, v100, v101\n"
"v_mfma_f32_16x16x32_bf16 v[8:11], a[164:167], v[140:143], 0\n"
"s_lshl_b32 s48, s48, 14\n"
"v_exp_f32 v199, v199\n"
"v_mfma_f32_16x16x32_bf16 v[8:11], a[160:163], v[136:139], v[8:11]\n"
"s_add_u32 s36, s79, s48\n"
"ds_read_b64_tr_b16 a[188:189], v217 offset:8456\n"
"v_cvt_pk_bf16_f32 v169, v198, v199\n"
"v_mfma_f32_16x16x32_bf16 v[16:19], a[160:163], v[144:147], 0\n"
"s_add_u32 s48, s42, s36\n"
"v_exp_f32 v248, v248\n"
"v_mfma_f32_16x16x32_bf16 v[16:19], a[164:167], v[148:151], v[16:19]\n"
"s_addc_u32 s49, s43, 0\n"
"ds_read_b64_tr_b16 a[190:191], v217 offset:12552\n"
"v_cvt_pk_bf16_f32 v99, v102, v103\n"
"v_mfma_f32_16x16x32_bf16 v[20:23], a[172:175], v[148:151], 0\n"
"s_add_u32 m0, s64, 49152\n"
"v_exp_f32 v249, v249\n"
"v_mfma_f32_16x16x32_bf16 v[20:23], a[168:171], v[144:147], v[20:23]\n"
"global_load_lds_dwordx4 v219, s[48:49] offset:0\n"
"v_cvt_pk_bf16_f32 v176, v248, v249\n"
"v_mfma_f32_16x16x32_bf16 v[28:31], a[168:171], v[152:155], 0\n"
"v_exp_f32 v250, v250\n"
"v_mfma_f32_16x16x32_bf16 v[28:31], a[172:175], v[156:159], v[28:31]\n"
"ds_write_b128 v225, v[96:99] offset:0\n"
"v_pk_mul_f32 v[72:73], v[72:73], v[210:211] op_sel_hi:[1,0]\n"
"v_pk_mul_f32 v[74:75], v[74:75], v[210:211] op_sel_hi:[1,0]\n"
"v_pk_mul_f32 v[76:77], v[76:77], v[210:211] op_sel_hi:[1,0]\n"
"v_pk_mul_f32 v[78:79], v[78:79], v[210:211] op_sel_hi:[1,0]\n"
"v_cvt_pk_bf16_f32 v72, v72, v73\n"
"v_cvt_pk_bf16_f32 v73, v74, v75\n"
"v_mfma_f32_16x16x32_bf16 v[24:27], a[164:167], v[156:159], 0\n"
"v_exp_f32 v251, v251\n"
"v_mfma_f32_16x16x32_bf16 v[24:27], a[160:163], v[152:155], v[24:27]\n"
"v_cvt_pk_bf16_f32 v177, v250, v251\n"
"v_cvt_pk_bf16_f32 v74, v76, v77\n"
"v_mfma_f32_16x16x32_bf16 v[192:195], a[144:147], a[64:67], -4.0\n"
"v_exp_f32 v252, v252\n"
"v_mfma_f32_16x16x32_bf16 v[192:195], a[148:151], a[68:71], v[192:195]\n"
"ds_read_b128 a[128:131], v216 offset:24576\n"
"v_cvt_pk_bf16_f32 v75, v78, v79\n"
"v_mfma_f32_16x16x32_bf16 v[192:195], a[152:155], a[72:75], v[192:195]\n"
"s_waitcnt lgkmcnt(5)\n"
"v_exp_f32 v253, v253\n"
"v_mfma_f32_16x16x32_bf16 v[192:195], a[156:159], a[76:79], v[192:195]\n"
"ds_read_b128 a[132:135], v216 offset:24832\n"
"ds_read_b128 a[248:251], v226 offset:0\n"
"v_cvt_pk_bf16_f32 v184, v252, v253\n"
"v_mfma_f32_16x16x32_bf16 v[196:199], a[156:159], a[92:95], -4.0\n"
"v_exp_f32 v254, v254\n"
"v_mfma_f32_16x16x32_bf16 v[196:199], a[152:155], a[88:91], v[196:199]\n"
"ds_read_b128 a[136:139], v216 offset:25088\n"
"v_mul_f32 v104, v104, v210\n"
"v_mfma_f32_16x16x32_bf16 v[196:199], a[148:151], a[84:87], v[196:199]\n"
"s_add_u32 m0, s64, 49152\n"
"v_exp_f32 v255, v255\n"
"v_mfma_f32_16x16x32_bf16 v[196:199], a[144:147], a[80:83], v[196:199]\n"
"global_load_lds_dwordx4 v219, s[48:49] offset:1024\n"
"v_cvt_pk_bf16_f32 v185, v254, v255\n"
"v_mfma_f32_16x16x32_bf16 v[248:251], a[144:147], a[96:99], -4.0\n"
"v_exp_f32 v192, v192\n"
"v_mfma_f32_16x16x32_bf16 v[248:251], a[148:151], a[100:103], v[248:251]\n"
"ds_read_b128 a[140:143], v216 offset:25344\n"
"v_mul_f32 v105, v105, v210\n"
"v_mfma_f32_16x16x32_bf16 v[248:251], a[152:155], a[104:107], v[248:251]\n"
"v_exp_f32 v193, v193\n"
"v_mfma_f32_16x16x32_bf16 v[248:251], a[156:159], a[108:111], v[248:251]\n"
"ds_read_b64_tr_b16 a[160:161], v217 offset:512\n"
"ds_write_b128 v224, v[72:75] offset:2048\n"
"v_cvt_pk_bf16_f32 v162, v192, v193\n"
"v_pk_mul_f32 v[106:107], v[106:107], v[210:211] op_sel_hi:[1,0]\n"
"v_pk_mul_f32 v[108:109], v[108:109], v[210:211] op_sel_hi:[1,0]\n"
"v_pk_mul_f32 v[110:111], v[110:111], v[210:211] op_sel_hi:[1,0]\n"
"v_cvt_pk_bf16_f32 v104, v104, v105\n"
"v_cvt_pk_bf16_f32 v105, v106, v107\n"
"v_mfma_f32_16x16x32_bf16 v[252:255], a[156:159], a[124:127], -4.0\n"
".Lattn_pad11:\n"
"s_cbranch_vccnz .Lattn_ret\n"
"v_exp_f32 v194, v194\n"
"v_mfma_f32_16x16x32_bf16 v[252:255], a[152:155], a[120:123], v[252:255]\n"
"ds_read_b64_tr_b16 a[162:163], v217 offset:4608\n"
"v_cvt_pk_bf16_f32 v106, v108, v109\n"
"v_mfma_f32_16x16x32_bf16 v[252:255], a[148:151], a[116:119], v[252:255]\n"
"s_add_u32 m0, s64, 49152\n"
"v_exp_f32 v195, v195\n"
"v_mfma_f32_16x16x32_bf16 v[252:255], a[144:147], a[112:115], v[252:255]\n"
"global_load_lds_dwordx4 v220, s[48:49] offset:2048\n"
"v_cvt_pk_bf16_f32 v163, v194, v195\n"
"v_mfma_f32_16x16x32_bf16 v[32:35], a[176:179], v[128:131], 0\n"
"v_exp_f32 v196, v196\n"
"v_mfma_f32_16x16x32_bf16 v[32:35], a[180:183], v[132:135], v[32:35]\n"
"s_waitcnt lgkmcnt(5)\n"
"ds_read_b64_tr_b16 a[164:165], v217 offset:8704\n"
"ds_read_b128 a[252:255], v226 offset:1024\n"
"v_mfma_f32_16x16x32_bf16 v[36:39], a[188:191], v[132:135], 0\n"
"v_exp_f32 v197, v197\n"
"v_mfma_f32_16x16x32_bf16 v[36:39], a[184:187], v[128:131], v[36:39]\n"
"ds_read_b64_tr_b16 a[166:167], v217 offset:12800\n"
"buffer_store_dwordx4 a[224:227], v223, s[56:59], s70 offen offset:0 sc1 nt\n"
"v_cvt_pk_bf16_f32 v170, v196, v197\n"
"v_cvt_pk_bf16_f32 v107, v110, v111\n"
"v_mfma_f32_16x16x32_bf16 v[44:47], a[184:187], v[136:139], 0\n"
"v_exp_f32 v198, v198\n"
"v_mfma_f32_16x16x32_bf16 v[44:47], a[188:191], v[140:143], v[44:47]\n"
"ds_read_b64_tr_b16 a[168:169], v217 offset:520\n"
"v_mul_f32 v80, v80, v212\n"
"v_mfma_f32_16x16x32_bf16 v[40:43], a[180:183], v[140:143], 0\n"
"v_exp_f32 v199, v199\n"
"v_mfma_f32_16x16x32_bf16 v[40:43], a[176:179], v[136:139], v[40:43]\n"
"ds_read_b128 v[200:203], v218 offset:0\n"
"v_cvt_pk_bf16_f32 v171, v198, v199\n"
"v_mfma_f32_16x16x32_bf16 v[48:51], a[176:179], v[144:147], 0\n"
"v_exp_f32 v248, v248\n"
"v_mfma_f32_16x16x32_bf16 v[48:51], a[180:183], v[148:151], v[48:51]\n"
"ds_read_b64_tr_b16 a[170:171], v217 offset:4616\n"
"v_mul_f32 v81, v81, v212\n"
"v_mfma_f32_16x16x32_bf16 v[52:55], a[188:191], v[148:151], 0\n"
"v_exp_f32 v249, v249\n"
"v_mfma_f32_16x16x32_bf16 v[52:55], a[184:187], v[144:147], v[52:55]\n"
"ds_read_b64_tr_b16 a[172:173], v217 offset:8712\n"
"v_cvt_pk_bf16_f32 v178, v248, v249\n"
"v_mfma_f32_16x16x32_bf16 v[60:63], a[184:187], v[152:155], 0\n"
"v_exp_f32 v250, v250\n"
"v_mfma_f32_16x16x32_bf16 v[60:63], a[188:191], v[156:159], v[60:63]\n"
"ds_read_b64_tr_b16 a[174:175], v217 offset:12808\n"
"ds_write_b128 v225, v[104:107] offset:2048\n"
"v_pk_mul_f32 v[82:83], v[82:83], v[212:213] op_sel_hi:[1,0]\n"
"v_pk_mul_f32 v[84:85], v[84:85], v[212:213] op_sel_hi:[1,0]\n"
"v_pk_mul_f32 v[86:87], v[86:87], v[212:213] op_sel_hi:[1,0]\n"
"v_cvt_pk_bf16_f32 v80, v80, v81\n"
"v_cvt_pk_bf16_f32 v81, v82, v83\n"
"v_mfma_f32_16x16x32_bf16 v[56:59], a[180:183], v[156:159], 0\n"
"s_add_u32 m0, s64, 49152\n"
"v_exp_f32 v251, v251\n"
"v_mfma_f32_16x16x32_bf16 v[56:59], a[176:179], v[152:155], v[56:59]\n"
"global_load_lds_dwordx4 v220, s[48:49] offset:3072\n"
"v_cvt_pk_bf16_f32 v179, v250, v251\n"
"v_mfma_f32_16x16x32_bf16 v[192:195], a[128:131], a[64:67], -4.0\n"
"v_exp_f32 v252, v252\n"
"v_mfma_f32_16x16x32_bf16 v[192:195], a[132:135], a[68:71], v[192:195]\n"
"s_waitcnt lgkmcnt(6)\n"
"ds_read_b128 a[144:147], v216 offset:28672\n"
"v_cvt_pk_bf16_f32 v82, v84, v85\n"
"v_mfma_f32_16x16x32_bf16 v[192:195], a[136:139], a[72:75], v[192:195]\n"
"v_exp_f32 v253, v253\n"
"v_mfma_f32_16x16x32_bf16 v[192:195], a[140:143], a[76:79], v[192:195]\n"
"ds_read_b128 a[148:151], v216 offset:28928\n"
"ds_read_b128 v[236:239], v226 offset:2048\n"
"v_cvt_pk_bf16_f32 v186, v252, v253\n"
"v_mfma_f32_16x16x32_bf16 v[196:199], a[140:143], a[92:95], -4.0\n"
"v_exp_f32 v254, v254\n"
"v_mfma_f32_16x16x32_bf16 v[196:199], a[136:139], a[88:91], v[196:199]\n"
"ds_read_b128 a[152:155], v216 offset:29184\n"
"v_cvt_pk_bf16_f32 v83, v86, v87\n"
"v_mfma_f32_16x16x32_bf16 v[196:199], a[132:135], a[84:87], v[196:199]\n"
"v_exp_f32 v255, v255\n"
"v_mfma_f32_16x16x32_bf16 v[196:199], a[128:131], a[80:83], v[196:199]\n"
"ds_read_b128 v[204:207], v218 offset:256\n"
"ds_read_b128 v[240:243], v226 offset:3072\n"
"v_cvt_pk_bf16_f32 v187, v254, v255\n"
"v_mfma_f32_16x16x32_bf16 v[248:251], a[128:131], a[96:99], -4.0\n"
"v_exp_f32 v192, v192\n"
"v_mfma_f32_16x16x32_bf16 v[248:251], a[132:135], a[100:103], v[248:251]\n"
"s_waitcnt lgkmcnt(8)\n"
"ds_read_b128 a[156:159], v216 offset:29440\n"
"v_lshlrev_b32 v228, 16, v200\n"
"v_mfma_f32_16x16x32_bf16 v[248:251], a[136:139], a[104:107], v[248:251]\n"
"v_exp_f32 v193, v193\n"
"v_mfma_f32_16x16x32_bf16 v[248:251], a[140:143], a[108:111], v[248:251]\n"
"ds_read_b64_tr_b16 a[176:177], v217 offset:768\n"
"ds_write_b128 v224, v[80:83] offset:0\n"
"v_cvt_pk_bf16_f32 v164, v192, v193\n"
"v_pk_mul_f32 v[88:89], v[88:89], v[214:215] op_sel_hi:[1,0]\n"
"v_pk_mul_f32 v[90:91], v[90:91], v[214:215] op_sel_hi:[1,0]\n"
"v_pk_mul_f32 v[92:93], v[92:93], v[214:215] op_sel_hi:[1,0]\n"
"v_pk_mul_f32 v[94:95], v[94:95], v[214:215] op_sel_hi:[1,0]\n"
"v_cvt_pk_bf16_f32 v88, v88, v89\n"
"v_cvt_pk_bf16_f32 v89, v90, v91\n"
"v_mfma_f32_16x16x32_bf16 v[252:255], a[140:143], a[124:127], -4.0\n"
"v_exp_f32 v194, v194\n"
"v_mfma_f32_16x16x32_bf16 v[252:255], a[136:139], a[120:123], v[252:255]\n"
"ds_read_b64_tr_b16 a[178:179], v217 offset:4864\n"
"v_cvt_pk_bf16_f32 v90, v92, v93\n"
"v_mfma_f32_16x16x32_bf16 v[252:255], a[132:135], a[116:119], v[252:255]\n"
"v_exp_f32 v195, v195\n"
"v_mfma_f32_16x16x32_bf16 v[252:255], a[128:131], a[112:115], v[252:255]\n"
"ds_read_b128 v[244:247], v218 offset:512\n"
"v_cvt_pk_bf16_f32 v165, v194, v195\n"
"v_mfma_f32_16x16x32_bf16 v[64:67], a[160:163], v[128:131], 0\n"
"v_exp_f32 v196, v196\n"
"v_mfma_f32_16x16x32_bf16 v[64:67], a[164:167], v[132:135], v[64:67]\n"
"s_waitcnt lgkmcnt(8)\n"
"ds_read_b64_tr_b16 a[180:181], v217 offset:8960\n"
"v_cvt_pk_bf16_f32 v91, v94, v95\n"
"v_mfma_f32_16x16x32_bf16 v[68:71], a[172:175], v[132:135], 0\n"
"v_exp_f32 v197, v197\n"
"v_mfma_f32_16x16x32_bf16 v[68:71], a[168:171], v[128:131], v[68:71]\n"
"ds_read_b64_tr_b16 a[182:183], v217 offset:13056\n"
"v_cvt_pk_bf16_f32 v172, v196, v197\n"
"v_mfma_f32_16x16x32_bf16 v[76:79], a[168:171], v[136:139], 0\n"
"v_exp_f32 v198, v198\n"
"v_mfma_f32_16x16x32_bf16 v[76:79], a[172:175], v[140:143], v[76:79]\n"
"ds_read_b64_tr_b16 a[184:185], v217 offset:776\n"
"ds_write_b128 v224, v[88:91] offset:2048\n"
"v_pk_mul_f32 v[112:113], v[112:113], v[212:213] op_sel_hi:[1,0]\n"
"v_pk_mul_f32 v[114:115], v[114:115], v[212:213] op_sel_hi:[1,0]\n"
"v_pk_mul_f32 v[116:117], v[116:117], v[212:213] op_sel_hi:[1,0]\n"
"v_pk_mul_f32 v[118:119], v[118:119], v[212:213] op_sel_hi:[1,0]\n"
"v_cvt_pk_bf16_f32 v112, v112, v113\n"
"v_cvt_pk_bf16_f32 v113, v114, v115\n"
"v_mfma_f32_16x16x32_bf16 v[72:75], a[164:167], v[140:143], 0\n"
"s_add_u32 m0, s66, 4096\n"
"v_exp_f32 v199, v199\n"
"v_mfma_f32_16x16x32_bf16 v[72:75], a[160:163], v[136:139], v[72:75]\n"
"global_load_lds_dwordx4 v222, s[52:53] offset:3072\n"
"v_cvt_pk_bf16_f32 v173, v198, v199\n"
"v_mfma_f32_16x16x32_bf16 v[80:83], a[160:163], v[144:147], 0\n"
"v_exp_f32 v248, v248\n"
"v_mfma_f32_16x16x32_bf16 v[80:83], a[164:167], v[148:151], v[80:83]\n"
"ds_read_b64_tr_b16 a[186:187], v217 offset:4872\n"
"buffer_store_dwordx4 a[228:231], v223, s[56:59], s70 offen offset:2048 sc1 nt\n"
"v_cvt_pk_bf16_f32 v114, v116, v117\n"
"v_cvt_pk_bf16_f32 v115, v118, v119\n"
"v_mfma_f32_16x16x32_bf16 v[84:87], a[172:175], v[148:151], 0\n"
"v_exp_f32 v249, v249\n"
"v_mfma_f32_16x16x32_bf16 v[84:87], a[168:171], v[144:147], v[84:87]\n"
"ds_read_b64_tr_b16 a[188:189], v217 offset:8968\n"
"v_cvt_pk_bf16_f32 v180, v248, v249\n"
"v_mfma_f32_16x16x32_bf16 v[92:95], a[168:171], v[152:155], 0\n"
"v_exp_f32 v250, v250\n"
"v_mfma_f32_16x16x32_bf16 v[92:95], a[172:175], v[156:159], v[92:95]\n"
"ds_read_b64_tr_b16 a[190:191], v217 offset:13064\n"
"v_and_b32 v229, s67, v200\n"
"v_mfma_f32_16x16x32_bf16 v[88:91], a[164:167], v[156:159], 0\n"
"v_exp_f32 v251, v251\n"
"v_mfma_f32_16x16x32_bf16 v[88:91], a[160:163], v[152:155], v[88:91]\n"
"s_waitcnt vmcnt(15) lgkmcnt(6)\n"
"v_cvt_pk_bf16_f32 v181, v250, v251\n"
"v_lshlrev_b32 v230, 16, v201\n"
"v_mfma_f32_16x16x32_bf16 v[192:195], a[144:147], a[64:67], -4.0\n"
"s_barrier\n"
"v_exp_f32 v252, v252\n"
"v_mfma_f32_16x16x32_bf16 v[192:195], a[148:151], a[68:71], v[192:195]\n"
"s_bfe_u32 s50, s38, 0x3000c\n"
"ds_read_b128 a[128:131], v216 offset:32768\n"
"v_and_b32 v231, s67, v201\n"
"v_mfma_f32_16x16x32_bf16 v[192:195], a[152:155], a[72:75], v[192:195]\n"
"s_lshl_b32 s50, s50, 14\n"
"v_exp_f32 v253, v253\n"
"v_mfma_f32_16x16x32_bf16 v[192:195], a[156:159], a[76:79], v[192:195]\n"
"s_add_u32 s39, s54, s50\n"
"ds_read_b128 a[132:135], v216 offset:33024\n"
"v_cvt_pk_bf16_f32 v188, v252, v253\n"
"v_mfma_f32_16x16x32_bf16 v[196:199], a[156:159], a[92:95], -4.0\n"
"s_add_u32 s50, s44, s39\n"
"v_exp_f32 v254, v254\n"
"v_mfma_f32_16x16x32_bf16 v[196:199], a[152:155], a[88:91], v[196:199]\n"
"s_addc_u32 s51, s45, 0\n"
"ds_read_b128 a[136:139], v216 offset:33280\n"
"v_lshlrev_b32 v232, 16, v202\n"
"v_mfma_f32_16x16x32_bf16 v[196:199], a[148:151], a[84:87], v[196:199]\n"
"s_add_u32 m0, s65, 49152\n"
"v_exp_f32 v255, v255\n"
"v_mfma_f32_16x16x32_bf16 v[196:199], a[144:147], a[80:83], v[196:199]\n"
"global_load_lds_dwordx4 v219, s[50:51] offset:0\n"
"v_cvt_pk_bf16_f32 v189, v254, v255\n"
"v_mfma_f32_16x16x32_bf16 v[248:251], a[144:147], a[96:99], -4.0\n"
"v_exp_f32 v192, v192\n"
"v_mfma_f32_16x16x32_bf16 v[248:251], a[148:151], a[100:103], v[248:251]\n"
"ds_read_b128 a[140:143], v216 offset:33536\n"
"v_and_b32 v233, s67, v202\n"
"v_mfma_f32_16x16x32_bf16 v[248:251], a[152:155], a[104:107], v[248:251]\n"
"v_exp_f32 v193, v193\n"
"v_mfma_f32_16x16x32_bf16 v[248:251], a[156:159], a[108:111], v[248:251]\n"
"ds_read_b64_tr_b16 a[160:161], v217 offset:16384\n"
"ds_write_b128 v225, v[112:115] offset:0\n"
"v_cvt_pk_bf16_f32 v166, v192, v193\n"
"v_pk_mul_f32 v[120:121], v[120:121], v[214:215] op_sel_hi:[1,0]\n"
"v_pk_mul_f32 v[122:123], v[122:123], v[214:215] op_sel_hi:[1,0]\n"
"v_pk_mul_f32 v[124:125], v[124:125], v[214:215] op_sel_hi:[1,0]\n"
"v_pk_mul_f32 v[126:127], v[126:127], v[214:215] op_sel_hi:[1,0]\n"
"v_cvt_pk_bf16_f32 v120, v120, v121\n"
"v_cvt_pk_bf16_f32 v121, v122, v123\n"
"v_mfma_f32_16x16x32_bf16 v[252:255], a[156:159], a[124:127], -4.0\n"
"v_exp_f32 v194, v194\n"
"v_mfma_f32_16x16x32_bf16 v[252:255], a[152:155], a[120:123], v[252:255]\n"
"ds_read_b64_tr_b16 a[162:163], v217 offset:20480\n"
"v_cvt_pk_bf16_f32 v122, v124, v125\n"
"v_mfma_f32_16x16x32_bf16 v[252:255], a[148:151], a[116:119], v[252:255]\n"
"v_exp_f32 v195, v195\n"
"v_mfma_f32_16x16x32_bf16 v[252:255], a[144:147], a[112:115], v[252:255]\n"
"ds_read_b128 a[216:219], v226 offset:0\n"
"v_cvt_pk_bf16_f32 v167, v194, v195\n"
"v_mfma_f32_16x16x32_bf16 v[96:99], a[176:179], v[128:131], 0\n"
"s_waitcnt lgkmcnt(7)\n"
"v_exp_f32 v196, v196\n"
"v_mfma_f32_16x16x32_bf16 v[96:99], a[180:183], v[132:135], v[96:99]\n"
"ds_read_b64_tr_b16 a[164:165], v217 offset:24576\n"
"ds_read_b128 a[220:223], v226 offset:1024\n"
"v_mfma_f32_16x16x32_bf16 v[100:103], a[188:191], v[132:135], 0\n"
"v_exp_f32 v197, v197\n"
"v_mfma_f32_16x16x32_bf16 v[100:103], a[184:187], v[128:131], v[100:103]\n"
"ds_read_b64_tr_b16 a[166:167], v217 offset:28672\n"
"v_cvt_pk_bf16_f32 v174, v196, v197\n"
"v_cvt_pk_bf16_f32 v123, v126, v127\n"
"v_mfma_f32_16x16x32_bf16 v[108:111], a[184:187], v[136:139], 0\n"
"v_exp_f32 v198, v198\n"
"v_mfma_f32_16x16x32_bf16 v[108:111], a[188:191], v[140:143], v[108:111]\n"
"ds_read_b64_tr_b16 a[168:169], v217 offset:16392\n"
"ds_write_b128 v225, v[120:123] offset:2048\n"
"v_mfma_f32_16x16x32_bf16 v[104:107], a[180:183], v[140:143], 0\n"
"s_add_u32 m0, s65, 49152\n"
"v_exp_f32 v199, v199\n"
"v_mfma_f32_16x16x32_bf16 v[104:107], a[176:179], v[136:139], v[104:107]\n"
"global_load_lds_dwordx4 v219, s[50:51] offset:1024\n"
"v_cvt_pk_bf16_f32 v175, v198, v199\n"
"v_lshlrev_b32 v234, 16, v203\n"
"v_and_b32 v235, s67, v203\n"
"v_mul_f32 v228, s68, v228\n"
"v_mul_f32 v229, s68, v229\n"
"v_pk_mul_f32 v[230:231], v[230:231], s[68:69]\n"
"v_pk_mul_f32 v[232:233], v[232:233], s[68:69]\n"
"v_pk_mul_f32 v[234:235], v[234:235], s[68:69]\n"
"v_cvt_pk_bf16_f32 v228, v228, v229\n"
"v_cvt_pk_bf16_f32 v230, v230, v231\n"
"v_mfma_f32_16x16x32_bf16 v[112:115], a[176:179], v[144:147], 0\n"
"v_exp_f32 v248, v248\n"
"v_mfma_f32_16x16x32_bf16 v[112:115], a[180:183], v[148:151], v[112:115]\n"
"ds_read_b64_tr_b16 a[170:171], v217 offset:20488\n"
"ds_read_b128 a[224:227], v226 offset:2048\n"
"v_mfma_f32_16x16x32_bf16 v[116:119], a[188:191], v[148:151], 0\n"
"v_exp_f32 v249, v249\n"
"v_mfma_f32_16x16x32_bf16 v[116:119], a[184:187], v[144:147], v[116:119]\n"
"ds_read_b64_tr_b16 a[172:173], v217 offset:24584\n"
"ds_read_b128 a[228:231], v226 offset:3072\n"
"v_cvt_pk_bf16_f32 v182, v248, v249\n"
"v_mfma_f32_16x16x32_bf16 v[124:127], a[184:187], v[152:155], 0\n"
"s_sub_u32 s72, s72, 1\n"
"v_exp_f32 v250, v250\n"
"v_mfma_f32_16x16x32_bf16 v[124:127], a[188:191], v[156:159], v[124:127]\n"
"ds_read_b64_tr_b16 a[174:175], v217 offset:28680\n"
"v_cvt_pk_bf16_f32 v232, v232, v233\n"
"v_mfma_f32_16x16x32_bf16 v[120:123], a[180:183], v[156:159], 0\n"
"s_add_u32 m0, s65, 49152\n"
"v_exp_f32 v251, v251\n"
"v_mfma_f32_16x16x32_bf16 v[120:123], a[176:179], v[152:155], v[120:123]\n"
"s_cmp_lg_u32 s72, 0\n"
"global_load_lds_dwordx4 v219, s[50:51] offset:2048\n"
"v_cvt_pk_bf16_f32 v183, v250, v251\n"
"s_cbranch_scc1 .Lattn_B7\n"
"v_mfma_f32_16x16x32_bf16 v[192:195], a[128:131], a[64:67], -4.0\n"
"s_waitcnt lgkmcnt(7)\n"
"v_exp_f32 v252, v252\n"
"v_mfma_f32_16x16x32_bf16 v[192:195], a[132:135], a[68:71], v[192:195]\n"
"ds_read_b128 a[144:147], v216 offset:36864\n"
"v_mfma_f32_16x16x32_bf16 v[192:195], a[136:139], a[72:75], v[192:195]\n"
"v_exp_f32 v253, v253\n"
"v_mfma_f32_16x16x32_bf16 v[192:195], a[140:143], a[76:79], v[192:195]\n"
"ds_read_b128 a[148:151], v216 offset:37120\n"
"v_cvt_pk_bf16_f32 v190, v252, v253\n"
"v_mfma_f32_16x16x32_bf16 v[196:199], a[140:143], a[92:95], -4.0\n"
"v_exp_f32 v254, v254\n"
"v_mfma_f32_16x16x32_bf16 v[196:199], a[136:139], a[88:91], v[196:199]\n"
"ds_read_b128 a[152:155], v216 offset:37376\n"
"v_mfma_f32_16x16x32_bf16 v[196:199], a[132:135], a[84:87], v[196:199]\n"
"v_exp_f32 v255, v255\n"
"v_mfma_f32_16x16x32_bf16 v[196:199], a[128:131], a[80:83], v[196:199]\n"
"v_cvt_pk_bf16_f32 v191, v254, v255\n"
"v_mfma_f32_16x16x32_bf16 v[248:251], a[128:131], a[96:99], -4.0\n"
"v_exp_f32 v192, v192\n"
"v_mfma_f32_16x16x32_bf16 v[248:251], a[132:135], a[100:103], v[248:251]\n"
"ds_read_b128 a[156:159], v216 offset:37632\n"
"v_mfma_f32_16x16x32_bf16 v[248:251], a[136:139], a[104:107], v[248:251]\n"
"v_exp_f32 v193, v193\n"
"v_mfma_f32_16x16x32_bf16 v[248:251], a[140:143], a[108:111], v[248:251]\n"
"ds_read_b64_tr_b16 a[176:177], v217 offset:16640\n"
"v_cvt_pk_bf16_f32 v128, v192, v193\n"
"v_mfma_f32_16x16x32_bf16 v[252:255], a[140:143], a[124:127], -4.0\n"
"v_exp_f32 v194, v194\n"
"v_mfma_f32_16x16x32_bf16 v[252:255], a[136:139], a[120:123], v[252:255]\n"
"ds_read_b64_tr_b16 a[178:179], v217 offset:20736\n"
"v_mfma_f32_16x16x32_bf16 v[252:255], a[132:135], a[116:119], v[252:255]\n"
"s_add_u32 m0, s65, 49152\n"
"v_exp_f32 v195, v195\n"
"v_mfma_f32_16x16x32_bf16 v[252:255], a[128:131], a[112:115], v[252:255]\n"
"global_load_lds_dwordx4 v219, s[50:51] offset:3072\n"
"v_cvt_pk_bf16_f32 v129, v194, v195\n"
"v_smfmac_f32_16x16x64_bf16 a[192:195], a[208:211], v[160:167], v215\n"
"buffer_store_dwordx4 a[232:235], v223, s[56:59], s71 offen offset:0 sc1 nt\n"
"v_smfmac_f32_16x16x64_bf16 a[196:199], a[208:211], v[168:175], v215\n"
"ds_read_b64_tr_b16 a[180:181], v217 offset:24832\n"
"v_smfmac_f32_16x16x64_bf16 a[200:203], a[208:211], v[176:183], v215\n"
"v_smfmac_f32_16x16x64_bf16 a[204:207], a[208:211], v[184:191], v215\n"
"ds_read_b64_tr_b16 a[182:183], v217 offset:28928\n"
"v_mfma_f32_16x16x32_bf16 v[0:3], a[160:163], v[160:163], v[0:3]\n"
"v_exp_f32 v196, v196\n"
"v_mfma_f32_16x16x32_bf16 v[0:3], a[164:167], v[164:167], v[0:3]\n"
"s_waitcnt lgkmcnt(6)\n"
"ds_read_b64_tr_b16 a[184:185], v217 offset:16648\n"
"v_mfma_f32_16x16x32_bf16 v[4:7], a[172:175], v[164:167], v[4:7]\n"
"v_exp_f32 v197, v197\n"
"v_mfma_f32_16x16x32_bf16 v[4:7], a[168:171], v[160:163], v[4:7]\n"
"v_cvt_pk_bf16_f32 v136, v196, v197\n"
"v_mfma_f32_16x16x32_bf16 v[12:15], a[168:171], v[168:171], v[12:15]\n"
"v_exp_f32 v198, v198\n"
"v_mfma_f32_16x16x32_bf16 v[12:15], a[172:175], v[172:175], v[12:15]\n"
"s_bfe_u32 s48, s38, 0x30010\n"
"ds_read_b64_tr_b16 a[186:187], v217 offset:20744\n"
"v_mfma_f32_16x16x32_bf16 v[8:11], a[164:167], v[172:175], v[8:11]\n"
"s_lshl_b32 s48, s48, 14\n"
"v_exp_f32 v199, v199\n"
"v_mfma_f32_16x16x32_bf16 v[8:11], a[160:163], v[168:171], v[8:11]\n"
"s_add_u32 s36, s79, s48\n"
"ds_read_b64_tr_b16 a[188:189], v217 offset:24840\n"
"v_cvt_pk_bf16_f32 v137, v198, v199\n"
"v_mfma_f32_16x16x32_bf16 v[16:19], a[160:163], v[176:179], v[16:19]\n"
"s_add_u32 s48, s42, s36\n"
"v_exp_f32 v248, v248\n"
"v_mfma_f32_16x16x32_bf16 v[16:19], a[164:167], v[180:183], v[16:19]\n"
"s_addc_u32 s49, s43, 0\n"
"ds_read_b64_tr_b16 a[190:191], v217 offset:28936\n"
"v_mfma_f32_16x16x32_bf16 v[20:23], a[172:175], v[180:183], v[20:23]\n"
"s_add_u32 m0, s64, 0\n"
"v_exp_f32 v249, v249\n"
"v_mfma_f32_16x16x32_bf16 v[20:23], a[168:171], v[176:179], v[20:23]\n"
"global_load_lds_dwordx4 v219, s[48:49] offset:0\n"
"v_cvt_pk_bf16_f32 v144, v248, v249\n"
"v_mfma_f32_16x16x32_bf16 v[28:31], a[168:171], v[184:187], v[28:31]\n"
"v_exp_f32 v250, v250\n"
"v_mfma_f32_16x16x32_bf16 v[28:31], a[172:175], v[188:191], v[28:31]\n"
"v_mfma_f32_16x16x32_bf16 v[24:27], a[164:167], v[188:191], v[24:27]\n"
"v_exp_f32 v251, v251\n"
"v_mfma_f32_16x16x32_bf16 v[24:27], a[160:163], v[184:187], v[24:27]\n"
"v_cvt_pk_bf16_f32 v145, v250, v251\n"
"v_mfma_f32_16x16x32_bf16 v[192:195], a[144:147], a[64:67], -4.0\n"
"v_exp_f32 v252, v252\n"
"v_mfma_f32_16x16x32_bf16 v[192:195], a[148:151], a[68:71], v[192:195]\n"
"s_waitcnt lgkmcnt(4)\n"
"ds_read_b128 a[128:131], v216 offset:40960\n"
"v_mfma_f32_16x16x32_bf16 v[192:195], a[152:155], a[72:75], v[192:195]\n"
"v_exp_f32 v253, v253\n"
"v_mfma_f32_16x16x32_bf16 v[192:195], a[156:159], a[76:79], v[192:195]\n"
"ds_read_b128 a[132:135], v216 offset:41216\n"
"v_cvt_pk_bf16_f32 v152, v252, v253\n"
"v_mfma_f32_16x16x32_bf16 v[196:199], a[156:159], a[92:95], -4.0\n"
"v_exp_f32 v254, v254\n"
"v_mfma_f32_16x16x32_bf16 v[196:199], a[152:155], a[88:91], v[196:199]\n"
"ds_read_b128 a[136:139], v216 offset:41472\n"
"v_mfma_f32_16x16x32_bf16 v[196:199], a[148:151], a[84:87], v[196:199]\n"
"s_add_u32 m0, s64, 0\n"
"v_exp_f32 v255, v255\n"
"v_mfma_f32_16x16x32_bf16 v[196:199], a[144:147], a[80:83], v[196:199]\n"
"global_load_lds_dwordx4 v219, s[48:49] offset:1024\n"
"v_cvt_pk_bf16_f32 v153, v254, v255\n"
"v_mfma_f32_16x16x32_bf16 v[248:251], a[144:147], a[96:99], -4.0\n"
"v_exp_f32 v192, v192\n"
"v_mfma_f32_16x16x32_bf16 v[248:251], a[148:151], a[100:103], v[248:251]\n"
"ds_read_b128 a[140:143], v216 offset:41728\n"
"v_mfma_f32_16x16x32_bf16 v[248:251], a[152:155], a[104:107], v[248:251]\n"
"v_exp_f32 v193, v193\n"
"v_mfma_f32_16x16x32_bf16 v[248:251], a[156:159], a[108:111], v[248:251]\n"
"ds_read_b64_tr_b16 a[160:161], v217 offset:16896\n"
"v_cvt_pk_bf16_f32 v130, v192, v193\n"
"v_mfma_f32_16x16x32_bf16 v[252:255], a[156:159], a[124:127], -4.0\n"
"v_exp_f32 v194, v194\n"
"v_mfma_f32_16x16x32_bf16 v[252:255], a[152:155], a[120:123], v[252:255]\n"
"ds_read_b64_tr_b16 a[162:163], v217 offset:20992\n"
"v_mfma_f32_16x16x32_bf16 v[252:255], a[148:151], a[116:119], v[252:255]\n"
"s_add_u32 m0, s64, 0\n"
"v_exp_f32 v195, v195\n"
"v_mfma_f32_16x16x32_bf16 v[252:255], a[144:147], a[112:115], v[252:255]\n"
"global_load_lds_dwordx4 v220, s[48:49] offset:2048\n"
"v_cvt_pk_bf16_f32 v131, v194, v195\n"
"v_mfma_f32_16x16x32_bf16 v[32:35], a[176:179], v[160:163], v[32:35]\n"
"v_exp_f32 v196, v196\n"
"v_mfma_f32_16x16x32_bf16 v[32:35], a[180:183], v[164:167], v[32:35]\n"
"s_waitcnt lgkmcnt(6)\n"
"ds_read_b64_tr_b16 a[164:165], v217 offset:25088\n"
"buffer_store_dwordx4 a[236:239], v223, s[56:59], s71 offen offset:2048 sc1 nt\n"
"v_mfma_f32_16x16x32_bf16 v[36:39], a[188:191], v[164:167], v[36:39]\n"
"v_exp_f32 v197, v197\n"
"v_mfma_f32_16x16x32_bf16 v[36:39], a[184:187], v[160:163], v[36:39]\n"
"ds_read_b64_tr_b16 a[166:167], v217 offset:29184\n"
"v_cvt_pk_bf16_f32 v138, v196, v197\n"
"v_mfma_f32_16x16x32_bf16 v[44:47], a[184:187], v[168:171], v[44:47]\n"
"v_exp_f32 v198, v198\n"
"v_mfma_f32_16x16x32_bf16 v[44:47], a[188:191], v[172:175], v[44:47]\n"
"ds_read_b64_tr_b16 a[168:169], v217 offset:16904\n"
"v_mfma_f32_16x16x32_bf16 v[40:43], a[180:183], v[172:175], v[40:43]\n"
"v_exp_f32 v199, v199\n"
"v_mfma_f32_16x16x32_bf16 v[40:43], a[176:179], v[168:171], v[40:43]\n"
"v_cvt_pk_bf16_f32 v139, v198, v199\n"
"v_mfma_f32_16x16x32_bf16 v[48:51], a[176:179], v[176:179], v[48:51]\n"
"v_exp_f32 v248, v248\n"
"v_mfma_f32_16x16x32_bf16 v[48:51], a[180:183], v[180:183], v[48:51]\n"
"ds_read_b64_tr_b16 a[170:171], v217 offset:21000\n"
"v_mfma_f32_16x16x32_bf16 v[52:55], a[188:191], v[180:183], v[52:55]\n"
"v_exp_f32 v249, v249\n"
"v_mfma_f32_16x16x32_bf16 v[52:55], a[184:187], v[176:179], v[52:55]\n"
"ds_read_b64_tr_b16 a[172:173], v217 offset:25096\n"
"v_cvt_pk_bf16_f32 v146, v248, v249\n"
"v_mfma_f32_16x16x32_bf16 v[60:63], a[184:187], v[184:187], v[60:63]\n"
"v_exp_f32 v250, v250\n"
"v_mfma_f32_16x16x32_bf16 v[60:63], a[188:191], v[188:191], v[60:63]\n"
"ds_read_b64_tr_b16 a[174:175], v217 offset:29192\n"
"v_mfma_f32_16x16x32_bf16 v[56:59], a[180:183], v[188:191], v[56:59]\n"
"s_add_u32 m0, s64, 0\n"
"v_exp_f32 v251, v251\n"
"v_mfma_f32_16x16x32_bf16 v[56:59], a[176:179], v[184:187], v[56:59]\n"
"s_waitcnt lgkmcnt(6)\n"
"global_load_lds_dwordx4 v220, s[48:49] offset:3072\n"
"v_cvt_pk_bf16_f32 v147, v250, v251\n"
"v_mfma_f32_16x16x32_bf16 v[192:195], a[128:131], a[64:67], -4.0\n"
"v_exp_f32 v252, v252\n"
"v_mfma_f32_16x16x32_bf16 v[192:195], a[132:135], a[68:71], v[192:195]\n"
"ds_read_b128 a[144:147], v216 offset:45056\n"
"v_mfma_f32_16x16x32_bf16 v[192:195], a[136:139], a[72:75], v[192:195]\n"
"v_exp_f32 v253, v253\n"
"v_mfma_f32_16x16x32_bf16 v[192:195], a[140:143], a[76:79], v[192:195]\n"
"ds_read_b128 a[148:151], v216 offset:45312\n"
"v_cvt_pk_bf16_f32 v154, v252, v253\n"
"v_mfma_f32_16x16x32_bf16 v[196:199], a[140:143], a[92:95], -4.0\n"
"v_exp_f32 v254, v254\n"
"v_mfma_f32_16x16x32_bf16 v[196:199], a[136:139], a[88:91], v[196:199]\n"
"ds_read_b128 a[152:155], v216 offset:45568\n"
"v_mfma_f32_16x16x32_bf16 v[196:199], a[132:135], a[84:87], v[196:199]\n"
"v_exp_f32 v255, v255\n"
"v_mfma_f32_16x16x32_bf16 v[196:199], a[128:131], a[80:83], v[196:199]\n"
"buffer_store_dwordx4 a[240:243], v223, s[56:59], s37 offen offset:0 sc1 nt\n"
"v_cvt_pk_bf16_f32 v155, v254, v255\n"
"v_mfma_f32_16x16x32_bf16 v[248:251], a[128:131], a[96:99], -4.0\n"
"v_exp_f32 v192, v192\n"
"v_mfma_f32_16x16x32_bf16 v[248:251], a[132:135], a[100:103], v[248:251]\n"
"ds_read_b128 a[156:159], v216 offset:45824\n"
"v_mfma_f32_16x16x32_bf16 v[248:251], a[136:139], a[104:107], v[248:251]\n"
"v_exp_f32 v193, v193\n"
"v_mfma_f32_16x16x32_bf16 v[248:251], a[140:143], a[108:111], v[248:251]\n"
"ds_read_b64_tr_b16 a[176:177], v217 offset:17152\n"
"v_cvt_pk_bf16_f32 v132, v192, v193\n"
"v_mfma_f32_16x16x32_bf16 v[252:255], a[140:143], a[124:127], -4.0\n"
"v_exp_f32 v194, v194\n"
"v_mfma_f32_16x16x32_bf16 v[252:255], a[136:139], a[120:123], v[252:255]\n"
"ds_read_b64_tr_b16 a[178:179], v217 offset:21248\n"
"v_mfma_f32_16x16x32_bf16 v[252:255], a[132:135], a[116:119], v[252:255]\n"
"v_exp_f32 v195, v195\n"
"v_mfma_f32_16x16x32_bf16 v[252:255], a[128:131], a[112:115], v[252:255]\n"
"v_cvt_pk_bf16_f32 v133, v194, v195\n"
"v_mfma_f32_16x16x32_bf16 v[64:67], a[160:163], v[160:163], v[64:67]\n"
"s_waitcnt lgkmcnt(6)\n"
"v_exp_f32 v196, v196\n"
"v_mfma_f32_16x16x32_bf16 v[64:67], a[164:167], v[164:167], v[64:67]\n"
"ds_read_b64_tr_b16 a[180:181], v217 offset:25344\n"
"v_mfma_f32_16x16x32_bf16 v[68:71], a[172:175], v[164:167], v[68:71]\n"
"v_exp_f32 v197, v197\n"
"v_mfma_f32_16x16x32_bf16 v[68:71], a[168:171], v[160:163], v[68:71]\n"
"ds_read_b64_tr_b16 a[182:183], v217 offset:29440\n"
"v_cvt_pk_bf16_f32 v140, v196, v197\n"
"v_mfma_f32_16x16x32_bf16 v[76:79], a[168:171], v[168:171], v[76:79]\n"
"v_exp_f32 v198, v198\n"
"v_mfma_f32_16x16x32_bf16 v[76:79], a[172:175], v[172:175], v[76:79]\n"
"ds_read_b64_tr_b16 a[184:185], v217 offset:17160\n"
"v_mfma_f32_16x16x32_bf16 v[72:75], a[164:167], v[172:175], v[72:75]\n"
"v_exp_f32 v199, v199\n"
"v_mfma_f32_16x16x32_bf16 v[72:75], a[160:163], v[168:171], v[72:75]\n"
"v_cvt_pk_bf16_f32 v141, v198, v199\n"
"v_mfma_f32_16x16x32_bf16 v[80:83], a[160:163], v[176:179], v[80:83]\n"
"v_exp_f32 v248, v248\n"
"v_mfma_f32_16x16x32_bf16 v[80:83], a[164:167], v[180:183], v[80:83]\n"
"ds_read_b64_tr_b16 a[186:187], v217 offset:21256\n"
"v_mfma_f32_16x16x32_bf16 v[84:87], a[172:175], v[180:183], v[84:87]\n"
"v_exp_f32 v249, v249\n"
"v_mfma_f32_16x16x32_bf16 v[84:87], a[168:171], v[176:179], v[84:87]\n"
"ds_read_b64_tr_b16 a[188:189], v217 offset:25352\n"
"v_cvt_pk_bf16_f32 v148, v248, v249\n"
"v_mfma_f32_16x16x32_bf16 v[92:95], a[168:171], v[184:187], v[92:95]\n"
"v_exp_f32 v250, v250\n"
"v_mfma_f32_16x16x32_bf16 v[92:95], a[172:175], v[188:191], v[92:95]\n"
"ds_read_b64_tr_b16 a[190:191], v217 offset:29448\n"
"v_mfma_f32_16x16x32_bf16 v[88:91], a[164:167], v[188:191], v[88:91]\n"
"v_exp_f32 v251, v251\n"
"v_mfma_f32_16x16x32_bf16 v[88:91], a[160:163], v[184:187], v[88:91]\n"
"s_waitcnt vmcnt(13) lgkmcnt(6)\n"
"v_cvt_pk_bf16_f32 v149, v250, v251\n"
"v_mfma_f32_16x16x32_bf16 v[192:195], a[144:147], a[64:67], -4.0\n"
"s_barrier\n"
"v_exp_f32 v252, v252\n"
"v_mfma_f32_16x16x32_bf16 v[192:195], a[148:151], a[68:71], v[192:195]\n"
"s_bfe_u32 s50, s38, 0x30010\n"
"ds_read_b128 a[128:131], v216 offset:49152\n"
"v_mfma_f32_16x16x32_bf16 v[192:195], a[152:155], a[72:75], v[192:195]\n"
"s_lshl_b32 s50, s50, 14\n"
"v_exp_f32 v253, v253\n"
"v_mfma_f32_16x16x32_bf16 v[192:195], a[156:159], a[76:79], v[192:195]\n"
"s_add_u32 s39, s54, s50\n"
"ds_read_b128 a[132:135], v216 offset:49408\n"
"v_cvt_pk_bf16_f32 v156, v252, v253\n"
"v_mfma_f32_16x16x32_bf16 v[196:199], a[156:159], a[92:95], -4.0\n"
"s_add_u32 s50, s44, s39\n"
"v_exp_f32 v254, v254\n"
"v_mfma_f32_16x16x32_bf16 v[196:199], a[152:155], a[88:91], v[196:199]\n"
"s_addc_u32 s51, s45, 0\n"
"ds_read_b128 a[136:139], v216 offset:49664\n"
"v_mfma_f32_16x16x32_bf16 v[196:199], a[148:151], a[84:87], v[196:199]\n"
"s_add_u32 m0, s65, 0\n"
"v_exp_f32 v255, v255\n"
"v_mfma_f32_16x16x32_bf16 v[196:199], a[144:147], a[80:83], v[196:199]\n"
"global_load_lds_dwordx4 v219, s[50:51] offset:0\n"
"v_cvt_pk_bf16_f32 v157, v254, v255\n"
"v_mfma_f32_16x16x32_bf16 v[248:251], a[144:147], a[96:99], -4.0\n"
"v_exp_f32 v192, v192\n"
"v_mfma_f32_16x16x32_bf16 v[248:251], a[148:151], a[100:103], v[248:251]\n"
"ds_read_b128 a[140:143], v216 offset:49920\n"
"v_mfma_f32_16x16x32_bf16 v[248:251], a[152:155], a[104:107], v[248:251]\n"
"v_exp_f32 v193, v193\n"
"v_mfma_f32_16x16x32_bf16 v[248:251], a[156:159], a[108:111], v[248:251]\n"
"ds_read_b64_tr_b16 a[160:161], v217 offset:32768\n"
"v_cvt_pk_bf16_f32 v134, v192, v193\n"
"v_mfma_f32_16x16x32_bf16 v[252:255], a[156:159], a[124:127], -4.0\n"
"v_exp_f32 v194, v194\n"
"v_mfma_f32_16x16x32_bf16 v[252:255], a[152:155], a[120:123], v[252:255]\n"
"ds_read_b64_tr_b16 a[162:163], v217 offset:36864\n"
"v_mfma_f32_16x16x32_bf16 v[252:255], a[148:151], a[116:119], v[252:255]\n"
"v_exp_f32 v195, v195\n"
"v_mfma_f32_16x16x32_bf16 v[252:255], a[144:147], a[112:115], v[252:255]\n"
"buffer_store_dwordx4 a[244:247], v223, s[56:59], s37 offen offset:2048 sc1 nt\n"
"v_cvt_pk_bf16_f32 v135, v194, v195\n"
"v_mfma_f32_16x16x32_bf16 v[96:99], a[176:179], v[160:163], v[96:99]\n"
"s_waitcnt lgkmcnt(6)\n"
"v_exp_f32 v196, v196\n"
"v_mfma_f32_16x16x32_bf16 v[96:99], a[180:183], v[164:167], v[96:99]\n"
"ds_read_b64_tr_b16 a[164:165], v217 offset:40960\n"
"v_mfma_f32_16x16x32_bf16 v[100:103], a[188:191], v[164:167], v[100:103]\n"
"v_exp_f32 v197, v197\n"
"v_mfma_f32_16x16x32_bf16 v[100:103], a[184:187], v[160:163], v[100:103]\n"
"ds_read_b64_tr_b16 a[166:167], v217 offset:45056\n"
"v_cvt_pk_bf16_f32 v142, v196, v197\n"
"v_mfma_f32_16x16x32_bf16 v[108:111], a[184:187], v[168:171], v[108:111]\n"
"v_exp_f32 v198, v198\n"
"v_mfma_f32_16x16x32_bf16 v[108:111], a[188:191], v[172:175], v[108:111]\n"
"ds_read_b64_tr_b16 a[168:169], v217 offset:32776\n"
"v_mfma_f32_16x16x32_bf16 v[104:107], a[180:183], v[172:175], v[104:107]\n"
"s_add_u32 m0, s65, 0\n"
"v_exp_f32 v199, v199\n"
"v_mfma_f32_16x16x32_bf16 v[104:107], a[176:179], v[168:171], v[104:107]\n"
"global_load_lds_dwordx4 v219, s[50:51] offset:1024\n"
"v_cvt_pk_bf16_f32 v143, v198, v199\n"
"v_mfma_f32_16x16x32_bf16 v[112:115], a[176:179], v[176:179], v[112:115]\n"
"v_exp_f32 v248, v248\n"
"v_mfma_f32_16x16x32_bf16 v[112:115], a[180:183], v[180:183], v[112:115]\n"
"ds_read_b64_tr_b16 a[170:171], v217 offset:36872\n"
"v_mfma_f32_16x16x32_bf16 v[116:119], a[188:191], v[180:183], v[116:119]\n"
"v_exp_f32 v249, v249\n"
"v_mfma_f32_16x16x32_bf16 v[116:119], a[184:187], v[176:179], v[116:119]\n"
"ds_read_b64_tr_b16 a[172:173], v217 offset:40968\n"
"v_cvt_pk_bf16_f32 v150, v248, v249\n"
"v_mfma_f32_16x16x32_bf16 v[124:127], a[184:187], v[184:187], v[124:127]\n"
"v_exp_f32 v250, v250\n"
"v_mfma_f32_16x16x32_bf16 v[124:127], a[188:191], v[188:191], v[124:127]\n"
"ds_read_b64_tr_b16 a[174:175], v217 offset:45064\n"
"v_mfma_f32_16x16x32_bf16 v[120:123], a[180:183], v[188:191], v[120:123]\n"
"s_add_u32 m0, s65, 0\n"
"v_exp_f32 v251, v251\n"
"v_mfma_f32_16x16x32_bf16 v[120:123], a[176:179], v[184:187], v[120:123]\n"
"s_waitcnt lgkmcnt(6)\n"
"global_load_lds_dwordx4 v219, s[50:51] offset:2048\n"
"v_cvt_pk_bf16_f32 v151, v250, v251\n"
"v_mfma_f32_16x16x32_bf16 v[192:195], a[128:131], a[64:67], -4.0\n"
".Lattn_pad12:\n"
"s_cbranch_vccnz .Lattn_ret\n"
"v_exp_f32 v252, v252\n"
"v_mfma_f32_16x16x32_bf16 v[192:195], a[132:135], a[68:71], v[192:195]\n"
"ds_read_b128 a[144:147], v216 offset:53248\n"
"v_mfma_f32_16x16x32_bf16 v[192:195], a[136:139], a[72:75], v[192:195]\n"
"v_exp_f32 v253, v253\n"
"v_mfma_f32_16x16x32_bf16 v[192:195], a[140:143], a[76:79], v[192:195]\n"
"ds_read_b128 a[148:151], v216 offset:53504\n"
"v_cvt_pk_bf16_f32 v158, v252, v253\n"
"v_mfma_f32_16x16x32_bf16 v[196:199], a[140:143], a[92:95], -4.0\n"
"v_exp_f32 v254, v254\n"
"v_mfma_f32_16x16x32_bf16 v[196:199], a[136:139], a[88:91], v[196:199]\n"
"ds_read_b128 a[152:155], v216 offset:53760\n"
"v_mfma_f32_16x16x32_bf16 v[196:199], a[132:135], a[84:87], v[196:199]\n"
"v_exp_f32 v255, v255\n"
"v_mfma_f32_16x16x32_bf16 v[196:199], a[128:131], a[80:83], v[196:199]\n"
"buffer_store_dwordx4 a[248:251], v223, s[56:59], s99 offen offset:128 sc1 nt\n"
"v_cvt_pk_bf16_f32 v159, v254, v255\n"
"v_mfma_f32_16x16x32_bf16 v[248:251], a[128:131], a[96:99], -4.0\n"
"v_exp_f32 v192, v192\n"
"v_mfma_f32_16x16x32_bf16 v[248:251], a[132:135], a[100:103], v[248:251]\n"
"ds_read_b128 a[156:159], v216 offset:54016\n"
"v_mfma_f32_16x16x32_bf16 v[248:251], a[136:139], a[104:107], v[248:251]\n"
"v_exp_f32 v193, v193\n"
"v_mfma_f32_16x16x32_bf16 v[248:251], a[140:143], a[108:111], v[248:251]\n"
"ds_read_b64_tr_b16 a[176:177], v217 offset:33024\n"
"v_cvt_pk_bf16_f32 v160, v192, v193\n"
"v_mfma_f32_16x16x32_bf16 v[252:255], a[140:143], a[124:127], -4.0\n"
"v_exp_f32 v194, v194\n"
"v_mfma_f32_16x16x32_bf16 v[252:255], a[136:139], a[120:123], v[252:255]\n"
"ds_read_b64_tr_b16 a[178:179], v217 offset:37120\n"
"v_mfma_f32_16x16x32_bf16 v[252:255], a[132:135], a[116:119], v[252:255]\n"
"s_add_u32 m0, s65, 0\n"
"v_exp_f32 v195, v195\n"
"v_mfma_f32_16x16x32_bf16 v[252:255], a[128:131], a[112:115], v[252:255]\n"
"global_load_lds_dwordx4 v219, s[50:51] offset:3072\n"
"v_cvt_pk_bf16_f32 v161, v194, v195\n"
"v_smfmac_f32_16x16x64_bf16 a[192:195], a[208:211], v[128:135], v215\n"
"v_smfmac_f32_16x16x64_bf16 a[196:199], a[208:211], v[136:143], v215\n"
"ds_read_b64_tr_b16 a[180:181], v217 offset:41216\n"
"v_smfmac_f32_16x16x64_bf16 a[200:203], a[208:211], v[144:151], v215\n"
"v_smfmac_f32_16x16x64_bf16 a[204:207], a[208:211], v[152:159], v215\n"
"ds_read_b64_tr_b16 a[182:183], v217 offset:45312\n"
"v_mfma_f32_16x16x32_bf16 v[0:3], a[160:163], v[128:131], v[0:3]\n"
"s_waitcnt lgkmcnt(7)\n"
"v_exp_f32 v196, v196\n"
"v_mfma_f32_16x16x32_bf16 v[0:3], a[164:167], v[132:135], v[0:3]\n"
"ds_read_b64_tr_b16 a[184:185], v217 offset:33032\n"
"v_mfma_f32_16x16x32_bf16 v[4:7], a[172:175], v[132:135], v[4:7]\n"
"v_exp_f32 v197, v197\n"
"v_mfma_f32_16x16x32_bf16 v[4:7], a[168:171], v[128:131], v[4:7]\n"
"v_cvt_pk_bf16_f32 v168, v196, v197\n"
"v_mfma_f32_16x16x32_bf16 v[12:15], a[168:171], v[136:139], v[12:15]\n"
"v_exp_f32 v198, v198\n"
"v_mfma_f32_16x16x32_bf16 v[12:15], a[172:175], v[140:143], v[12:15]\n"
"s_bfe_u32 s48, s38, 0x30014\n"
"ds_read_b64_tr_b16 a[186:187], v217 offset:37128\n"
"v_mfma_f32_16x16x32_bf16 v[8:11], a[164:167], v[140:143], v[8:11]\n"
"s_lshl_b32 s48, s48, 14\n"
"v_exp_f32 v199, v199\n"
"v_mfma_f32_16x16x32_bf16 v[8:11], a[160:163], v[136:139], v[8:11]\n"
"s_add_u32 s36, s79, s48\n"
"ds_read_b64_tr_b16 a[188:189], v217 offset:41224\n"
"v_cvt_pk_bf16_f32 v169, v198, v199\n"
"v_mfma_f32_16x16x32_bf16 v[16:19], a[160:163], v[144:147], v[16:19]\n"
"s_add_u32 s48, s42, s36\n"
"v_exp_f32 v248, v248\n"
"v_mfma_f32_16x16x32_bf16 v[16:19], a[164:167], v[148:151], v[16:19]\n"
"s_addc_u32 s49, s43, 0\n"
"ds_read_b64_tr_b16 a[190:191], v217 offset:45320\n"
"v_mfma_f32_16x16x32_bf16 v[20:23], a[172:175], v[148:151], v[20:23]\n"
"s_add_u32 m0, s64, 16384\n"
"v_exp_f32 v249, v249\n"
"v_mfma_f32_16x16x32_bf16 v[20:23], a[168:171], v[144:147], v[20:23]\n"
"global_load_lds_dwordx4 v219, s[48:49] offset:0\n"
"v_cvt_pk_bf16_f32 v176, v248, v249\n"
"v_mfma_f32_16x16x32_bf16 v[28:31], a[168:171], v[152:155], v[28:31]\n"
"v_exp_f32 v250, v250\n"
"v_mfma_f32_16x16x32_bf16 v[28:31], a[172:175], v[156:159], v[28:31]\n"
"v_mfma_f32_16x16x32_bf16 v[24:27], a[164:167], v[156:159], v[24:27]\n"
"v_exp_f32 v251, v251\n"
"v_mfma_f32_16x16x32_bf16 v[24:27], a[160:163], v[152:155], v[24:27]\n"
"v_cvt_pk_bf16_f32 v177, v250, v251\n"
"v_mfma_f32_16x16x32_bf16 v[192:195], a[144:147], a[64:67], -4.0\n"
"s_waitcnt lgkmcnt(5)\n"
"v_exp_f32 v252, v252\n"
"v_mfma_f32_16x16x32_bf16 v[192:195], a[148:151], a[68:71], v[192:195]\n"
"ds_read_b128 a[128:131], v216 offset:57344\n"
"v_mfma_f32_16x16x32_bf16 v[192:195], a[152:155], a[72:75], v[192:195]\n"
"v_exp_f32 v253, v253\n"
"v_mfma_f32_16x16x32_bf16 v[192:195], a[156:159], a[76:79], v[192:195]\n"
"ds_read_b128 a[132:135], v216 offset:57600\n"
"v_cvt_pk_bf16_f32 v184, v252, v253\n"
"v_mfma_f32_16x16x32_bf16 v[196:199], a[156:159], a[92:95], -4.0\n"
"v_exp_f32 v254, v254\n"
"v_mfma_f32_16x16x32_bf16 v[196:199], a[152:155], a[88:91], v[196:199]\n"
"ds_read_b128 a[136:139], v216 offset:57856\n"
"v_mfma_f32_16x16x32_bf16 v[196:199], a[148:151], a[84:87], v[196:199]\n"
"s_add_u32 m0, s64, 16384\n"
"v_exp_f32 v255, v255\n"
"v_mfma_f32_16x16x32_bf16 v[196:199], a[144:147], a[80:83], v[196:199]\n"
"global_load_lds_dwordx4 v219, s[48:49] offset:1024\n"
"v_cvt_pk_bf16_f32 v185, v254, v255\n"
"v_mfma_f32_16x16x32_bf16 v[248:251], a[144:147], a[96:99], -4.0\n"
"v_exp_f32 v192, v192\n"
"v_mfma_f32_16x16x32_bf16 v[248:251], a[148:151], a[100:103], v[248:251]\n"
"ds_read_b128 a[140:143], v216 offset:58112\n"
"v_mfma_f32_16x16x32_bf16 v[248:251], a[152:155], a[104:107], v[248:251]\n"
"v_exp_f32 v193, v193\n"
"v_mfma_f32_16x16x32_bf16 v[248:251], a[156:159], a[108:111], v[248:251]\n"
"ds_read_b64_tr_b16 a[160:161], v217 offset:33280\n"
"buffer_store_dwordx4 a[252:255], v223, s[56:59], s99 offen offset:2176 sc1 nt\n"
"v_cvt_pk_bf16_f32 v162, v192, v193\n"
"v_mfma_f32_16x16x32_bf16 v[252:255], a[156:159], a[124:127], -4.0\n"
"v_exp_f32 v194, v194\n"
"v_mfma_f32_16x16x32_bf16 v[252:255], a[152:155], a[120:123], v[252:255]\n"
"ds_read_b64_tr_b16 a[162:163], v217 offset:37376\n"
"v_mfma_f32_16x16x32_bf16 v[252:255], a[148:151], a[116:119], v[252:255]\n"
"s_add_u32 m0, s64, 16384\n"
"v_exp_f32 v195, v195\n"
"v_mfma_f32_16x16x32_bf16 v[252:255], a[144:147], a[112:115], v[252:255]\n"
"global_load_lds_dwordx4 v220, s[48:49] offset:2048\n"
"v_cvt_pk_bf16_f32 v163, v194, v195\n"
"v_mfma_f32_16x16x32_bf16 v[32:35], a[176:179], v[128:131], v[32:35]\n"
"s_waitcnt lgkmcnt(6)\n"
"v_exp_f32 v196, v196\n"
"v_mfma_f32_16x16x32_bf16 v[32:35], a[180:183], v[132:135], v[32:35]\n"
"ds_read_b64_tr_b16 a[164:165], v217 offset:41472\n"
"v_mfma_f32_16x16x32_bf16 v[36:39], a[188:191], v[132:135], v[36:39]\n"
"v_exp_f32 v197, v197\n"
"v_mfma_f32_16x16x32_bf16 v[36:39], a[184:187], v[128:131], v[36:39]\n"
"ds_read_b64_tr_b16 a[166:167], v217 offset:45568\n"
"v_cvt_pk_bf16_f32 v170, v196, v197\n"
"v_mfma_f32_16x16x32_bf16 v[44:47], a[184:187], v[136:139], v[44:47]\n"
"v_exp_f32 v198, v198\n"
"v_mfma_f32_16x16x32_bf16 v[44:47], a[188:191], v[140:143], v[44:47]\n"
"ds_read_b64_tr_b16 a[168:169], v217 offset:33288\n"
"v_mfma_f32_16x16x32_bf16 v[40:43], a[180:183], v[140:143], v[40:43]\n"
"v_exp_f32 v199, v199\n"
"v_mfma_f32_16x16x32_bf16 v[40:43], a[176:179], v[136:139], v[40:43]\n"
"v_cvt_pk_bf16_f32 v171, v198, v199\n"
"v_mfma_f32_16x16x32_bf16 v[48:51], a[176:179], v[144:147], v[48:51]\n"
"v_exp_f32 v248, v248\n"
"v_mfma_f32_16x16x32_bf16 v[48:51], a[180:183], v[148:151], v[48:51]\n"
"ds_read_b64_tr_b16 a[170:171], v217 offset:37384\n"
"v_mfma_f32_16x16x32_bf16 v[52:55], a[188:191], v[148:151], v[52:55]\n"
"v_exp_f32 v249, v249\n"
"v_mfma_f32_16x16x32_bf16 v[52:55], a[184:187], v[144:147], v[52:55]\n"
"ds_read_b64_tr_b16 a[172:173], v217 offset:41480\n"
"v_cvt_pk_bf16_f32 v178, v248, v249\n"
"v_mfma_f32_16x16x32_bf16 v[60:63], a[184:187], v[152:155], v[60:63]\n"
"v_exp_f32 v250, v250\n"
"v_mfma_f32_16x16x32_bf16 v[60:63], a[188:191], v[156:159], v[60:63]\n"
"ds_read_b64_tr_b16 a[174:175], v217 offset:45576\n"
"v_mfma_f32_16x16x32_bf16 v[56:59], a[180:183], v[156:159], v[56:59]\n"
"s_add_u32 m0, s64, 16384\n"
"v_exp_f32 v251, v251\n"
"v_mfma_f32_16x16x32_bf16 v[56:59], a[176:179], v[152:155], v[56:59]\n"
"s_waitcnt lgkmcnt(6)\n"
"global_load_lds_dwordx4 v220, s[48:49] offset:3072\n"
"v_cvt_pk_bf16_f32 v179, v250, v251\n"
"v_mfma_f32_16x16x32_bf16 v[192:195], a[128:131], a[64:67], -4.0\n"
"v_exp_f32 v252, v252\n"
"v_mfma_f32_16x16x32_bf16 v[192:195], a[132:135], a[68:71], v[192:195]\n"
"ds_read_b128 a[144:147], v216 offset:61440\n"
"v_mfma_f32_16x16x32_bf16 v[192:195], a[136:139], a[72:75], v[192:195]\n"
"v_exp_f32 v253, v253\n"
"v_mfma_f32_16x16x32_bf16 v[192:195], a[140:143], a[76:79], v[192:195]\n"
"ds_read_b128 a[148:151], v216 offset:61696\n"
"v_cvt_pk_bf16_f32 v186, v252, v253\n"
"v_mfma_f32_16x16x32_bf16 v[196:199], a[140:143], a[92:95], -4.0\n"
"v_exp_f32 v254, v254\n"
"v_mfma_f32_16x16x32_bf16 v[196:199], a[136:139], a[88:91], v[196:199]\n"
"ds_read_b128 a[152:155], v216 offset:61952\n"
"v_mfma_f32_16x16x32_bf16 v[196:199], a[132:135], a[84:87], v[196:199]\n"
"v_exp_f32 v255, v255\n"
"v_mfma_f32_16x16x32_bf16 v[196:199], a[128:131], a[80:83], v[196:199]\n"
"buffer_store_dwordx4 v[236:239], v223, s[56:59], s70 offen offset:128 sc1 nt\n"
"v_cvt_pk_bf16_f32 v187, v254, v255\n"
"v_mfma_f32_16x16x32_bf16 v[248:251], a[128:131], a[96:99], -4.0\n"
"v_exp_f32 v192, v192\n"
"v_mfma_f32_16x16x32_bf16 v[248:251], a[132:135], a[100:103], v[248:251]\n"
"ds_read_b128 a[156:159], v216 offset:62208\n"
"v_mfma_f32_16x16x32_bf16 v[248:251], a[136:139], a[104:107], v[248:251]\n"
"v_exp_f32 v193, v193\n"
"v_mfma_f32_16x16x32_bf16 v[248:251], a[140:143], a[108:111], v[248:251]\n"
"ds_read_b64_tr_b16 a[176:177], v217 offset:33536\n"
"v_cvt_pk_bf16_f32 v164, v192, v193\n"
"v_mfma_f32_16x16x32_bf16 v[252:255], a[140:143], a[124:127], -4.0\n"
"v_exp_f32 v194, v194\n"
"v_mfma_f32_16x16x32_bf16 v[252:255], a[136:139], a[120:123], v[252:255]\n"
"ds_read_b64_tr_b16 a[178:179], v217 offset:37632\n"
"v_mfma_f32_16x16x32_bf16 v[252:255], a[132:135], a[116:119], v[252:255]\n"
"v_exp_f32 v195, v195\n"
"v_mfma_f32_16x16x32_bf16 v[252:255], a[128:131], a[112:115], v[252:255]\n"
"v_cvt_pk_bf16_f32 v165, v194, v195\n"
"v_mfma_f32_16x16x32_bf16 v[64:67], a[160:163], v[128:131], v[64:67]\n"
"s_waitcnt lgkmcnt(6)\n"
"v_exp_f32 v196, v196\n"
"v_mfma_f32_16x16x32_bf16 v[64:67], a[164:167], v[132:135], v[64:67]\n"
"ds_read_b64_tr_b16 a[180:181], v217 offset:41728\n"
"v_mfma_f32_16x16x32_bf16 v[68:71], a[172:175], v[132:135], v[68:71]\n"
"v_exp_f32 v197, v197\n"
"v_mfma_f32_16x16x32_bf16 v[68:71], a[168:171], v[128:131], v[68:71]\n"
"ds_read_b64_tr_b16 a[182:183], v217 offset:45824\n"
"v_cvt_pk_bf16_f32 v172, v196, v197\n"
"v_mfma_f32_16x16x32_bf16 v[76:79], a[168:171], v[136:139], v[76:79]\n"
"v_exp_f32 v198, v198\n"
"v_mfma_f32_16x16x32_bf16 v[76:79], a[172:175], v[140:143], v[76:79]\n"
"ds_read_b64_tr_b16 a[184:185], v217 offset:33544\n"
"v_mfma_f32_16x16x32_bf16 v[72:75], a[164:167], v[140:143], v[72:75]\n"
"v_exp_f32 v199, v199\n"
"v_mfma_f32_16x16x32_bf16 v[72:75], a[160:163], v[136:139], v[72:75]\n"
"v_cvt_pk_bf16_f32 v173, v198, v199\n"
"v_mfma_f32_16x16x32_bf16 v[80:83], a[160:163], v[144:147], v[80:83]\n"
"v_exp_f32 v248, v248\n"
"v_mfma_f32_16x16x32_bf16 v[80:83], a[164:167], v[148:151], v[80:83]\n"
"ds_read_b64_tr_b16 a[186:187], v217 offset:37640\n"
"v_mfma_f32_16x16x32_bf16 v[84:87], a[172:175], v[148:151], v[84:87]\n"
"v_exp_f32 v249, v249\n"
"v_mfma_f32_16x16x32_bf16 v[84:87], a[168:171], v[144:147], v[84:87]\n"
"ds_read_b64_tr_b16 a[188:189], v217 offset:41736\n"
"v_cvt_pk_bf16_f32 v180, v248, v249\n"
"v_mfma_f32_16x16x32_bf16 v[92:95], a[168:171], v[152:155], v[92:95]\n"
"v_exp_f32 v250, v250\n"
"v_mfma_f32_16x16x32_bf16 v[92:95], a[172:175], v[156:159], v[92:95]\n"
"ds_read_b64_tr_b16 a[190:191], v217 offset:45832\n"
"v_mfma_f32_16x16x32_bf16 v[88:91], a[164:167], v[156:159], v[88:91]\n"
"v_exp_f32 v251, v251\n"
"v_mfma_f32_16x16x32_bf16 v[88:91], a[160:163], v[152:155], v[88:91]\n"
"s_waitcnt vmcnt(13) lgkmcnt(6)\n"
"v_cvt_pk_bf16_f32 v181, v250, v251\n"
"v_mfma_f32_16x16x32_bf16 v[192:195], a[144:147], a[64:67], -4.0\n"
"s_barrier\n"
"v_exp_f32 v252, v252\n"
"v_mfma_f32_16x16x32_bf16 v[192:195], a[148:151], a[68:71], v[192:195]\n"
"s_bfe_u32 s50, s38, 0x30014\n"
"ds_read_b128 a[128:131], v216 offset:0\n"
"v_mfma_f32_16x16x32_bf16 v[192:195], a[152:155], a[72:75], v[192:195]\n"
"s_lshl_b32 s50, s50, 14\n"
"v_exp_f32 v253, v253\n"
"v_mfma_f32_16x16x32_bf16 v[192:195], a[156:159], a[76:79], v[192:195]\n"
"s_add_u32 s39, s54, s50\n"
"ds_read_b128 a[132:135], v216 offset:256\n"
"v_cvt_pk_bf16_f32 v188, v252, v253\n"
"v_mfma_f32_16x16x32_bf16 v[196:199], a[156:159], a[92:95], -4.0\n"
"s_add_u32 s50, s44, s39\n"
"v_exp_f32 v254, v254\n"
"v_mfma_f32_16x16x32_bf16 v[196:199], a[152:155], a[88:91], v[196:199]\n"
"s_addc_u32 s51, s45, 0\n"
"ds_read_b128 a[136:139], v216 offset:512\n"
"v_mfma_f32_16x16x32_bf16 v[196:199], a[148:151], a[84:87], v[196:199]\n"
"s_add_u32 m0, s65, 16384\n"
"v_exp_f32 v255, v255\n"
"v_mfma_f32_16x16x32_bf16 v[196:199], a[144:147], a[80:83], v[196:199]\n"
"global_load_lds_dwordx4 v219, s[50:51] offset:0\n"
"v_cvt_pk_bf16_f32 v189, v254, v255\n"
"v_mfma_f32_16x16x32_bf16 v[248:251], a[144:147], a[96:99], -4.0\n"
"v_exp_f32 v192, v192\n"
"v_mfma_f32_16x16x32_bf16 v[248:251], a[148:151], a[100:103], v[248:251]\n"
"ds_read_b128 a[140:143], v216 offset:768\n"
"v_mfma_f32_16x16x32_bf16 v[248:251], a[152:155], a[104:107], v[248:251]\n"
"v_exp_f32 v193, v193\n"
"v_mfma_f32_16x16x32_bf16 v[248:251], a[156:159], a[108:111], v[248:251]\n"
"ds_read_b64_tr_b16 a[160:161], v217 offset:49152\n"
"buffer_store_dwordx4 v[240:243], v223, s[56:59], s70 offen offset:2176 sc1 nt\n"
"v_cvt_pk_bf16_f32 v166, v192, v193\n"
"v_mfma_f32_16x16x32_bf16 v[252:255], a[156:159], a[124:127], -4.0\n"
"v_exp_f32 v194, v194\n"
"v_mfma_f32_16x16x32_bf16 v[252:255], a[152:155], a[120:123], v[252:255]\n"
"ds_read_b64_tr_b16 a[162:163], v217 offset:53248\n"
"v_mfma_f32_16x16x32_bf16 v[252:255], a[148:151], a[116:119], v[252:255]\n"
"v_exp_f32 v195, v195\n"
"v_mfma_f32_16x16x32_bf16 v[252:255], a[144:147], a[112:115], v[252:255]\n"
"v_cvt_pk_bf16_f32 v167, v194, v195\n"
"v_mfma_f32_16x16x32_bf16 v[96:99], a[176:179], v[128:131], v[96:99]\n"
"s_waitcnt lgkmcnt(6)\n"
"v_exp_f32 v196, v196\n"
"v_mfma_f32_16x16x32_bf16 v[96:99], a[180:183], v[132:135], v[96:99]\n"
"ds_read_b64_tr_b16 a[164:165], v217 offset:57344\n"
"v_mfma_f32_16x16x32_bf16 v[100:103], a[188:191], v[132:135], v[100:103]\n"
"v_exp_f32 v197, v197\n"
"v_mfma_f32_16x16x32_bf16 v[100:103], a[184:187], v[128:131], v[100:103]\n"
"ds_read_b64_tr_b16 a[166:167], v217 offset:61440\n"
"v_cvt_pk_bf16_f32 v174, v196, v197\n"
"v_mfma_f32_16x16x32_bf16 v[108:111], a[184:187], v[136:139], v[108:111]\n"
"v_exp_f32 v198, v198\n"
"v_mfma_f32_16x16x32_bf16 v[108:111], a[188:191], v[140:143], v[108:111]\n"
"ds_read_b64_tr_b16 a[168:169], v217 offset:49160\n"
"v_mfma_f32_16x16x32_bf16 v[104:107], a[180:183], v[140:143], v[104:107]\n"
"s_add_u32 m0, s65, 16384\n"
"v_exp_f32 v199, v199\n"
"v_mfma_f32_16x16x32_bf16 v[104:107], a[176:179], v[136:139], v[104:107]\n"
"global_load_lds_dwordx4 v219, s[50:51] offset:1024\n"
"v_cvt_pk_bf16_f32 v175, v198, v199\n"
"v_mfma_f32_16x16x32_bf16 v[112:115], a[176:179], v[144:147], v[112:115]\n"
"v_exp_f32 v248, v248\n"
"v_mfma_f32_16x16x32_bf16 v[112:115], a[180:183], v[148:151], v[112:115]\n"
"ds_read_b64_tr_b16 a[170:171], v217 offset:53256\n"
"v_mfma_f32_16x16x32_bf16 v[116:119], a[188:191], v[148:151], v[116:119]\n"
"v_exp_f32 v249, v249\n"
"v_mfma_f32_16x16x32_bf16 v[116:119], a[184:187], v[144:147], v[116:119]\n"
"ds_read_b64_tr_b16 a[172:173], v217 offset:57352\n"
"v_cvt_pk_bf16_f32 v182, v248, v249\n"
"v_mfma_f32_16x16x32_bf16 v[124:127], a[184:187], v[152:155], v[124:127]\n"
"v_exp_f32 v250, v250\n"
"v_mfma_f32_16x16x32_bf16 v[124:127], a[188:191], v[156:159], v[124:127]\n"
"ds_read_b64_tr_b16 a[174:175], v217 offset:61448\n"
"v_mfma_f32_16x16x32_bf16 v[120:123], a[180:183], v[156:159], v[120:123]\n"
"s_add_u32 m0, s65, 16384\n"
"v_exp_f32 v251, v251\n"
"v_mfma_f32_16x16x32_bf16 v[120:123], a[176:179], v[152:155], v[120:123]\n"
"s_waitcnt lgkmcnt(6)\n"
"global_load_lds_dwordx4 v219, s[50:51] offset:2048\n"
"v_cvt_pk_bf16_f32 v183, v250, v251\n"
"v_mfma_f32_16x16x32_bf16 v[192:195], a[128:131], a[64:67], -4.0\n"
"v_exp_f32 v252, v252\n"
"v_mfma_f32_16x16x32_bf16 v[192:195], a[132:135], a[68:71], v[192:195]\n"
"ds_read_b128 a[144:147], v216 offset:4096\n"
"v_mfma_f32_16x16x32_bf16 v[192:195], a[136:139], a[72:75], v[192:195]\n"
"v_exp_f32 v253, v253\n"
"v_mfma_f32_16x16x32_bf16 v[192:195], a[140:143], a[76:79], v[192:195]\n"
"ds_read_b128 a[148:151], v216 offset:4352\n"
"v_cvt_pk_bf16_f32 v190, v252, v253\n"
"v_mfma_f32_16x16x32_bf16 v[196:199], a[140:143], a[92:95], -4.0\n"
"v_exp_f32 v254, v254\n"
"v_mfma_f32_16x16x32_bf16 v[196:199], a[136:139], a[88:91], v[196:199]\n"
"ds_read_b128 a[152:155], v216 offset:4608\n"
"v_mfma_f32_16x16x32_bf16 v[196:199], a[132:135], a[84:87], v[196:199]\n"
"v_exp_f32 v255, v255\n"
"v_mfma_f32_16x16x32_bf16 v[196:199], a[128:131], a[80:83], v[196:199]\n"
"buffer_store_dwordx4 a[216:219], v223, s[56:59], s71 offen offset:128 sc1 nt\n"
"v_cvt_pk_bf16_f32 v191, v254, v255\n"
"v_mfma_f32_16x16x32_bf16 v[248:251], a[128:131], a[96:99], -4.0\n"
"v_exp_f32 v192, v192\n"
"v_mfma_f32_16x16x32_bf16 v[248:251], a[132:135], a[100:103], v[248:251]\n"
"ds_read_b128 a[156:159], v216 offset:4864\n"
"v_mfma_f32_16x16x32_bf16 v[248:251], a[136:139], a[104:107], v[248:251]\n"
"v_exp_f32 v193, v193\n"
"v_mfma_f32_16x16x32_bf16 v[248:251], a[140:143], a[108:111], v[248:251]\n"
"ds_read_b64_tr_b16 a[176:177], v217 offset:49408\n"
"v_cvt_pk_bf16_f32 v128, v192, v193\n"
"v_mfma_f32_16x16x32_bf16 v[252:255], a[140:143], a[124:127], -4.0\n"
"v_exp_f32 v194, v194\n"
"v_mfma_f32_16x16x32_bf16 v[252:255], a[136:139], a[120:123], v[252:255]\n"
"ds_read_b64_tr_b16 a[178:179], v217 offset:53504\n"
"v_mfma_f32_16x16x32_bf16 v[252:255], a[132:135], a[116:119], v[252:255]\n"
"s_add_u32 m0, s65, 16384\n"
"v_exp_f32 v195, v195\n"
"v_mfma_f32_16x16x32_bf16 v[252:255], a[128:131], a[112:115], v[252:255]\n"
"global_load_lds_dwordx4 v219, s[50:51] offset:3072\n"
"v_cvt_pk_bf16_f32 v129, v194, v195\n"
"v_smfmac_f32_16x16x64_bf16 a[192:195], a[208:211], v[160:167], v215\n"
"v_smfmac_f32_16x16x64_bf16 a[196:199], a[208:211], v[168:175], v215\n"
"ds_read_b64_tr_b16 a[180:181], v217 offset:57600\n"
"v_smfmac_f32_16x16x64_bf16 a[200:203], a[208:211], v[176:183], v215\n"
"v_smfmac_f32_16x16x64_bf16 a[204:207], a[208:211], v[184:191], v215\n"
"ds_read_b64_tr_b16 a[182:183], v217 offset:61696\n"
"v_mfma_f32_16x16x32_bf16 v[0:3], a[160:163], v[160:163], v[0:3]\n"
"s_waitcnt lgkmcnt(7)\n"
"v_exp_f32 v196, v196\n"
"v_mfma_f32_16x16x32_bf16 v[0:3], a[164:167], v[164:167], v[0:3]\n"
"ds_read_b64_tr_b16 a[184:185], v217 offset:49416\n"
"v_mfma_f32_16x16x32_bf16 v[4:7], a[172:175], v[164:167], v[4:7]\n"
"v_exp_f32 v197, v197\n"
"v_mfma_f32_16x16x32_bf16 v[4:7], a[168:171], v[160:163], v[4:7]\n"
"v_cvt_pk_bf16_f32 v136, v196, v197\n"
"v_mfma_f32_16x16x32_bf16 v[12:15], a[168:171], v[168:171], v[12:15]\n"
"v_exp_f32 v198, v198\n"
"v_mfma_f32_16x16x32_bf16 v[12:15], a[172:175], v[172:175], v[12:15]\n"
"s_bfe_u32 s48, s38, 0x30018\n"
"ds_read_b64_tr_b16 a[186:187], v217 offset:53512\n"
"v_mfma_f32_16x16x32_bf16 v[8:11], a[164:167], v[172:175], v[8:11]\n"
"s_lshl_b32 s48, s48, 14\n"
"v_exp_f32 v199, v199\n"
"v_mfma_f32_16x16x32_bf16 v[8:11], a[160:163], v[168:171], v[8:11]\n"
"s_add_u32 s36, s79, s48\n"
"ds_read_b64_tr_b16 a[188:189], v217 offset:57608\n"
"v_cvt_pk_bf16_f32 v137, v198, v199\n"
"v_mfma_f32_16x16x32_bf16 v[16:19], a[160:163], v[176:179], v[16:19]\n"
"s_add_u32 s48, s42, s36\n"
"v_exp_f32 v248, v248\n"
"v_mfma_f32_16x16x32_bf16 v[16:19], a[164:167], v[180:183], v[16:19]\n"
"s_addc_u32 s49, s43, 0\n"
"ds_read_b64_tr_b16 a[190:191], v217 offset:61704\n"
"v_mfma_f32_16x16x32_bf16 v[20:23], a[172:175], v[180:183], v[20:23]\n"
"s_add_u32 m0, s64, 32768\n"
"v_exp_f32 v249, v249\n"
"v_mfma_f32_16x16x32_bf16 v[20:23], a[168:171], v[176:179], v[20:23]\n"
"global_load_lds_dwordx4 v219, s[48:49] offset:0\n"
"v_cvt_pk_bf16_f32 v144, v248, v249\n"
"v_mfma_f32_16x16x32_bf16 v[28:31], a[168:171], v[184:187], v[28:31]\n"
"v_exp_f32 v250, v250\n"
"v_mfma_f32_16x16x32_bf16 v[28:31], a[172:175], v[188:191], v[28:31]\n"
"buffer_store_dwordx4 a[220:223], v223, s[56:59], s71 offen offset:2176 sc1 nt\n"
"v_mfma_f32_16x16x32_bf16 v[24:27], a[164:167], v[188:191], v[24:27]\n"
"v_exp_f32 v251, v251\n"
"v_mfma_f32_16x16x32_bf16 v[24:27], a[160:163], v[184:187], v[24:27]\n"
"v_cvt_pk_bf16_f32 v145, v250, v251\n"
"v_mfma_f32_16x16x32_bf16 v[192:195], a[144:147], a[64:67], -4.0\n"
"s_waitcnt lgkmcnt(5)\n"
"v_exp_f32 v252, v252\n"
"v_mfma_f32_16x16x32_bf16 v[192:195], a[148:151], a[68:71], v[192:195]\n"
"ds_read_b128 a[128:131], v216 offset:8192\n"
"v_mfma_f32_16x16x32_bf16 v[192:195], a[152:155], a[72:75], v[192:195]\n"
"v_exp_f32 v253, v253\n"
"v_mfma_f32_16x16x32_bf16 v[192:195], a[156:159], a[76:79], v[192:195]\n"
"ds_read_b128 a[132:135], v216 offset:8448\n"
"v_cvt_pk_bf16_f32 v152, v252, v253\n"
"v_mfma_f32_16x16x32_bf16 v[196:199], a[156:159], a[92:95], -4.0\n"
"v_exp_f32 v254, v254\n"
"v_mfma_f32_16x16x32_bf16 v[196:199], a[152:155], a[88:91], v[196:199]\n"
"ds_read_b128 a[136:139], v216 offset:8704\n"
"v_mfma_f32_16x16x32_bf16 v[196:199], a[148:151], a[84:87], v[196:199]\n"
"s_add_u32 m0, s64, 32768\n"
"v_exp_f32 v255, v255\n"
"v_mfma_f32_16x16x32_bf16 v[196:199], a[144:147], a[80:83], v[196:199]\n"
"global_load_lds_dwordx4 v219, s[48:49] offset:1024\n"
"v_cvt_pk_bf16_f32 v153, v254, v255\n"
"v_mfma_f32_16x16x32_bf16 v[248:251], a[144:147], a[96:99], -4.0\n"
"v_exp_f32 v192, v192\n"
"v_mfma_f32_16x16x32_bf16 v[248:251], a[148:151], a[100:103], v[248:251]\n"
"ds_read_b128 a[140:143], v216 offset:8960\n"
"v_mfma_f32_16x16x32_bf16 v[248:251], a[152:155], a[104:107], v[248:251]\n"
"v_exp_f32 v193, v193\n"
"v_mfma_f32_16x16x32_bf16 v[248:251], a[156:159], a[108:111], v[248:251]\n"
"ds_read_b64_tr_b16 a[160:161], v217 offset:49664\n"
"v_cvt_pk_bf16_f32 v130, v192, v193\n"
"v_mfma_f32_16x16x32_bf16 v[252:255], a[156:159], a[124:127], -4.0\n"
"v_exp_f32 v194, v194\n"
"v_mfma_f32_16x16x32_bf16 v[252:255], a[152:155], a[120:123], v[252:255]\n"
"ds_read_b64_tr_b16 a[162:163], v217 offset:53760\n"
"v_mfma_f32_16x16x32_bf16 v[252:255], a[148:151], a[116:119], v[252:255]\n"
"s_add_u32 m0, s64, 32768\n"
"v_exp_f32 v195, v195\n"
"v_mfma_f32_16x16x32_bf16 v[252:255], a[144:147], a[112:115], v[252:255]\n"
"global_load_lds_dwordx4 v220, s[48:49] offset:2048\n"
"v_cvt_pk_bf16_f32 v131, v194, v195\n"
"v_mfma_f32_16x16x32_bf16 v[32:35], a[176:179], v[160:163], v[32:35]\n"
"s_waitcnt lgkmcnt(6)\n"
"v_exp_f32 v196, v196\n"
"v_mfma_f32_16x16x32_bf16 v[32:35], a[180:183], v[164:167], v[32:35]\n"
"ds_read_b64_tr_b16 a[164:165], v217 offset:57856\n"
"v_mfma_f32_16x16x32_bf16 v[36:39], a[188:191], v[164:167], v[36:39]\n"
"v_exp_f32 v197, v197\n"
"v_mfma_f32_16x16x32_bf16 v[36:39], a[184:187], v[160:163], v[36:39]\n"
"ds_read_b64_tr_b16 a[166:167], v217 offset:61952\n"
"v_cvt_pk_bf16_f32 v138, v196, v197\n"
"v_mfma_f32_16x16x32_bf16 v[44:47], a[184:187], v[168:171], v[44:47]\n"
"v_exp_f32 v198, v198\n"
"v_mfma_f32_16x16x32_bf16 v[44:47], a[188:191], v[172:175], v[44:47]\n"
"ds_read_b64_tr_b16 a[168:169], v217 offset:49672\n"
"v_mfma_f32_16x16x32_bf16 v[40:43], a[180:183], v[172:175], v[40:43]\n"
"v_exp_f32 v199, v199\n"
"v_mfma_f32_16x16x32_bf16 v[40:43], a[176:179], v[168:171], v[40:43]\n"
"v_cvt_pk_bf16_f32 v139, v198, v199\n"
"v_mfma_f32_16x16x32_bf16 v[48:51], a[176:179], v[176:179], v[48:51]\n"
"v_exp_f32 v248, v248\n"
"v_mfma_f32_16x16x32_bf16 v[48:51], a[180:183], v[180:183], v[48:51]\n"
"ds_read_b64_tr_b16 a[170:171], v217 offset:53768\n"
"v_mfma_f32_16x16x32_bf16 v[52:55], a[188:191], v[180:183], v[52:55]\n"
"v_exp_f32 v249, v249\n"
"v_mfma_f32_16x16x32_bf16 v[52:55], a[184:187], v[176:179], v[52:55]\n"
"ds_read_b64_tr_b16 a[172:173], v217 offset:57864\n"
"v_cvt_pk_bf16_f32 v146, v248, v249\n"
"v_mfma_f32_16x16x32_bf16 v[60:63], a[184:187], v[184:187], v[60:63]\n"
"v_exp_f32 v250, v250\n"
"v_mfma_f32_16x16x32_bf16 v[60:63], a[188:191], v[188:191], v[60:63]\n"
"ds_read_b64_tr_b16 a[174:175], v217 offset:61960\n"
"v_mfma_f32_16x16x32_bf16 v[56:59], a[180:183], v[188:191], v[56:59]\n"
"s_add_u32 m0, s64, 32768\n"
"v_exp_f32 v251, v251\n"
"v_mfma_f32_16x16x32_bf16 v[56:59], a[176:179], v[184:187], v[56:59]\n"
"s_waitcnt lgkmcnt(6)\n"
"global_load_lds_dwordx4 v220, s[48:49] offset:3072\n"
"v_cvt_pk_bf16_f32 v147, v250, v251\n"
"v_mfma_f32_16x16x32_bf16 v[192:195], a[128:131], a[64:67], -4.0\n"
"v_exp_f32 v252, v252\n"
"v_mfma_f32_16x16x32_bf16 v[192:195], a[132:135], a[68:71], v[192:195]\n"
"ds_read_b128 a[144:147], v216 offset:12288\n"
"v_mfma_f32_16x16x32_bf16 v[192:195], a[136:139], a[72:75], v[192:195]\n"
"v_exp_f32 v253, v253\n"
"v_mfma_f32_16x16x32_bf16 v[192:195], a[140:143], a[76:79], v[192:195]\n"
"ds_read_b128 a[148:151], v216 offset:12544\n"
"v_cvt_pk_bf16_f32 v154, v252, v253\n"
"v_mfma_f32_16x16x32_bf16 v[196:199], a[140:143], a[92:95], -4.0\n"
"v_exp_f32 v254, v254\n"
"v_mfma_f32_16x16x32_bf16 v[196:199], a[136:139], a[88:91], v[196:199]\n"
"ds_read_b128 a[152:155], v216 offset:12800\n"
"v_mfma_f32_16x16x32_bf16 v[196:199], a[132:135], a[84:87], v[196:199]\n"
"v_exp_f32 v255, v255\n"
"v_mfma_f32_16x16x32_bf16 v[196:199], a[128:131], a[80:83], v[196:199]\n"
"buffer_store_dwordx4 a[224:227], v223, s[56:59], s37 offen offset:128 sc1 nt\n"
"v_cvt_pk_bf16_f32 v155, v254, v255\n"
"v_mfma_f32_16x16x32_bf16 v[248:251], a[128:131], a[96:99], -4.0\n"
"v_exp_f32 v192, v192\n"
"v_mfma_f32_16x16x32_bf16 v[248:251], a[132:135], a[100:103], v[248:251]\n"
"ds_read_b128 a[156:159], v216 offset:13056\n"
"v_mfma_f32_16x16x32_bf16 v[248:251], a[136:139], a[104:107], v[248:251]\n"
"v_exp_f32 v193, v193\n"
"v_mfma_f32_16x16x32_bf16 v[248:251], a[140:143], a[108:111], v[248:251]\n"
"ds_read_b64_tr_b16 a[176:177], v217 offset:49920\n"
"v_cvt_pk_bf16_f32 v132, v192, v193\n"
"v_mfma_f32_16x16x32_bf16 v[252:255], a[140:143], a[124:127], -4.0\n"
"v_exp_f32 v194, v194\n"
"v_mfma_f32_16x16x32_bf16 v[252:255], a[136:139], a[120:123], v[252:255]\n"
"ds_read_b64_tr_b16 a[178:179], v217 offset:54016\n"
"v_mfma_f32_16x16x32_bf16 v[252:255], a[132:135], a[116:119], v[252:255]\n"
"v_exp_f32 v195, v195\n"
"v_mfma_f32_16x16x32_bf16 v[252:255], a[128:131], a[112:115], v[252:255]\n"
"v_cvt_pk_bf16_f32 v133, v194, v195\n"
"v_mfma_f32_16x16x32_bf16 v[64:67], a[160:163], v[160:163], v[64:67]\n"
"s_waitcnt lgkmcnt(6)\n"
"v_exp_f32 v196, v196\n"
"v_mfma_f32_16x16x32_bf16 v[64:67], a[164:167], v[164:167], v[64:67]\n"
"ds_read_b64_tr_b16 a[180:181], v217 offset:58112\n"
"v_mfma_f32_16x16x32_bf16 v[68:71], a[172:175], v[164:167], v[68:71]\n"
"v_exp_f32 v197, v197\n"
"v_mfma_f32_16x16x32_bf16 v[68:71], a[168:171], v[160:163], v[68:71]\n"
"ds_read_b64_tr_b16 a[182:183], v217 offset:62208\n"
"v_cvt_pk_bf16_f32 v140, v196, v197\n"
"v_mfma_f32_16x16x32_bf16 v[76:79], a[168:171], v[168:171], v[76:79]\n"
"v_exp_f32 v198, v198\n"
"v_mfma_f32_16x16x32_bf16 v[76:79], a[172:175], v[172:175], v[76:79]\n"
"ds_read_b64_tr_b16 a[184:185], v217 offset:49928\n"
"v_mfma_f32_16x16x32_bf16 v[72:75], a[164:167], v[172:175], v[72:75]\n"
"v_exp_f32 v199, v199\n"
"v_mfma_f32_16x16x32_bf16 v[72:75], a[160:163], v[168:171], v[72:75]\n"
"v_cvt_pk_bf16_f32 v141, v198, v199\n"
"v_mfma_f32_16x16x32_bf16 v[80:83], a[160:163], v[176:179], v[80:83]\n"
"v_exp_f32 v248, v248\n"
"v_mfma_f32_16x16x32_bf16 v[80:83], a[164:167], v[180:183], v[80:83]\n"
"ds_read_b64_tr_b16 a[186:187], v217 offset:54024\n"
"v_mfma_f32_16x16x32_bf16 v[84:87], a[172:175], v[180:183], v[84:87]\n"
"v_exp_f32 v249, v249\n"
"v_mfma_f32_16x16x32_bf16 v[84:87], a[168:171], v[176:179], v[84:87]\n"
"ds_read_b64_tr_b16 a[188:189], v217 offset:58120\n"
"v_cvt_pk_bf16_f32 v148, v248, v249\n"
"v_mfma_f32_16x16x32_bf16 v[92:95], a[168:171], v[184:187], v[92:95]\n"
"v_exp_f32 v250, v250\n"
"v_mfma_f32_16x16x32_bf16 v[92:95], a[172:175], v[188:191], v[92:95]\n"
"ds_read_b64_tr_b16 a[190:191], v217 offset:62216\n"
"v_mfma_f32_16x16x32_bf16 v[88:91], a[164:167], v[188:191], v[88:91]\n"
"v_exp_f32 v251, v251\n"
"v_mfma_f32_16x16x32_bf16 v[88:91], a[160:163], v[184:187], v[88:91]\n"
"s_waitcnt vmcnt(13) lgkmcnt(0)\n"
"buffer_store_dwordx4 a[228:231], v223, s[56:59], s37 offen offset:2176 sc1 nt\n"
"v_cvt_pk_bf16_f32 v149, v250, v251\n"
"v_mfma_f32_16x16x32_bf16 v[192:195], a[144:147], a[64:67], -4.0\n"
"s_barrier\n"
"v_exp_f32 v252, v252\n"
"v_mfma_f32_16x16x32_bf16 v[192:195], a[148:151], a[68:71], v[192:195]\n"
"s_bfe_u32 s50, s38, 0x30018\n"
"ds_read_b128 a[128:131], v216 offset:16384\n"
"v_mfma_f32_16x16x32_bf16 v[192:195], a[152:155], a[72:75], v[192:195]\n"
"s_lshl_b32 s50, s50, 14\n"
"v_exp_f32 v253, v253\n"
"v_mfma_f32_16x16x32_bf16 v[192:195], a[156:159], a[76:79], v[192:195]\n"
"s_add_u32 s39, s54, s50\n"
"ds_read_b128 a[132:135], v216 offset:16640\n"
"v_cvt_pk_bf16_f32 v156, v252, v253\n"
"v_mfma_f32_16x16x32_bf16 v[196:199], a[156:159], a[92:95], -4.0\n"
"s_add_u32 s50, s44, s39\n"
"v_exp_f32 v254, v254\n"
"v_mfma_f32_16x16x32_bf16 v[196:199], a[152:155], a[88:91], v[196:199]\n"
"s_addc_u32 s51, s45, 0\n"
"ds_read_b128 a[136:139], v216 offset:16896\n"
"v_mfma_f32_16x16x32_bf16 v[196:199], a[148:151], a[84:87], v[196:199]\n"
"s_add_u32 m0, s65, 32768\n"
"v_exp_f32 v255, v255\n"
"v_mfma_f32_16x16x32_bf16 v[196:199], a[144:147], a[80:83], v[196:199]\n"
"global_load_lds_dwordx4 v219, s[50:51] offset:0\n"
"v_cvt_pk_bf16_f32 v157, v254, v255\n"
"v_mfma_f32_16x16x32_bf16 v[248:251], a[144:147], a[96:99], -4.0\n"
"v_exp_f32 v192, v192\n"
"v_mfma_f32_16x16x32_bf16 v[248:251], a[148:151], a[100:103], v[248:251]\n"
"ds_read_b128 a[140:143], v216 offset:17152\n"
"v_mfma_f32_16x16x32_bf16 v[248:251], a[152:155], a[104:107], v[248:251]\n"
"v_exp_f32 v193, v193\n"
"v_mfma_f32_16x16x32_bf16 v[248:251], a[156:159], a[108:111], v[248:251]\n"
"ds_read_b64_tr_b16 a[160:161], v217 offset:0\n"
"v_cvt_pk_bf16_f32 v134, v192, v193\n"
"v_mfma_f32_16x16x32_bf16 v[252:255], a[156:159], a[124:127], -4.0\n"
"v_exp_f32 v194, v194\n"
"v_mfma_f32_16x16x32_bf16 v[252:255], a[152:155], a[120:123], v[252:255]\n"
"ds_read_b64_tr_b16 a[162:163], v217 offset:4096\n"
"v_mfma_f32_16x16x32_bf16 v[252:255], a[148:151], a[116:119], v[252:255]\n"
".Lattn_pad13:\n"
"s_cbranch_vccnz .Lattn_ret\n"
"v_exp_f32 v195, v195\n"
"v_mfma_f32_16x16x32_bf16 v[252:255], a[144:147], a[112:115], v[252:255]\n"
"v_cvt_pk_bf16_f32 v135, v194, v195\n"
"v_mfma_f32_16x16x32_bf16 v[96:99], a[176:179], v[160:163], v[96:99]\n"
"v_exp_f32 v196, v196\n"
"v_mfma_f32_16x16x32_bf16 v[96:99], a[180:183], v[164:167], v[96:99]\n"
"ds_read_b64_tr_b16 a[164:165], v217 offset:8192\n"
"v_mfma_f32_16x16x32_bf16 v[100:103], a[188:191], v[164:167], v[100:103]\n"
"v_exp_f32 v197, v197\n"
"v_mfma_f32_16x16x32_bf16 v[100:103], a[184:187], v[160:163], v[100:103]\n"
"ds_read_b64_tr_b16 a[166:167], v217 offset:12288\n"
"v_cvt_pk_bf16_f32 v142, v196, v197\n"
"v_mfma_f32_16x16x32_bf16 v[108:111], a[184:187], v[168:171], v[108:111]\n"
"v_exp_f32 v198, v198\n"
"v_mfma_f32_16x16x32_bf16 v[108:111], a[188:191], v[172:175], v[108:111]\n"
"ds_read_b64_tr_b16 a[168:169], v217 offset:8\n"
"v_mfma_f32_16x16x32_bf16 v[104:107], a[180:183], v[172:175], v[104:107]\n"
"s_add_u32 m0, s65, 32768\n"
"v_exp_f32 v199, v199\n"
"v_mfma_f32_16x16x32_bf16 v[104:107], a[176:179], v[168:171], v[104:107]\n"
"global_load_lds_dwordx4 v219, s[50:51] offset:1024\n"
"v_cvt_pk_bf16_f32 v143, v198, v199\n"
"v_mfma_f32_16x16x32_bf16 v[112:115], a[176:179], v[176:179], v[112:115]\n"
"v_exp_f32 v248, v248\n"
"v_mfma_f32_16x16x32_bf16 v[112:115], a[180:183], v[180:183], v[112:115]\n"
"ds_read_b64_tr_b16 a[170:171], v217 offset:4104\n"
"v_mfma_f32_16x16x32_bf16 v[116:119], a[188:191], v[180:183], v[116:119]\n"
"v_exp_f32 v249, v249\n"
"v_mfma_f32_16x16x32_bf16 v[116:119], a[184:187], v[176:179], v[116:119]\n"
"ds_read_b64_tr_b16 a[172:173], v217 offset:8200\n"
"v_cvt_pk_bf16_f32 v150, v248, v249\n"
"v_mfma_f32_16x16x32_bf16 v[124:127], a[184:187], v[184:187], v[124:127]\n"
"v_exp_f32 v250, v250\n"
"v_mfma_f32_16x16x32_bf16 v[124:127], a[188:191], v[188:191], v[124:127]\n"
"ds_read_b64_tr_b16 a[174:175], v217 offset:12296\n"
"v_mfma_f32_16x16x32_bf16 v[120:123], a[180:183], v[188:191], v[120:123]\n"
"s_add_u32 m0, s65, 32768\n"
"v_exp_f32 v251, v251\n"
"v_mfma_f32_16x16x32_bf16 v[120:123], a[176:179], v[184:187], v[120:123]\n"
"s_waitcnt lgkmcnt(6)\n"
"global_load_lds_dwordx4 v219, s[50:51] offset:2048\n"
"v_cvt_pk_bf16_f32 v151, v250, v251\n"
"v_mfma_f32_16x16x32_bf16 v[192:195], a[128:131], a[64:67], -4.0\n"
"v_exp_f32 v252, v252\n"
"v_mfma_f32_16x16x32_bf16 v[192:195], a[132:135], a[68:71], v[192:195]\n"
"ds_read_b128 a[144:147], v216 offset:20480\n"
"v_mfma_f32_16x16x32_bf16 v[192:195], a[136:139], a[72:75], v[192:195]\n"
"v_exp_f32 v253, v253\n"
"v_mfma_f32_16x16x32_bf16 v[192:195], a[140:143], a[76:79], v[192:195]\n"
"ds_read_b128 a[148:151], v216 offset:20736\n"
"v_cvt_pk_bf16_f32 v158, v252, v253\n"
"v_mfma_f32_16x16x32_bf16 v[196:199], a[140:143], a[92:95], -4.0\n"
"v_exp_f32 v254, v254\n"
"v_mfma_f32_16x16x32_bf16 v[196:199], a[136:139], a[88:91], v[196:199]\n"
"ds_read_b128 a[152:155], v216 offset:20992\n"
"v_mfma_f32_16x16x32_bf16 v[196:199], a[132:135], a[84:87], v[196:199]\n"
"v_exp_f32 v255, v255\n"
"v_mfma_f32_16x16x32_bf16 v[196:199], a[128:131], a[80:83], v[196:199]\n"
"v_cvt_pk_bf16_f32 v159, v254, v255\n"
"v_mfma_f32_16x16x32_bf16 v[248:251], a[128:131], a[96:99], -4.0\n"
"v_exp_f32 v192, v192\n"
"v_mfma_f32_16x16x32_bf16 v[248:251], a[132:135], a[100:103], v[248:251]\n"
"ds_read_b128 a[156:159], v216 offset:21248\n"
"v_mfma_f32_16x16x32_bf16 v[248:251], a[136:139], a[104:107], v[248:251]\n"
"v_exp_f32 v193, v193\n"
"v_mfma_f32_16x16x32_bf16 v[248:251], a[140:143], a[108:111], v[248:251]\n"
"ds_read_b64_tr_b16 a[176:177], v217 offset:256\n"
"v_cvt_pk_bf16_f32 v160, v192, v193\n"
"v_mfma_f32_16x16x32_bf16 v[252:255], a[140:143], a[124:127], -4.0\n"
"v_exp_f32 v194, v194\n"
"v_mfma_f32_16x16x32_bf16 v[252:255], a[136:139], a[120:123], v[252:255]\n"
"ds_read_b64_tr_b16 a[178:179], v217 offset:4352\n"
"v_mfma_f32_16x16x32_bf16 v[252:255], a[132:135], a[116:119], v[252:255]\n"
"s_add_u32 m0, s65, 32768\n"
"v_exp_f32 v195, v195\n"
"v_mfma_f32_16x16x32_bf16 v[252:255], a[128:131], a[112:115], v[252:255]\n"
"global_load_lds_dwordx4 v219, s[50:51] offset:3072\n"
"v_cvt_pk_bf16_f32 v161, v194, v195\n"
"v_smfmac_f32_16x16x64_bf16 a[192:195], a[208:211], v[128:135], v215\n"
"s_waitcnt lgkmcnt(6)\n"
"v_smfmac_f32_16x16x64_bf16 a[196:199], a[208:211], v[136:143], v215\n"
"s_bfe_u32 s50, s38, 0x3001c\n"
"ds_read_b64_tr_b16 a[180:181], v217 offset:8448\n"
"v_smfmac_f32_16x16x64_bf16 a[200:203], a[208:211], v[144:151], v215\n"
"s_lshl_b32 s50, s50, 14\n"
"v_smfmac_f32_16x16x64_bf16 a[204:207], a[208:211], v[152:159], v215\n"
"s_add_u32 s39, s54, s50\n"
"ds_read_b64_tr_b16 a[182:183], v217 offset:12544\n"
"v_mfma_f32_16x16x32_bf16 v[0:3], a[160:163], v[128:131], v[0:3]\n"
"s_add_u32 s50, s44, s39\n"
"v_exp_f32 v196, v196\n"
"v_mfma_f32_16x16x32_bf16 v[0:3], a[164:167], v[132:135], v[0:3]\n"
"s_addc_u32 s51, s45, 0\n"
"ds_read_b64_tr_b16 a[184:185], v217 offset:264\n"
"v_mfma_f32_16x16x32_bf16 v[4:7], a[172:175], v[132:135], v[4:7]\n"
"s_add_u32 m0, s65, 49152\n"
"v_exp_f32 v197, v197\n"
"v_mfma_f32_16x16x32_bf16 v[4:7], a[168:171], v[128:131], v[4:7]\n"
"global_load_lds_dwordx4 v219, s[50:51] offset:0\n"
"v_cvt_pk_bf16_f32 v168, v196, v197\n"
"v_mfma_f32_16x16x32_bf16 v[12:15], a[168:171], v[136:139], v[12:15]\n"
"v_exp_f32 v198, v198\n"
"v_mfma_f32_16x16x32_bf16 v[12:15], a[172:175], v[140:143], v[12:15]\n"
"s_bfe_u32 s48, s38, 0x3001c\n"
"ds_read_b64_tr_b16 a[186:187], v217 offset:4360\n"
"v_mfma_f32_16x16x32_bf16 v[8:11], a[164:167], v[140:143], v[8:11]\n"
"s_lshl_b32 s48, s48, 14\n"
"v_exp_f32 v199, v199\n"
"v_mfma_f32_16x16x32_bf16 v[8:11], a[160:163], v[136:139], v[8:11]\n"
"s_add_u32 s36, s79, s48\n"
"ds_read_b64_tr_b16 a[188:189], v217 offset:8456\n"
"v_cvt_pk_bf16_f32 v169, v198, v199\n"
"v_mfma_f32_16x16x32_bf16 v[16:19], a[160:163], v[144:147], v[16:19]\n"
"s_add_u32 s48, s42, s36\n"
"v_exp_f32 v248, v248\n"
"v_mfma_f32_16x16x32_bf16 v[16:19], a[164:167], v[148:151], v[16:19]\n"
"s_addc_u32 s49, s43, 0\n"
"ds_read_b64_tr_b16 a[190:191], v217 offset:12552\n"
"v_mfma_f32_16x16x32_bf16 v[20:23], a[172:175], v[148:151], v[20:23]\n"
"s_add_u32 m0, s64, 49152\n"
"v_exp_f32 v249, v249\n"
"v_mfma_f32_16x16x32_bf16 v[20:23], a[168:171], v[144:147], v[20:23]\n"
"global_load_lds_dwordx4 v219, s[48:49] offset:0\n"
"v_cvt_pk_bf16_f32 v176, v248, v249\n"
"v_mfma_f32_16x16x32_bf16 v[28:31], a[168:171], v[152:155], v[28:31]\n"
"v_exp_f32 v250, v250\n"
"v_mfma_f32_16x16x32_bf16 v[28:31], a[172:175], v[156:159], v[28:31]\n"
"v_mfma_f32_16x16x32_bf16 v[24:27], a[164:167], v[156:159], v[24:27]\n"
"s_add_u32 m0, s65, 49152\n"
"v_exp_f32 v251, v251\n"
"v_mfma_f32_16x16x32_bf16 v[24:27], a[160:163], v[152:155], v[24:27]\n"
"s_waitcnt lgkmcnt(5)\n"
"global_load_lds_dwordx4 v219, s[50:51] offset:1024\n"
"v_cvt_pk_bf16_f32 v177, v250, v251\n"
"v_mfma_f32_16x16x32_bf16 v[192:195], a[144:147], a[64:67], -4.0\n"
"v_exp_f32 v252, v252\n"
"v_mfma_f32_16x16x32_bf16 v[192:195], a[148:151], a[68:71], v[192:195]\n"
"s_mov_b32 s98, s61\n"
"ds_read_b128 a[128:131], v216 offset:24576\n"
"v_mfma_f32_16x16x32_bf16 v[192:195], a[152:155], a[72:75], v[192:195]\n"
"v_exp_f32 v253, v253\n"
"v_mfma_f32_16x16x32_bf16 v[192:195], a[156:159], a[76:79], v[192:195]\n"
"s_mov_b32 s61, s62\n"
"ds_read_b128 a[132:135], v216 offset:24832\n"
"v_cvt_pk_bf16_f32 v184, v252, v253\n"
"v_mfma_f32_16x16x32_bf16 v[196:199], a[156:159], a[92:95], -4.0\n"
"v_exp_f32 v254, v254\n"
"v_mfma_f32_16x16x32_bf16 v[196:199], a[152:155], a[88:91], v[196:199]\n"
"s_add_u32 s74, s60, 32\n"
"ds_read_b128 a[136:139], v216 offset:25088\n"
"v_mfma_f32_16x16x32_bf16 v[196:199], a[148:151], a[84:87], v[196:199]\n"
"s_add_u32 m0, s64, 49152\n"
"v_exp_f32 v255, v255\n"
"v_mfma_f32_16x16x32_bf16 v[196:199], a[144:147], a[80:83], v[196:199]\n"
"s_sub_u32 s60, s60, 160\n"
"global_load_lds_dwordx4 v219, s[48:49] offset:1024\n"
"v_cvt_pk_bf16_f32 v185, v254, v255\n"
"v_mfma_f32_16x16x32_bf16 v[248:251], a[144:147], a[96:99], -4.0\n"
"s_cselect_b32 s60, s74, s60\n"
"v_exp_f32 v192, v192\n"
"v_mfma_f32_16x16x32_bf16 v[248:251], a[148:151], a[100:103], v[248:251]\n"
"ds_read_b128 a[140:143], v216 offset:25344\n"
"v_mfma_f32_16x16x32_bf16 v[248:251], a[152:155], a[104:107], v[248:251]\n"
"s_lshr_b32 s80, s80, 1\n"
"v_exp_f32 v193, v193\n"
"v_mfma_f32_16x16x32_bf16 v[248:251], a[156:159], a[108:111], v[248:251]\n"
"s_cselect_b32 s73, -1, 0\n"
"ds_read_b64_tr_b16 a[160:161], v217 offset:512\n"
"v_cvt_pk_bf16_f32 v162, v192, v193\n"
"v_mfma_f32_16x16x32_bf16 v[252:255], a[156:159], a[124:127], -4.0\n"
"v_exp_f32 v194, v194\n"
"v_mfma_f32_16x16x32_bf16 v[252:255], a[152:155], a[120:123], v[252:255]\n"
"s_mul_i32 s74, s76, 3\n"
"ds_read_b64_tr_b16 a[162:163], v217 offset:4608\n"
"v_mfma_f32_16x16x32_bf16 v[252:255], a[148:151], a[116:119], v[252:255]\n"
"s_add_u32 m0, s64, 49152\n"
"v_exp_f32 v195, v195\n"
"v_mfma_f32_16x16x32_bf16 v[252:255], a[144:147], a[112:115], v[252:255]\n"
"s_add_u32 s74, s74, s60\n"
"global_load_lds_dwordx4 v220, s[48:49] offset:2048\n"
"v_cvt_pk_bf16_f32 v163, v194, v195\n"
"v_mfma_f32_16x16x32_bf16 v[32:35], a[176:179], v[128:131], v[32:35]\n"
"s_waitcnt lgkmcnt(6)\n"
"v_exp_f32 v196, v196\n"
"v_mfma_f32_16x16x32_bf16 v[32:35], a[180:183], v[132:135], v[32:35]\n"
"s_lshl_b32 s74, s74, 16\n"
"ds_read_b64_tr_b16 a[164:165], v217 offset:8704\n"
"v_mfma_f32_16x16x32_bf16 v[36:39], a[188:191], v[132:135], v[36:39]\n"
"v_exp_f32 v197, v197\n"
"v_mfma_f32_16x16x32_bf16 v[36:39], a[184:187], v[128:131], v[36:39]\n"
"s_add_u32 s62, s74, s78\n"
"ds_read_b64_tr_b16 a[166:167], v217 offset:12800\n"
"v_cvt_pk_bf16_f32 v170, v196, v197\n"
"v_mfma_f32_16x16x32_bf16 v[44:47], a[184:187], v[136:139], v[44:47]\n"
"v_exp_f32 v198, v198\n"
"v_mfma_f32_16x16x32_bf16 v[44:47], a[188:191], v[140:143], v[44:47]\n"
"ds_read_b64_tr_b16 a[168:169], v217 offset:520\n"
"v_mfma_f32_16x16x32_bf16 v[40:43], a[180:183], v[140:143], v[40:43]\n"
"s_add_u32 m0, s65, 49152\n"
"v_exp_f32 v199, v199\n"
"v_mfma_f32_16x16x32_bf16 v[40:43], a[176:179], v[136:139], v[40:43]\n"
"global_load_lds_dwordx4 v219, s[50:51] offset:2048\n"
"v_cvt_pk_bf16_f32 v171, v198, v199\n"
"v_mfma_f32_16x16x32_bf16 v[48:51], a[176:179], v[144:147], v[48:51]\n"
"v_exp_f32 v248, v248\n"
"v_mfma_f32_16x16x32_bf16 v[48:51], a[180:183], v[148:151], v[48:51]\n"
"ds_read_b64_tr_b16 a[170:171], v217 offset:4616\n"
"v_mfma_f32_16x16x32_bf16 v[52:55], a[188:191], v[148:151], v[52:55]\n"
"v_exp_f32 v249, v249\n"
"v_mfma_f32_16x16x32_bf16 v[52:55], a[184:187], v[144:147], v[52:55]\n"
"ds_read_b64_tr_b16 a[172:173], v217 offset:8712\n"
"v_cvt_pk_bf16_f32 v178, v248, v249\n"
"v_mfma_f32_16x16x32_bf16 v[60:63], a[184:187], v[152:155], v[60:63]\n"
"v_exp_f32 v250, v250\n"
"v_mfma_f32_16x16x32_bf16 v[60:63], a[188:191], v[156:159], v[60:63]\n"
"ds_read_b64_tr_b16 a[174:175], v217 offset:12808\n"
"v_mfma_f32_16x16x32_bf16 v[56:59], a[180:183], v[156:159], v[56:59]\n"
"s_add_u32 m0, s64, 49152\n"
"v_exp_f32 v251, v251\n"
"v_mfma_f32_16x16x32_bf16 v[56:59], a[176:179], v[152:155], v[56:59]\n"
"s_waitcnt lgkmcnt(6)\n"
"global_load_lds_dwordx4 v220, s[48:49] offset:3072\n"
"v_cvt_pk_bf16_f32 v179, v250, v251\n"
"v_mfma_f32_16x16x32_bf16 v[192:195], a[128:131], a[64:67], -4.0\n"
"v_exp_f32 v252, v252\n"
"v_mfma_f32_16x16x32_bf16 v[192:195], a[132:135], a[68:71], v[192:195]\n"
"ds_read_b128 a[144:147], v216 offset:28672\n"
"v_mfma_f32_16x16x32_bf16 v[192:195], a[136:139], a[72:75], v[192:195]\n"
"v_exp_f32 v253, v253\n"
"v_mfma_f32_16x16x32_bf16 v[192:195], a[140:143], a[76:79], v[192:195]\n"
"ds_read_b128 a[148:151], v216 offset:28928\n"
"v_cvt_pk_bf16_f32 v186, v252, v253\n"
"v_mfma_f32_16x16x32_bf16 v[196:199], a[140:143], a[92:95], -4.0\n"
"v_exp_f32 v254, v254\n"
"v_mfma_f32_16x16x32_bf16 v[196:199], a[136:139], a[88:91], v[196:199]\n"
"ds_read_b128 a[152:155], v216 offset:29184\n"
"v_mfma_f32_16x16x32_bf16 v[196:199], a[132:135], a[84:87], v[196:199]\n"
"s_add_u32 m0, s65, 49152\n"
"v_exp_f32 v255, v255\n"
"v_mfma_f32_16x16x32_bf16 v[196:199], a[128:131], a[80:83], v[196:199]\n"
"global_load_lds_dwordx4 v219, s[50:51] offset:3072\n"
"v_cvt_pk_bf16_f32 v187, v254, v255\n"
"v_mfma_f32_16x16x32_bf16 v[248:251], a[128:131], a[96:99], -4.0\n"
"v_exp_f32 v192, v192\n"
"v_mfma_f32_16x16x32_bf16 v[248:251], a[132:135], a[100:103], v[248:251]\n"
"ds_read_b128 a[156:159], v216 offset:29440\n"
"v_mfma_f32_16x16x32_bf16 v[248:251], a[136:139], a[104:107], v[248:251]\n"
"v_exp_f32 v193, v193\n"
"v_mfma_f32_16x16x32_bf16 v[248:251], a[140:143], a[108:111], v[248:251]\n"
"ds_read_b64_tr_b16 a[176:177], v217 offset:768\n"
"v_cvt_pk_bf16_f32 v164, v192, v193\n"
"v_mfma_f32_16x16x32_bf16 v[252:255], a[140:143], a[124:127], -4.0\n"
"v_exp_f32 v194, v194\n"
"v_mfma_f32_16x16x32_bf16 v[252:255], a[136:139], a[120:123], v[252:255]\n"
"ds_read_b64_tr_b16 a[178:179], v217 offset:4864\n"
"v_mfma_f32_16x16x32_bf16 v[252:255], a[132:135], a[116:119], v[252:255]\n"
"v_exp_f32 v195, v195\n"
"v_mfma_f32_16x16x32_bf16 v[252:255], a[128:131], a[112:115], v[252:255]\n"
"v_cvt_pk_bf16_f32 v165, v194, v195\n"
"v_mfma_f32_16x16x32_bf16 v[64:67], a[160:163], v[128:131], v[64:67]\n"
"s_waitcnt lgkmcnt(6)\n"
"v_exp_f32 v196, v196\n"
"v_mfma_f32_16x16x32_bf16 v[64:67], a[164:167], v[132:135], v[64:67]\n"
"ds_read_b64_tr_b16 a[180:181], v217 offset:8960\n"
"v_mfma_f32_16x16x32_bf16 v[68:71], a[172:175], v[132:135], v[68:71]\n"
"v_exp_f32 v197, v197\n"
"v_mfma_f32_16x16x32_bf16 v[68:71], a[168:171], v[128:131], v[68:71]\n"
"ds_read_b64_tr_b16 a[182:183], v217 offset:13056\n"
"v_cvt_pk_bf16_f32 v172, v196, v197\n"
"v_mfma_f32_16x16x32_bf16 v[76:79], a[168:171], v[136:139], v[76:79]\n"
"v_exp_f32 v198, v198\n"
"v_mfma_f32_16x16x32_bf16 v[76:79], a[172:175], v[140:143], v[76:79]\n"
"ds_read_b64_tr_b16 a[184:185], v217 offset:776\n"
"v_mfma_f32_16x16x32_bf16 v[72:75], a[164:167], v[140:143], v[72:75]\n"
"v_exp_f32 v199, v199\n"
"v_mfma_f32_16x16x32_bf16 v[72:75], a[160:163], v[136:139], v[72:75]\n"
"v_cvt_pk_bf16_f32 v173, v198, v199\n"
"v_mfma_f32_16x16x32_bf16 v[80:83], a[160:163], v[144:147], v[80:83]\n"
"v_exp_f32 v248, v248\n"
"v_mfma_f32_16x16x32_bf16 v[80:83], a[164:167], v[148:151], v[80:83]\n"
"ds_read_b64_tr_b16 a[186:187], v217 offset:4872\n"
"v_mfma_f32_16x16x32_bf16 v[84:87], a[172:175], v[148:151], v[84:87]\n"
"v_exp_f32 v249, v249\n"
"v_mfma_f32_16x16x32_bf16 v[84:87], a[168:171], v[144:147], v[84:87]\n"
"ds_read_b64_tr_b16 a[188:189], v217 offset:8968\n"
"v_cvt_pk_bf16_f32 v180, v248, v249\n"
"v_mfma_f32_16x16x32_bf16 v[92:95], a[168:171], v[152:155], v[92:95]\n"
"v_exp_f32 v250, v250\n"
"v_mfma_f32_16x16x32_bf16 v[92:95], a[172:175], v[156:159], v[92:95]\n"
"ds_read_b64_tr_b16 a[190:191], v217 offset:13064\n"
"v_mfma_f32_16x16x32_bf16 v[88:91], a[164:167], v[156:159], v[88:91]\n"
"v_exp_f32 v251, v251\n"
"v_mfma_f32_16x16x32_bf16 v[88:91], a[160:163], v[152:155], v[88:91]\n"
"s_waitcnt vmcnt(14) lgkmcnt(6)\n"
"v_cvt_pk_bf16_f32 v181, v250, v251\n"
"v_mfma_f32_16x16x32_bf16 v[192:195], a[144:147], a[64:67], -4.0\n"
"s_barrier\n"
"v_exp_f32 v252, v252\n"
"v_mfma_f32_16x16x32_bf16 v[192:195], a[148:151], a[68:71], v[192:195]\n"
"ds_read_b128 a[128:131], v216 offset:32768\n"
"v_mfma_f32_16x16x32_bf16 v[192:195], a[152:155], a[72:75], v[192:195]\n"
"v_exp_f32 v253, v253\n"
"v_mfma_f32_16x16x32_bf16 v[192:195], a[156:159], a[76:79], v[192:195]\n"
"ds_read_b128 a[132:135], v216 offset:33024\n"
"v_cvt_pk_bf16_f32 v188, v252, v253\n"
"v_mfma_f32_16x16x32_bf16 v[196:199], a[156:159], a[92:95], -4.0\n"
"v_exp_f32 v254, v254\n"
"v_mfma_f32_16x16x32_bf16 v[196:199], a[152:155], a[88:91], v[196:199]\n"
"ds_read_b128 a[136:139], v216 offset:33280\n"
"v_mfma_f32_16x16x32_bf16 v[196:199], a[148:151], a[84:87], v[196:199]\n"
"v_exp_f32 v255, v255\n"
"v_mfma_f32_16x16x32_bf16 v[196:199], a[144:147], a[80:83], v[196:199]\n"
"v_cvt_pk_bf16_f32 v189, v254, v255\n"
"v_mfma_f32_16x16x32_bf16 v[248:251], a[144:147], a[96:99], -4.0\n"
"v_exp_f32 v192, v192\n"
"v_mfma_f32_16x16x32_bf16 v[248:251], a[148:151], a[100:103], v[248:251]\n"
"ds_read_b128 a[140:143], v216 offset:33536\n"
"v_mfma_f32_16x16x32_bf16 v[248:251], a[152:155], a[104:107], v[248:251]\n"
"s_mov_b32 s55, s62\n"
"v_exp_f32 v193, v193\n"
"v_mfma_f32_16x16x32_bf16 v[248:251], a[156:159], a[108:111], v[248:251]\n"
"s_add_u32 s35, s55, s99\n"
"ds_read_b64_tr_b16 a[160:161], v217 offset:16384\n"
"v_cvt_pk_bf16_f32 v166, v192, v193\n"
"v_mfma_f32_16x16x32_bf16 v[252:255], a[156:159], a[124:127], -4.0\n"
"s_add_u32 s52, s40, s35\n"
"v_exp_f32 v194, v194\n"
"v_mfma_f32_16x16x32_bf16 v[252:255], a[152:155], a[120:123], v[252:255]\n"
"s_addc_u32 s53, s41, 0\n"
"ds_read_b64_tr_b16 a[162:163], v217 offset:20480\n"
"v_mfma_f32_16x16x32_bf16 v[252:255], a[148:151], a[116:119], v[252:255]\n"
"v_exp_f32 v195, v195\n"
"v_mfma_f32_16x16x32_bf16 v[252:255], a[144:147], a[112:115], v[252:255]\n"
"v_cvt_pk_bf16_f32 v167, v194, v195\n"
"v_mfma_f32_16x16x32_bf16 v[96:99], a[176:179], v[128:131], v[96:99]\n"
"s_waitcnt lgkmcnt(6)\n"
"v_exp_f32 v196, v196\n"
"v_mfma_f32_16x16x32_bf16 v[96:99], a[180:183], v[132:135], v[96:99]\n"
"ds_read_b64_tr_b16 a[164:165], v217 offset:24576\n"
"v_mfma_f32_16x16x32_bf16 v[100:103], a[188:191], v[132:135], v[100:103]\n"
"v_exp_f32 v197, v197\n"
"v_mfma_f32_16x16x32_bf16 v[100:103], a[184:187], v[128:131], v[100:103]\n"
"ds_read_b64_tr_b16 a[166:167], v217 offset:28672\n"
"v_cvt_pk_bf16_f32 v174, v196, v197\n"
"v_mfma_f32_16x16x32_bf16 v[108:111], a[184:187], v[136:139], v[108:111]\n"
"v_exp_f32 v198, v198\n"
"v_mfma_f32_16x16x32_bf16 v[108:111], a[188:191], v[140:143], v[108:111]\n"
"ds_read_b64_tr_b16 a[168:169], v217 offset:16392\n"
"v_mfma_f32_16x16x32_bf16 v[104:107], a[180:183], v[140:143], v[104:107]\n"
"v_exp_f32 v199, v199\n"
"v_mfma_f32_16x16x32_bf16 v[104:107], a[176:179], v[136:139], v[104:107]\n"
"v_cvt_pk_bf16_f32 v175, v198, v199\n"
"v_mfma_f32_16x16x32_bf16 v[112:115], a[176:179], v[144:147], v[112:115]\n"
"v_exp_f32 v248, v248\n"
"v_mfma_f32_16x16x32_bf16 v[112:115], a[180:183], v[148:151], v[112:115]\n"
"ds_read_b64_tr_b16 a[170:171], v217 offset:20488\n"
"v_mfma_f32_16x16x32_bf16 v[116:119], a[188:191], v[148:151], v[116:119]\n"
"v_exp_f32 v249, v249\n"
"v_mfma_f32_16x16x32_bf16 v[116:119], a[184:187], v[144:147], v[116:119]\n"
"ds_read_b64_tr_b16 a[172:173], v217 offset:24584\n"
"v_cvt_pk_bf16_f32 v182, v248, v249\n"
"v_mfma_f32_16x16x32_bf16 v[124:127], a[184:187], v[152:155], v[124:127]\n"
"v_exp_f32 v250, v250\n"
"v_mfma_f32_16x16x32_bf16 v[124:127], a[188:191], v[156:159], v[124:127]\n"
"ds_read_b64_tr_b16 a[174:175], v217 offset:28680\n"
"v_mfma_f32_16x16x32_bf16 v[120:123], a[180:183], v[156:159], v[120:123]\n"
"v_exp_f32 v251, v251\n"
"v_mfma_f32_16x16x32_bf16 v[120:123], a[176:179], v[152:155], v[120:123]\n"
"s_waitcnt lgkmcnt(6)\n"
"v_cvt_pk_bf16_f32 v183, v250, v251\n"
"v_mfma_f32_16x16x32_bf16 v[192:195], a[128:131], a[64:67], -4.0\n"
"v_exp_f32 v252, v252\n"
"v_mfma_f32_16x16x32_bf16 v[192:195], a[132:135], a[68:71], v[192:195]\n"
"ds_read_b128 a[144:147], v216 offset:36864\n"
"v_mfma_f32_16x16x32_bf16 v[192:195], a[136:139], a[72:75], v[192:195]\n"
"v_exp_f32 v253, v253\n"
"v_mfma_f32_16x16x32_bf16 v[192:195], a[140:143], a[76:79], v[192:195]\n"
"ds_read_b128 a[148:151], v216 offset:37120\n"
"v_cvt_pk_bf16_f32 v190, v252, v253\n"
"v_mfma_f32_16x16x32_bf16 v[196:199], a[140:143], a[92:95], -4.0\n"
"v_exp_f32 v254, v254\n"
"v_mfma_f32_16x16x32_bf16 v[196:199], a[136:139], a[88:91], v[196:199]\n"
"ds_read_b128 a[152:155], v216 offset:37376\n"
"v_mfma_f32_16x16x32_bf16 v[196:199], a[132:135], a[84:87], v[196:199]\n"
"v_exp_f32 v255, v255\n"
"v_mfma_f32_16x16x32_bf16 v[196:199], a[128:131], a[80:83], v[196:199]\n"
"v_cvt_pk_bf16_f32 v191, v254, v255\n"
"v_mfma_f32_16x16x32_bf16 v[248:251], a[128:131], a[96:99], -4.0\n"
"v_exp_f32 v192, v192\n"
"v_mfma_f32_16x16x32_bf16 v[248:251], a[132:135], a[100:103], v[248:251]\n"
"ds_read_b128 a[156:159], v216 offset:37632\n"
"v_mfma_f32_16x16x32_bf16 v[248:251], a[136:139], a[104:107], v[248:251]\n"
"v_exp_f32 v193, v193\n"
"v_mfma_f32_16x16x32_bf16 v[248:251], a[140:143], a[108:111], v[248:251]\n"
"ds_read_b64_tr_b16 a[176:177], v217 offset:16640\n"
"v_cvt_pk_bf16_f32 v128, v192, v193\n"
"v_mfma_f32_16x16x32_bf16 v[252:255], a[140:143], a[124:127], -4.0\n"
"v_exp_f32 v194, v194\n"
"v_mfma_f32_16x16x32_bf16 v[252:255], a[136:139], a[120:123], v[252:255]\n"
"ds_read_b64_tr_b16 a[178:179], v217 offset:20736\n"
"v_mfma_f32_16x16x32_bf16 v[252:255], a[132:135], a[116:119], v[252:255]\n"
"v_exp_f32 v195, v195\n"
"v_mfma_f32_16x16x32_bf16 v[252:255], a[128:131], a[112:115], v[252:255]\n"
"v_cvt_pk_bf16_f32 v129, v194, v195\n"
"v_smfmac_f32_16x16x64_bf16 a[192:195], a[208:211], v[160:167], v215\n"
"v_smfmac_f32_16x16x64_bf16 a[196:199], a[208:211], v[168:175], v215\n"
"ds_read_b64_tr_b16 a[180:181], v217 offset:24832\n"
"v_smfmac_f32_16x16x64_bf16 a[200:203], a[208:211], v[176:183], v215\n"
"v_smfmac_f32_16x16x64_bf16 a[204:207], a[208:211], v[184:191], v215\n"
"ds_read_b64_tr_b16 a[182:183], v217 offset:28928\n"
"v_mfma_f32_16x16x32_bf16 v[0:3], a[160:163], v[160:163], v[0:3]\n"
"s_waitcnt lgkmcnt(7)\n"
"v_exp_f32 v196, v196\n"
"v_mfma_f32_16x16x32_bf16 v[0:3], a[164:167], v[164:167], v[0:3]\n"
"ds_read_b64_tr_b16 a[184:185], v217 offset:16648\n"
"v_mfma_f32_16x16x32_bf16 v[4:7], a[172:175], v[164:167], v[4:7]\n"
"v_exp_f32 v197, v197\n"
"v_mfma_f32_16x16x32_bf16 v[4:7], a[168:171], v[160:163], v[4:7]\n"
"v_cvt_pk_bf16_f32 v136, v196, v197\n"
"v_mfma_f32_16x16x32_bf16 v[12:15], a[168:171], v[168:171], v[12:15]\n"
"v_exp_f32 v198, v198\n"
"v_mfma_f32_16x16x32_bf16 v[12:15], a[172:175], v[172:175], v[12:15]\n"
"ds_read_b64_tr_b16 a[186:187], v217 offset:20744\n"
"v_mfma_f32_16x16x32_bf16 v[8:11], a[164:167], v[172:175], v[8:11]\n"
"v_exp_f32 v199, v199\n"
"v_mfma_f32_16x16x32_bf16 v[8:11], a[160:163], v[168:171], v[8:11]\n"
"ds_read_b64_tr_b16 a[188:189], v217 offset:24840\n"
"v_cvt_pk_bf16_f32 v137, v198, v199\n"
"v_mfma_f32_16x16x32_bf16 v[16:19], a[160:163], v[176:179], v[16:19]\n"
"v_exp_f32 v248, v248\n"
"v_mfma_f32_16x16x32_bf16 v[16:19], a[164:167], v[180:183], v[16:19]\n"
"ds_read_b64_tr_b16 a[190:191], v217 offset:28936\n"
"v_mfma_f32_16x16x32_bf16 v[20:23], a[172:175], v[180:183], v[20:23]\n"
"v_exp_f32 v249, v249\n"
"v_mfma_f32_16x16x32_bf16 v[20:23], a[168:171], v[176:179], v[20:23]\n"
"v_cvt_pk_bf16_f32 v144, v248, v249\n"
"v_mfma_f32_16x16x32_bf16 v[28:31], a[168:171], v[184:187], v[28:31]\n"
"v_exp_f32 v250, v250\n"
"v_mfma_f32_16x16x32_bf16 v[28:31], a[172:175], v[188:191], v[28:31]\n"
"v_mfma_f32_16x16x32_bf16 v[24:27], a[164:167], v[188:191], v[24:27]\n"
"v_exp_f32 v251, v251\n"
"v_mfma_f32_16x16x32_bf16 v[24:27], a[160:163], v[184:187], v[24:27]\n"
"v_cvt_pk_bf16_f32 v145, v250, v251\n"
"v_mfma_f32_16x16x32_bf16 v[192:195], a[144:147], a[64:67], -4.0\n"
"s_waitcnt lgkmcnt(5)\n"
"v_exp_f32 v252, v252\n"
"v_mfma_f32_16x16x32_bf16 v[192:195], a[148:151], a[68:71], v[192:195]\n"
"ds_read_b128 a[128:131], v216 offset:40960\n"
"v_mfma_f32_16x16x32_bf16 v[192:195], a[152:155], a[72:75], v[192:195]\n"
"s_mov_b32 s93, s63\n"
"v_exp_f32 v253, v253\n"
"v_mfma_f32_16x16x32_bf16 v[192:195], a[156:159], a[76:79], v[192:195]\n"
"ds_read_b128 a[132:135], v216 offset:41216\n"
"v_cvt_pk_bf16_f32 v152, v252, v253\n"
"v_mfma_f32_16x16x32_bf16 v[196:199], a[156:159], a[92:95], -4.0\n"
"s_mul_i32 s74, s60, 43691\n"
"v_exp_f32 v254, v254\n"
"v_mfma_f32_16x16x32_bf16 v[196:199], a[152:155], a[88:91], v[196:199]\n"
"ds_read_b128 a[136:139], v216 offset:41472\n"
"v_mfma_f32_16x16x32_bf16 v[196:199], a[148:151], a[84:87], v[196:199]\n"
"v_exp_f32 v255, v255\n"
"v_mfma_f32_16x16x32_bf16 v[196:199], a[144:147], a[80:83], v[196:199]\n"
"s_lshr_b32 s74, s74, 17\n"
"v_cvt_pk_bf16_f32 v153, v254, v255\n"
"v_mfma_f32_16x16x32_bf16 v[248:251], a[144:147], a[96:99], -4.0\n"
"v_exp_f32 v192, v192\n"
"v_mfma_f32_16x16x32_bf16 v[248:251], a[148:151], a[100:103], v[248:251]\n"
"s_add_u32 s74, s74, s76\n"
"ds_read_b128 a[140:143], v216 offset:41728\n"
"v_mfma_f32_16x16x32_bf16 v[248:251], a[152:155], a[104:107], v[248:251]\n"
"v_exp_f32 v193, v193\n"
"v_mfma_f32_16x16x32_bf16 v[248:251], a[156:159], a[108:111], v[248:251]\n"
"s_lshl_b32 s74, s74, 17\n"
"ds_read_b64_tr_b16 a[160:161], v217 offset:16896\n"
"v_cvt_pk_bf16_f32 v130, v192, v193\n"
"v_mfma_f32_16x16x32_bf16 v[252:255], a[156:159], a[124:127], -4.0\n"
"v_exp_f32 v194, v194\n"
"v_mfma_f32_16x16x32_bf16 v[252:255], a[152:155], a[120:123], v[252:255]\n"
"s_add_u32 s63, s74, s77\n"
"ds_read_b64_tr_b16 a[162:163], v217 offset:20992\n"
"v_mfma_f32_16x16x32_bf16 v[252:255], a[148:151], a[116:119], v[252:255]\n"
"v_exp_f32 v195, v195\n"
"v_mfma_f32_16x16x32_bf16 v[252:255], a[144:147], a[112:115], v[252:255]\n"
"v_cvt_pk_bf16_f32 v131, v194, v195\n"
"v_mfma_f32_16x16x32_bf16 v[32:35], a[176:179], v[160:163], v[32:35]\n"
"s_waitcnt lgkmcnt(6)\n"
"v_exp_f32 v196, v196\n"
"v_mfma_f32_16x16x32_bf16 v[32:35], a[180:183], v[164:167], v[32:35]\n"
"ds_read_b64_tr_b16 a[164:165], v217 offset:25088\n"
"v_mfma_f32_16x16x32_bf16 v[36:39], a[188:191], v[164:167], v[36:39]\n"
"v_exp_f32 v197, v197\n"
"v_mfma_f32_16x16x32_bf16 v[36:39], a[184:187], v[160:163], v[36:39]\n"
"ds_read_b64_tr_b16 a[166:167], v217 offset:29184\n"
"v_cvt_pk_bf16_f32 v138, v196, v197\n"
"v_mfma_f32_16x16x32_bf16 v[44:47], a[184:187], v[168:171], v[44:47]\n"
"v_exp_f32 v198, v198\n"
"v_mfma_f32_16x16x32_bf16 v[44:47], a[188:191], v[172:175], v[44:47]\n"
"ds_read_b64_tr_b16 a[168:169], v217 offset:16904\n"
"v_mfma_f32_16x16x32_bf16 v[40:43], a[180:183], v[172:175], v[40:43]\n"
"v_exp_f32 v199, v199\n"
"v_mfma_f32_16x16x32_bf16 v[40:43], a[176:179], v[168:171], v[40:43]\n"
"v_cvt_pk_bf16_f32 v139, v198, v199\n"
"v_mfma_f32_16x16x32_bf16 v[48:51], a[176:179], v[176:179], v[48:51]\n"
"v_exp_f32 v248, v248\n"
"v_mfma_f32_16x16x32_bf16 v[48:51], a[180:183], v[180:183], v[48:51]\n"
"ds_read_b64_tr_b16 a[170:171], v217 offset:21000\n"
"v_mfma_f32_16x16x32_bf16 v[52:55], a[188:191], v[180:183], v[52:55]\n"
"v_exp_f32 v249, v249\n"
"v_mfma_f32_16x16x32_bf16 v[52:55], a[184:187], v[176:179], v[52:55]\n"
"ds_read_b64_tr_b16 a[172:173], v217 offset:25096\n"
"v_cvt_pk_bf16_f32 v146, v248, v249\n"
"v_mfma_f32_16x16x32_bf16 v[60:63], a[184:187], v[184:187], v[60:63]\n"
"v_exp_f32 v250, v250\n"
"v_mfma_f32_16x16x32_bf16 v[60:63], a[188:191], v[188:191], v[60:63]\n"
"ds_read_b64_tr_b16 a[174:175], v217 offset:29192\n"
"v_mfma_f32_16x16x32_bf16 v[56:59], a[180:183], v[188:191], v[56:59]\n"
"v_exp_f32 v251, v251\n"
"v_mfma_f32_16x16x32_bf16 v[56:59], a[176:179], v[184:187], v[56:59]\n"
"s_waitcnt lgkmcnt(6)\n"
"v_cvt_pk_bf16_f32 v147, v250, v251\n"
"v_mfma_f32_16x16x32_bf16 v[192:195], a[128:131], a[64:67], -4.0\n"
"v_exp_f32 v252, v252\n"
"v_mfma_f32_16x16x32_bf16 v[192:195], a[132:135], a[68:71], v[192:195]\n"
"ds_read_b128 a[144:147], v216 offset:45056\n"
"v_mfma_f32_16x16x32_bf16 v[192:195], a[136:139], a[72:75], v[192:195]\n"
"v_exp_f32 v253, v253\n"
"v_mfma_f32_16x16x32_bf16 v[192:195], a[140:143], a[76:79], v[192:195]\n"
"ds_read_b128 a[148:151], v216 offset:45312\n"
"v_cvt_pk_bf16_f32 v154, v252, v253\n"
"v_mfma_f32_16x16x32_bf16 v[196:199], a[140:143], a[92:95], -4.0\n"
"v_exp_f32 v254, v254\n"
"v_mfma_f32_16x16x32_bf16 v[196:199], a[136:139], a[88:91], v[196:199]\n"
"ds_read_b128 a[152:155], v216 offset:45568\n"
"v_mfma_f32_16x16x32_bf16 v[196:199], a[132:135], a[84:87], v[196:199]\n"
"v_exp_f32 v255, v255\n"
"v_mfma_f32_16x16x32_bf16 v[196:199], a[128:131], a[80:83], v[196:199]\n"
"v_cvt_pk_bf16_f32 v155, v254, v255\n"
"v_mfma_f32_16x16x32_bf16 v[248:251], a[128:131], a[96:99], -4.0\n"
"v_exp_f32 v192, v192\n"
"v_mfma_f32_16x16x32_bf16 v[248:251], a[132:135], a[100:103], v[248:251]\n"
"ds_read_b128 a[156:159], v216 offset:45824\n"
"v_mfma_f32_16x16x32_bf16 v[248:251], a[136:139], a[104:107], v[248:251]\n"
"v_exp_f32 v193, v193\n"
"v_mfma_f32_16x16x32_bf16 v[248:251], a[140:143], a[108:111], v[248:251]\n"
"ds_read_b64_tr_b16 a[176:177], v217 offset:17152\n"
"v_cvt_pk_bf16_f32 v132, v192, v193\n"
"v_mfma_f32_16x16x32_bf16 v[252:255], a[140:143], a[124:127], -4.0\n"
"v_exp_f32 v194, v194\n"
"v_mfma_f32_16x16x32_bf16 v[252:255], a[136:139], a[120:123], v[252:255]\n"
"ds_read_b64_tr_b16 a[178:179], v217 offset:21248\n"
"v_mfma_f32_16x16x32_bf16 v[252:255], a[132:135], a[116:119], v[252:255]\n"
"v_exp_f32 v195, v195\n"
"v_mfma_f32_16x16x32_bf16 v[252:255], a[128:131], a[112:115], v[252:255]\n"
"v_cvt_pk_bf16_f32 v133, v194, v195\n"
"v_mfma_f32_16x16x32_bf16 v[64:67], a[160:163], v[160:163], v[64:67]\n"
"s_waitcnt lgkmcnt(6)\n"
"v_exp_f32 v196, v196\n"
"v_mfma_f32_16x16x32_bf16 v[64:67], a[164:167], v[164:167], v[64:67]\n"
"ds_read_b64_tr_b16 a[180:181], v217 offset:25344\n"
"v_mfma_f32_16x16x32_bf16 v[68:71], a[172:175], v[164:167], v[68:71]\n"
"v_exp_f32 v197, v197\n"
"v_mfma_f32_16x16x32_bf16 v[68:71], a[168:171], v[160:163], v[68:71]\n"
"ds_read_b64_tr_b16 a[182:183], v217 offset:29440\n"
"v_cvt_pk_bf16_f32 v140, v196, v197\n"
"v_mfma_f32_16x16x32_bf16 v[76:79], a[168:171], v[168:171], v[76:79]\n"
"v_exp_f32 v198, v198\n"
"v_mfma_f32_16x16x32_bf16 v[76:79], a[172:175], v[172:175], v[76:79]\n"
"ds_read_b64_tr_b16 a[184:185], v217 offset:17160\n"
"v_mfma_f32_16x16x32_bf16 v[72:75], a[164:167], v[172:175], v[72:75]\n"
"v_exp_f32 v199, v199\n"
"v_mfma_f32_16x16x32_bf16 v[72:75], a[160:163], v[168:171], v[72:75]\n"
"v_cvt_pk_bf16_f32 v141, v198, v199\n"
"v_mfma_f32_16x16x32_bf16 v[80:83], a[160:163], v[176:179], v[80:83]\n"
"v_exp_f32 v248, v248\n"
"v_mfma_f32_16x16x32_bf16 v[80:83], a[164:167], v[180:183], v[80:83]\n"
"ds_read_b64_tr_b16 a[186:187], v217 offset:21256\n"
"v_mfma_f32_16x16x32_bf16 v[84:87], a[172:175], v[180:183], v[84:87]\n"
"v_exp_f32 v249, v249\n"
"v_mfma_f32_16x16x32_bf16 v[84:87], a[168:171], v[176:179], v[84:87]\n"
"ds_read_b64_tr_b16 a[188:189], v217 offset:25352\n"
"v_cvt_pk_bf16_f32 v148, v248, v249\n"
"v_mfma_f32_16x16x32_bf16 v[92:95], a[168:171], v[184:187], v[92:95]\n"
"v_exp_f32 v250, v250\n"
"v_mfma_f32_16x16x32_bf16 v[92:95], a[172:175], v[188:191], v[92:95]\n"
"ds_read_b64_tr_b16 a[190:191], v217 offset:29448\n"
"v_mfma_f32_16x16x32_bf16 v[88:91], a[164:167], v[188:191], v[88:91]\n"
"v_exp_f32 v251, v251\n"
"v_mfma_f32_16x16x32_bf16 v[88:91], a[160:163], v[184:187], v[88:91]\n"
"s_waitcnt vmcnt(1) lgkmcnt(6)\n"
"v_cvt_pk_bf16_f32 v149, v250, v251\n"
"v_mfma_f32_16x16x32_bf16 v[192:195], a[144:147], a[64:67], -4.0\n"
"s_barrier\n"
"v_exp_f32 v252, v252\n"
"v_mfma_f32_16x16x32_bf16 v[192:195], a[148:151], a[68:71], v[192:195]\n"
".Lattn_pad14:\n"
"s_cbranch_vccnz .Lattn_ret\n"
"ds_read_b128 a[128:131], v216 offset:49152\n"
"v_mfma_f32_16x16x32_bf16 v[192:195], a[152:155], a[72:75], v[192:195]\n"
"v_exp_f32 v253, v253\n"
"v_mfma_f32_16x16x32_bf16 v[192:195], a[156:159], a[76:79], v[192:195]\n"
"ds_read_b128 a[132:135], v216 offset:49408\n"
"v_cvt_pk_bf16_f32 v156, v252, v253\n"
"v_mfma_f32_16x16x32_bf16 v[196:199], a[156:159], a[92:95], -4.0\n"
"v_exp_f32 v254, v254\n"
"v_mfma_f32_16x16x32_bf16 v[196:199], a[152:155], a[88:91], v[196:199]\n"
"ds_read_b128 a[136:139], v216 offset:49664\n"
"v_mfma_f32_16x16x32_bf16 v[196:199], a[148:151], a[84:87], v[196:199]\n"
"v_exp_f32 v255, v255\n"
"v_mfma_f32_16x16x32_bf16 v[196:199], a[144:147], a[80:83], v[196:199]\n"
"v_cvt_pk_bf16_f32 v157, v254, v255\n"
"v_mfma_f32_16x16x32_bf16 v[248:251], a[144:147], a[96:99], -4.0\n"
"v_exp_f32 v192, v192\n"
"v_mfma_f32_16x16x32_bf16 v[248:251], a[148:151], a[100:103], v[248:251]\n"
"ds_read_b128 a[140:143], v216 offset:49920\n"
"v_mfma_f32_16x16x32_bf16 v[248:251], a[152:155], a[104:107], v[248:251]\n"
"v_exp_f32 v193, v193\n"
"v_mfma_f32_16x16x32_bf16 v[248:251], a[156:159], a[108:111], v[248:251]\n"
"ds_read_b64_tr_b16 a[160:161], v217 offset:32768\n"
"v_cvt_pk_bf16_f32 v134, v192, v193\n"
"v_mfma_f32_16x16x32_bf16 v[252:255], a[156:159], a[124:127], -4.0\n"
"v_exp_f32 v194, v194\n"
"v_mfma_f32_16x16x32_bf16 v[252:255], a[152:155], a[120:123], v[252:255]\n"
"ds_read_b64_tr_b16 a[162:163], v217 offset:36864\n"
"v_mfma_f32_16x16x32_bf16 v[252:255], a[148:151], a[116:119], v[252:255]\n"
"v_exp_f32 v195, v195\n"
"v_mfma_f32_16x16x32_bf16 v[252:255], a[144:147], a[112:115], v[252:255]\n"
"v_cvt_pk_bf16_f32 v135, v194, v195\n"
"v_mfma_f32_16x16x32_bf16 v[96:99], a[176:179], v[160:163], v[96:99]\n"
"s_waitcnt lgkmcnt(6)\n"
"v_exp_f32 v196, v196\n"
"v_mfma_f32_16x16x32_bf16 v[96:99], a[180:183], v[164:167], v[96:99]\n"
"ds_read_b64_tr_b16 a[164:165], v217 offset:40960\n"
"v_mfma_f32_16x16x32_bf16 v[100:103], a[188:191], v[164:167], v[100:103]\n"
"v_exp_f32 v197, v197\n"
"v_mfma_f32_16x16x32_bf16 v[100:103], a[184:187], v[160:163], v[100:103]\n"
"ds_read_b64_tr_b16 a[166:167], v217 offset:45056\n"
"v_cvt_pk_bf16_f32 v142, v196, v197\n"
"v_mfma_f32_16x16x32_bf16 v[108:111], a[184:187], v[168:171], v[108:111]\n"
"v_exp_f32 v198, v198\n"
"v_mfma_f32_16x16x32_bf16 v[108:111], a[188:191], v[172:175], v[108:111]\n"
"ds_read_b64_tr_b16 a[168:169], v217 offset:32776\n"
"v_mfma_f32_16x16x32_bf16 v[104:107], a[180:183], v[172:175], v[104:107]\n"
"v_exp_f32 v199, v199\n"
"v_mfma_f32_16x16x32_bf16 v[104:107], a[176:179], v[168:171], v[104:107]\n"
"v_cvt_pk_bf16_f32 v143, v198, v199\n"
"v_mfma_f32_16x16x32_bf16 v[112:115], a[176:179], v[176:179], v[112:115]\n"
"v_exp_f32 v248, v248\n"
"v_mfma_f32_16x16x32_bf16 v[112:115], a[180:183], v[180:183], v[112:115]\n"
"ds_read_b64_tr_b16 a[170:171], v217 offset:36872\n"
"v_mfma_f32_16x16x32_bf16 v[116:119], a[188:191], v[180:183], v[116:119]\n"
"v_exp_f32 v249, v249\n"
"v_mfma_f32_16x16x32_bf16 v[116:119], a[184:187], v[176:179], v[116:119]\n"
"ds_read_b64_tr_b16 a[172:173], v217 offset:40968\n"
"v_cvt_pk_bf16_f32 v150, v248, v249\n"
"v_mfma_f32_16x16x32_bf16 v[124:127], a[184:187], v[184:187], v[124:127]\n"
"v_exp_f32 v250, v250\n"
"v_mfma_f32_16x16x32_bf16 v[124:127], a[188:191], v[188:191], v[124:127]\n"
"ds_read_b64_tr_b16 a[174:175], v217 offset:45064\n"
"v_mfma_f32_16x16x32_bf16 v[120:123], a[180:183], v[188:191], v[120:123]\n"
"v_exp_f32 v251, v251\n"
"v_mfma_f32_16x16x32_bf16 v[120:123], a[176:179], v[184:187], v[120:123]\n"
"v_cvt_pk_bf16_f32 v151, v250, v251\n"
"s_waitcnt lgkmcnt(6)\n"
"v_mfma_f32_16x16x32_bf16 v[192:195], a[128:131], a[64:67], -4.0\n"
"v_exp_f32 v252, v252\n"
"v_mfma_f32_16x16x32_bf16 v[192:195], a[132:135], a[68:71], v[192:195]\n"
"v_exp_f32 v253, v253\n"
"v_mfma_f32_16x16x32_bf16 v[192:195], a[136:139], a[72:75], v[192:195]\n"
"ds_read_b128 a[0:3], v216 offset:53248\n"
"v_cvt_pk_bf16_f32 v158, v252, v253\n"
"v_mfma_f32_16x16x32_bf16 v[192:195], a[140:143], a[76:79], v[192:195]\n"
"ds_read_b128 a[4:7], v216 offset:53504\n"
"ds_read_b128 a[8:11], v216 offset:53760\n"
"v_mfma_f32_16x16x32_bf16 v[196:199], a[140:143], a[92:95], -4.0\n"
"v_exp_f32 v254, v254\n"
"v_mfma_f32_16x16x32_bf16 v[196:199], a[136:139], a[88:91], v[196:199]\n"
"v_exp_f32 v255, v255\n"
"v_mfma_f32_16x16x32_bf16 v[196:199], a[132:135], a[84:87], v[196:199]\n"
"ds_read_b128 a[12:15], v216 offset:54016\n"
"v_cvt_pk_bf16_f32 v159, v254, v255\n"
"v_mfma_f32_16x16x32_bf16 v[196:199], a[128:131], a[80:83], v[196:199]\n"
"ds_read_b64_tr_b16 a[176:177], v217 offset:33024\n"
"ds_read_b64_tr_b16 a[178:179], v217 offset:37120\n"
"v_smfmac_f32_16x16x64_bf16 a[192:195], a[208:211], v[128:135], v215\n"
"s_add_u32 s56, s46, s98\n"
"ds_read_b64_tr_b16 a[180:181], v217 offset:41216\n"
"ds_read_b64_tr_b16 a[182:183], v217 offset:45312\n"
"v_smfmac_f32_16x16x64_bf16 a[196:199], a[208:211], v[136:143], v215\n"
"s_addc_u32 s57, s47, 0\n"
"ds_read_b64_tr_b16 a[184:185], v217 offset:33032\n"
"ds_read_b64_tr_b16 a[186:187], v217 offset:37128\n"
"v_mfma_f32_16x16x32_bf16 v[0:3], a[160:163], v[128:131], v[0:3]\n"
"s_mov_b32 s58, 16384\n"
"v_exp_f32 v192, v192\n"
"s_waitcnt lgkmcnt(10)\n"
"v_mfma_f32_16x16x32_bf16 v[0:3], a[164:167], v[132:135], v[0:3]\n"
"ds_read_b64_tr_b16 a[188:189], v217 offset:41224\n"
"v_mfma_f32_16x16x32_bf16 v[4:7], a[172:175], v[132:135], v[4:7]\n"
"v_exp_f32 v193, v193\n"
"v_mfma_f32_16x16x32_bf16 v[4:7], a[168:171], v[128:131], v[4:7]\n"
"ds_read_b64_tr_b16 a[190:191], v217 offset:45320\n"
"ds_read_b128 a[16:19], v216 offset:57344\n"
"v_cvt_pk_bf16_f32 v160, v192, v193\n"
"v_mfma_f32_16x16x32_bf16 v[12:15], a[168:171], v[136:139], v[12:15]\n"
"v_exp_f32 v194, v194\n"
"v_mfma_f32_16x16x32_bf16 v[12:15], a[172:175], v[140:143], v[12:15]\n"
"ds_read_b128 a[20:23], v216 offset:57600\n"
"ds_read_b128 a[24:27], v216 offset:57856\n"
"v_mfma_f32_16x16x32_bf16 v[8:11], a[164:167], v[140:143], v[8:11]\n"
"v_exp_f32 v195, v195\n"
"v_mfma_f32_16x16x32_bf16 v[8:11], a[160:163], v[136:139], v[8:11]\n"
"ds_read_b128 a[28:31], v216 offset:58112\n"
"ds_read_b64_tr_b16 a[48:49], v217 offset:33280\n"
"v_cvt_pk_bf16_f32 v161, v194, v195\n"
"s_waitcnt lgkmcnt(12)\n"
"v_mfma_f32_16x16x32_bf16 v[248:251], a[0:3], a[64:67], -4.0\n"
"v_exp_f32 v196, v196\n"
"v_mfma_f32_16x16x32_bf16 v[248:251], a[4:7], a[68:71], v[248:251]\n"
"v_exp_f32 v197, v197\n"
"v_mfma_f32_16x16x32_bf16 v[248:251], a[8:11], a[72:75], v[248:251]\n"
"ds_read_b64_tr_b16 a[50:51], v217 offset:37376\n"
"v_cvt_pk_bf16_f32 v168, v196, v197\n"
"v_mfma_f32_16x16x32_bf16 v[248:251], a[12:15], a[76:79], v[248:251]\n"
"ds_read_b64_tr_b16 a[52:53], v217 offset:41472\n"
"ds_read_b64_tr_b16 a[54:55], v217 offset:45568\n"
"v_mfma_f32_16x16x32_bf16 v[252:255], a[12:15], a[92:95], -4.0\n"
"v_exp_f32 v198, v198\n"
"v_mfma_f32_16x16x32_bf16 v[252:255], a[8:11], a[88:91], v[252:255]\n"
"v_exp_f32 v199, v199\n"
"v_mfma_f32_16x16x32_bf16 v[252:255], a[4:7], a[84:87], v[252:255]\n"
"ds_read_b64_tr_b16 a[56:57], v217 offset:33288\n"
"v_cvt_pk_bf16_f32 v169, v198, v199\n"
"v_mfma_f32_16x16x32_bf16 v[252:255], a[0:3], a[80:83], v[252:255]\n"
"ds_read_b64_tr_b16 a[58:59], v217 offset:37384\n"
"ds_read_b64_tr_b16 a[60:61], v217 offset:41480\n"
"s_waitcnt lgkmcnt(10)\n"
"v_mfma_f32_16x16x32_bf16 v[32:35], a[176:179], v[128:131], v[32:35]\n"
"v_exp_f32 v248, v248\n"
"v_mfma_f32_16x16x32_bf16 v[32:35], a[180:183], v[132:135], v[32:35]\n"
"ds_read_b64_tr_b16 a[62:63], v217 offset:45576\n"
"v_mfma_f32_16x16x32_bf16 v[36:39], a[188:191], v[132:135], v[36:39]\n"
"v_exp_f32 v249, v249\n"
"v_mfma_f32_16x16x32_bf16 v[36:39], a[184:187], v[128:131], v[36:39]\n"
"ds_read_b128 a[32:35], v216 offset:61440\n"
"v_cvt_pk_bf16_f32 v162, v248, v249\n"
"v_mfma_f32_16x16x32_bf16 v[44:47], a[184:187], v[136:139], v[44:47]\n"
"v_exp_f32 v250, v250\n"
"v_mfma_f32_16x16x32_bf16 v[44:47], a[188:191], v[140:143], v[44:47]\n"
"ds_read_b128 a[36:39], v216 offset:61696\n"
"ds_read_b128 a[40:43], v216 offset:61952\n"
"v_mfma_f32_16x16x32_bf16 v[40:43], a[180:183], v[140:143], v[40:43]\n"
"v_exp_f32 v251, v251\n"
"v_mfma_f32_16x16x32_bf16 v[40:43], a[176:179], v[136:139], v[40:43]\n"
"ds_read_b128 a[44:47], v216 offset:62208\n"
"ds_read_b64_tr_b16 a[144:145], v217 offset:33536\n"
"v_cvt_pk_bf16_f32 v163, v250, v251\n"
"v_mfma_f32_16x16x32_bf16 v[192:195], a[16:19], a[64:67], -4.0\n"
"v_exp_f32 v252, v252\n"
"s_waitcnt lgkmcnt(9)\n"
"v_mfma_f32_16x16x32_bf16 v[192:195], a[20:23], a[68:71], v[192:195]\n"
"v_exp_f32 v253, v253\n"
"v_mfma_f32_16x16x32_bf16 v[192:195], a[24:27], a[72:75], v[192:195]\n"
"ds_read_b64_tr_b16 a[146:147], v217 offset:37632\n"
"v_cvt_pk_bf16_f32 v170, v252, v253\n"
"v_mfma_f32_16x16x32_bf16 v[192:195], a[28:31], a[76:79], v[192:195]\n"
"ds_read_b64_tr_b16 a[148:149], v217 offset:41728\n"
"v_mfma_f32_16x16x32_bf16 v[196:199], a[28:31], a[92:95], -4.0\n"
"v_exp_f32 v254, v254\n"
"v_mfma_f32_16x16x32_bf16 v[196:199], a[24:27], a[88:91], v[196:199]\n"
"v_exp_f32 v255, v255\n"
"v_mfma_f32_16x16x32_bf16 v[196:199], a[20:23], a[84:87], v[196:199]\n"
"ds_read_b64_tr_b16 a[150:151], v217 offset:45824\n"
"v_cvt_pk_bf16_f32 v171, v254, v255\n"
"v_mfma_f32_16x16x32_bf16 v[196:199], a[16:19], a[80:83], v[196:199]\n"
"ds_read_b64_tr_b16 a[152:153], v217 offset:33544\n"
"v_mfma_f32_16x16x32_bf16 v[64:67], a[48:51], v[128:131], v[64:67]\n"
"v_exp_f32 v192, v192\n"
"v_mfma_f32_16x16x32_bf16 v[64:67], a[52:55], v[132:135], v[64:67]\n"
"ds_read_b64_tr_b16 a[154:155], v217 offset:37640\n"
"s_waitcnt lgkmcnt(9)\n"
"v_mfma_f32_16x16x32_bf16 v[68:71], a[60:63], v[132:135], v[68:71]\n"
"s_waitcnt vmcnt(0)\n"
"s_barrier\n"
"v_exp_f32 v193, v193\n"
"v_mfma_f32_16x16x32_bf16 v[68:71], a[56:59], v[128:131], v[68:71]\n"
"ds_read_b64_tr_b16 a[156:157], v217 offset:41736\n"
"v_cvt_pk_bf16_f32 v164, v192, v193\n"
"v_mfma_f32_16x16x32_bf16 v[76:79], a[56:59], v[136:139], v[76:79]\n"
"v_exp_f32 v194, v194\n"
"v_mfma_f32_16x16x32_bf16 v[76:79], a[60:63], v[140:143], v[76:79]\n"
"ds_read_b64_tr_b16 a[158:159], v217 offset:45832\n"
"v_mfma_f32_16x16x32_bf16 v[72:75], a[52:55], v[140:143], v[72:75]\n"
"v_exp_f32 v195, v195\n"
"v_mfma_f32_16x16x32_bf16 v[72:75], a[48:51], v[136:139], v[72:75]\n"
"ds_read_b64_tr_b16 a[216:217], v217 offset:49152\n"
"v_cvt_pk_bf16_f32 v165, v194, v195\n"
"v_mfma_f32_16x16x32_bf16 v[248:251], a[32:35], a[64:67], -4.0\n"
"v_exp_f32 v196, v196\n"
"s_waitcnt lgkmcnt(7)\n"
"v_mfma_f32_16x16x32_bf16 v[248:251], a[36:39], a[68:71], v[248:251]\n"
"v_exp_f32 v197, v197\n"
"v_mfma_f32_16x16x32_bf16 v[248:251], a[40:43], a[72:75], v[248:251]\n"
"ds_read_b64_tr_b16 a[218:219], v217 offset:53248\n"
"v_cvt_pk_bf16_f32 v172, v196, v197\n"
"v_mfma_f32_16x16x32_bf16 v[248:251], a[44:47], a[76:79], v[248:251]\n"
"ds_read_b64_tr_b16 a[220:221], v217 offset:57344\n"
"ds_read_b64_tr_b16 a[222:223], v217 offset:61440\n"
"v_mfma_f32_16x16x32_bf16 v[252:255], a[44:47], a[92:95], -4.0\n"
"v_exp_f32 v198, v198\n"
"v_mfma_f32_16x16x32_bf16 v[252:255], a[40:43], a[88:91], v[252:255]\n"
"v_exp_f32 v199, v199\n"
"v_mfma_f32_16x16x32_bf16 v[252:255], a[36:39], a[84:87], v[252:255]\n"
"ds_read_b64_tr_b16 a[224:225], v217 offset:49160\n"
"v_cvt_pk_bf16_f32 v173, v198, v199\n"
"v_mfma_f32_16x16x32_bf16 v[252:255], a[32:35], a[80:83], v[252:255]\n"
"ds_read_b64_tr_b16 a[226:227], v217 offset:53256\n"
"ds_read_b64_tr_b16 a[228:229], v217 offset:57352\n"
"v_mfma_f32_16x16x32_bf16 v[96:99], a[144:147], v[128:131], v[96:99]\n"
"v_exp_f32 v248, v248\n"
"s_waitcnt lgkmcnt(8)\n"
"v_mfma_f32_16x16x32_bf16 v[96:99], a[148:151], v[132:135], v[96:99]\n"
"ds_read_b64_tr_b16 a[230:231], v217 offset:61448\n"
"ds_read_b64_tr_b16 a[232:233], v217 offset:49408\n"
"s_waitcnt lgkmcnt(9)\n"
"v_mfma_f32_16x16x32_bf16 v[100:103], a[156:159], v[132:135], v[100:103]\n"
"v_exp_f32 v249, v249\n"
"v_mfma_f32_16x16x32_bf16 v[100:103], a[152:155], v[128:131], v[100:103]\n"
"ds_read_b64_tr_b16 a[234:235], v217 offset:53504\n"
"ds_read_b64_tr_b16 a[236:237], v217 offset:57600\n"
"v_cvt_pk_bf16_f32 v166, v248, v249\n"
"v_mfma_f32_16x16x32_bf16 v[108:111], a[152:155], v[136:139], v[108:111]\n"
"v_exp_f32 v250, v250\n"
"v_mfma_f32_16x16x32_bf16 v[108:111], a[156:159], v[140:143], v[108:111]\n"
"ds_read_b64_tr_b16 a[238:239], v217 offset:61696\n"
"v_mfma_f32_16x16x32_bf16 v[104:107], a[148:151], v[140:143], v[104:107]\n"
"v_exp_f32 v251, v251\n"
"v_mfma_f32_16x16x32_bf16 v[104:107], a[144:147], v[136:139], v[104:107]\n"
"ds_read_b64_tr_b16 a[240:241], v217 offset:49416\n"
"v_cvt_pk_bf16_f32 v167, v250, v251\n"
"v_smfmac_f32_16x16x64_bf16 a[200:203], a[208:211], v[144:151], v215\n"
"ds_read_b64_tr_b16 a[242:243], v217 offset:53512\n"
"v_smfmac_f32_16x16x64_bf16 a[204:207], a[208:211], v[152:159], v215\n"
"ds_read_b64_tr_b16 a[244:245], v217 offset:57608\n"
"v_mfma_f32_16x16x32_bf16 v[192:195], a[128:131], a[96:99], -4.0\n"
"v_exp_f32 v252, v252\n"
"v_mfma_f32_16x16x32_bf16 v[192:195], a[132:135], a[100:103], v[192:195]\n"
"v_exp_f32 v253, v253\n"
"v_mfma_f32_16x16x32_bf16 v[192:195], a[136:139], a[104:107], v[192:195]\n"
"ds_read_b64_tr_b16 a[246:247], v217 offset:61704\n"
"v_cvt_pk_bf16_f32 v174, v252, v253\n"
"v_mfma_f32_16x16x32_bf16 v[192:195], a[140:143], a[108:111], v[192:195]\n"
"ds_read_b64_tr_b16 a[64:65], v217 offset:49664\n"
"v_mfma_f32_16x16x32_bf16 v[196:199], a[140:143], a[124:127], -4.0\n"
"v_exp_f32 v254, v254\n"
"v_mfma_f32_16x16x32_bf16 v[196:199], a[136:139], a[120:123], v[196:199]\n"
"v_exp_f32 v255, v255\n"
"v_mfma_f32_16x16x32_bf16 v[196:199], a[132:135], a[116:119], v[196:199]\n"
"ds_read_b64_tr_b16 a[66:67], v217 offset:53760\n"
"v_cvt_pk_bf16_f32 v175, v254, v255\n"
"v_mfma_f32_16x16x32_bf16 v[196:199], a[128:131], a[112:115], v[196:199]\n"
"ds_read_b64_tr_b16 a[68:69], v217 offset:57856\n"
"v_smfmac_f32_16x16x64_bf16 a[192:195], a[208:211], v[160:167], v215\n"
"ds_read_b64_tr_b16 a[70:71], v217 offset:61952\n"
"v_smfmac_f32_16x16x64_bf16 a[196:199], a[208:211], v[168:175], v215\n"
"ds_read_b64_tr_b16 a[72:73], v217 offset:49672\n"
"s_waitcnt lgkmcnt(9)\n"
"v_mfma_f32_16x16x32_bf16 v[0:3], a[216:219], v[160:163], v[0:3]\n"
"v_exp_f32 v192, v192\n"
"v_mfma_f32_16x16x32_bf16 v[0:3], a[220:223], v[164:167], v[0:3]\n"
"ds_read_b64_tr_b16 a[74:75], v217 offset:53768\n"
"s_nop 0\n"
"v_accvgpr_read_b32 v208, a192\n"
"v_accvgpr_read_b32 v213, a193\n"
"v_add_f32 v208, v208, v213\n"
"v_mfma_f32_16x16x32_bf16 v[4:7], a[228:231], v[164:167], v[4:7]\n"
"v_exp_f32 v193, v193\n"
"v_mfma_f32_16x16x32_bf16 v[4:7], a[224:227], v[160:163], v[4:7]\n"
"ds_read_b64_tr_b16 a[76:77], v217 offset:57864\n"
"v_cvt_pk_bf16_f32 v176, v192, v193\n"
"v_accvgpr_read_b32 v210, a196\n"
"v_accvgpr_read_b32 v213, a197\n"
"v_add_f32 v210, v210, v213\n"
"v_mfma_f32_16x16x32_bf16 v[32:35], a[232:235], v[160:163], v[32:35]\n"
"v_exp_f32 v194, v194\n"
"v_mfma_f32_16x16x32_bf16 v[32:35], a[236:239], v[164:167], v[32:35]\n"
"ds_read_b64_tr_b16 a[78:79], v217 offset:61960\n"
"v_max3_u32 v209, v209, v208, v210\n"
"v_min3_u32 v211, v211, v208, v210\n"
"v_rcp_f32 v208, v208\n"
"s_waitcnt lgkmcnt(7)\n"
"v_mfma_f32_16x16x32_bf16 v[36:39], a[244:247], v[164:167], v[36:39]\n"
"v_exp_f32 v195, v195\n"
"v_mfma_f32_16x16x32_bf16 v[36:39], a[240:243], v[160:163], v[36:39]\n"
"ds_read_b64_tr_b16 a[80:81], v217 offset:49920\n"
"v_cvt_pk_bf16_f32 v177, v194, v195\n"
"v_mfma_f32_16x16x32_bf16 v[248:251], a[0:3], a[96:99], -4.0\n"
"v_exp_f32 v196, v196\n"
"v_mfma_f32_16x16x32_bf16 v[248:251], a[4:7], a[100:103], v[248:251]\n"
"v_exp_f32 v197, v197\n"
"v_mfma_f32_16x16x32_bf16 v[248:251], a[8:11], a[104:107], v[248:251]\n"
"ds_read_b64_tr_b16 a[82:83], v217 offset:54016\n"
"v_cvt_pk_bf16_f32 v184, v196, v197\n"
"v_mfma_f32_16x16x32_bf16 v[248:251], a[12:15], a[108:111], v[248:251]\n"
"ds_read_b64_tr_b16 a[84:85], v217 offset:58112\n"
"ds_read_b64_tr_b16 a[86:87], v217 offset:62208\n"
"v_rcp_f32 v210, v210\n"
"v_mul_f32 v0, v0, v208\n"
"v_mul_f32 v1, v1, v208\n"
"v_pk_mul_f32 v[2:3], v[2:3], v[208:209] op_sel_hi:[1,0]\n"
"v_pk_mul_f32 v[4:5], v[4:5], v[208:209] op_sel_hi:[1,0]\n"
"v_pk_mul_f32 v[6:7], v[6:7], v[208:209] op_sel_hi:[1,0]\n"
"v_cvt_pk_bf16_f32 v0, v0, v1\n"
"v_cvt_pk_bf16_f32 v1, v2, v3\n"
"v_mfma_f32_16x16x32_bf16 v[252:255], a[12:15], a[124:127], -4.0\n"
"v_exp_f32 v198, v198\n"
"v_mfma_f32_16x16x32_bf16 v[252:255], a[8:11], a[120:123], v[252:255]\n"
"v_exp_f32 v199, v199\n"
"v_mfma_f32_16x16x32_bf16 v[252:255], a[4:7], a[116:119], v[252:255]\n"
"ds_read_b64_tr_b16 a[88:89], v217 offset:49928\n"
"v_cvt_pk_bf16_f32 v185, v198, v199\n"
"v_mfma_f32_16x16x32_bf16 v[252:255], a[0:3], a[112:115], v[252:255]\n"
"ds_read_b64_tr_b16 a[90:91], v217 offset:54024\n"
"ds_read_b64_tr_b16 a[92:93], v217 offset:58120\n"
"v_cvt_pk_bf16_f32 v2, v4, v5\n"
"v_cvt_pk_bf16_f32 v3, v6, v7\n"
"v_mul_f32 v32, v32, v208\n"
"v_mul_f32 v33, v33, v208\n"
"v_pk_mul_f32 v[34:35], v[34:35], v[208:209] op_sel_hi:[1,0]\n"
"v_pk_mul_f32 v[36:37], v[36:37], v[208:209] op_sel_hi:[1,0]\n"
"v_pk_mul_f32 v[38:39], v[38:39], v[208:209] op_sel_hi:[1,0]\n"
"v_cvt_pk_bf16_f32 v32, v32, v33\n"
"v_cvt_pk_bf16_f32 v33, v34, v35\n"
"v_mfma_f32_16x16x32_bf16 v[12:15], a[224:227], v[168:171], v[12:15]\n"
"v_exp_f32 v248, v248\n"
"v_mfma_f32_16x16x32_bf16 v[12:15], a[228:231], v[172:175], v[12:15]\n"
"ds_read_b64_tr_b16 a[94:95], v217 offset:62216\n"
"v_cvt_pk_bf16_f32 v34, v36, v37\n"
"v_cvt_pk_bf16_f32 v35, v38, v39\n"
"v_mfma_f32_16x16x32_bf16 v[8:11], a[220:223], v[172:175], v[8:11]\n"
"v_exp_f32 v249, v249\n"
"v_mfma_f32_16x16x32_bf16 v[8:11], a[216:219], v[168:171], v[8:11]\n"
"ds_write_b128 v224, v[0:3] offset:32768\n"
"v_cvt_pk_bf16_f32 v178, v248, v249\n"
"v_mfma_f32_16x16x32_bf16 v[44:47], a[240:243], v[168:171], v[44:47]\n"
"v_exp_f32 v250, v250\n"
"v_mfma_f32_16x16x32_bf16 v[44:47], a[244:247], v[172:175], v[44:47]\n"
"ds_write_b128 v225, v[32:35] offset:32768\n"
"v_mfma_f32_16x16x32_bf16 v[40:43], a[236:239], v[172:175], v[40:43]\n"
"v_exp_f32 v251, v251\n"
"v_mfma_f32_16x16x32_bf16 v[40:43], a[232:235], v[168:171], v[40:43]\n"
"ds_read_b128 v[200:203], v226 offset:32768\n"
"v_cvt_pk_bf16_f32 v179, v250, v251\n"
"v_mfma_f32_16x16x32_bf16 v[192:195], a[16:19], a[96:99], -4.0\n"
"v_exp_f32 v252, v252\n"
"v_mfma_f32_16x16x32_bf16 v[192:195], a[20:23], a[100:103], v[192:195]\n"
"v_exp_f32 v253, v253\n"
"v_mfma_f32_16x16x32_bf16 v[192:195], a[24:27], a[104:107], v[192:195]\n"
"ds_read_b128 v[204:207], v226 offset:33792\n"
"v_cvt_pk_bf16_f32 v186, v252, v253\n"
"v_mfma_f32_16x16x32_bf16 v[192:195], a[28:31], a[108:111], v[192:195]\n"
"v_exp_f32 v254, v254\n"
"v_mul_f32 v8, v8, v210\n"
"v_mul_f32 v9, v9, v210\n"
"v_mul_f32 v10, v10, v210\n"
"v_mul_f32 v11, v11, v210\n"
"v_pk_mul_f32 v[12:13], v[12:13], v[210:211] op_sel_hi:[1,0]\n"
"v_pk_mul_f32 v[14:15], v[14:15], v[210:211] op_sel_hi:[1,0]\n"
"v_cvt_pk_bf16_f32 v8, v8, v9\n"
"v_cvt_pk_bf16_f32 v9, v10, v11\n"
"v_mfma_f32_16x16x32_bf16 v[196:199], a[28:31], a[124:127], -4.0\n"
"v_exp_f32 v255, v255\n"
"v_mfma_f32_16x16x32_bf16 v[196:199], a[24:27], a[120:123], v[196:199]\n"
"v_cvt_pk_bf16_f32 v187, v254, v255\n"
"v_cvt_pk_bf16_f32 v10, v12, v13\n"
"v_mfma_f32_16x16x32_bf16 v[196:199], a[20:23], a[116:119], v[196:199]\n"
"v_cvt_pk_bf16_f32 v11, v14, v15\n"
"v_mul_f32 v40, v40, v210\n"
"v_mfma_f32_16x16x32_bf16 v[196:199], a[16:19], a[112:115], v[196:199]\n"
"s_waitcnt lgkmcnt(1)\n"
"buffer_store_dwordx4 v[200:203], v223, s[56:59], s99 offen offset:0 sc1 nt\n"
"v_exp_f32 v192, v192\n"
"v_mul_f32 v41, v41, v210\n"
"v_pk_mul_f32 v[42:43], v[42:43], v[210:211] op_sel_hi:[1,0]\n"
"v_pk_mul_f32 v[44:45], v[44:45], v[210:211] op_sel_hi:[1,0]\n"
"v_pk_mul_f32 v[46:47], v[46:47], v[210:211] op_sel_hi:[1,0]\n"
"v_cvt_pk_bf16_f32 v40, v40, v41\n"
"v_cvt_pk_bf16_f32 v41, v42, v43\n"
"v_mfma_f32_16x16x32_bf16 v[64:67], a[64:67], v[160:163], v[64:67]\n"
"v_exp_f32 v193, v193\n"
"v_mfma_f32_16x16x32_bf16 v[64:67], a[68:71], v[164:167], v[64:67]\n"
"v_cvt_pk_bf16_f32 v180, v192, v193\n"
"v_cvt_pk_bf16_f32 v42, v44, v45\n"
"v_cvt_pk_bf16_f32 v43, v46, v47\n"
"v_mfma_f32_16x16x32_bf16 v[68:71], a[76:79], v[164:167], v[68:71]\n"
"v_exp_f32 v194, v194\n"
"v_mfma_f32_16x16x32_bf16 v[68:71], a[72:75], v[160:163], v[68:71]\n"
"s_waitcnt lgkmcnt(0)\n"
"buffer_store_dwordx4 v[204:207], v223, s[56:59], s99 offen offset:2048 sc1 nt\n"
"v_exp_f32 v195, v195\n"
"s_nop 0\n"
"v_cvt_pk_bf16_f32 v181, v194, v195\n"
"v_mfma_f32_16x16x32_bf16 v[96:99], a[80:83], v[160:163], v[96:99]\n"
"v_exp_f32 v196, v196\n"
"v_mfma_f32_16x16x32_bf16 v[96:99], a[84:87], v[164:167], v[96:99]\n"
"ds_write_b128 v224, v[8:11] offset:34816\n"
"v_mfma_f32_16x16x32_bf16 v[100:103], a[92:95], v[164:167], v[100:103]\n"
"v_exp_f32 v197, v197\n"
"v_mfma_f32_16x16x32_bf16 v[100:103], a[88:91], v[160:163], v[100:103]\n"
"ds_write_b128 v225, v[40:43] offset:34816\n"
"v_cvt_pk_bf16_f32 v188, v196, v197\n"
"v_pk_mul_f32 v[64:65], v[64:65], v[208:209] op_sel_hi:[1,0]\n"
"v_pk_mul_f32 v[66:67], v[66:67], v[208:209] op_sel_hi:[1,0]\n"
"v_pk_mul_f32 v[68:69], v[68:69], v[208:209] op_sel_hi:[1,0]\n"
"v_pk_mul_f32 v[70:71], v[70:71], v[208:209] op_sel_hi:[1,0]\n"
"v_cvt_pk_bf16_f32 v64, v64, v65\n"
"v_cvt_pk_bf16_f32 v65, v66, v67\n"
"v_mfma_f32_16x16x32_bf16 v[248:251], a[32:35], a[96:99], -4.0\n"
"v_exp_f32 v198, v198\n"
"v_mfma_f32_16x16x32_bf16 v[248:251], a[36:39], a[100:103], v[248:251]\n"
"v_exp_f32 v199, v199\n"
"v_mfma_f32_16x16x32_bf16 v[248:251], a[40:43], a[104:107], v[248:251]\n"
"ds_read_b128 v[228:231], v226 offset:34816\n"
"v_cvt_pk_bf16_f32 v189, v198, v199\n"
"v_mfma_f32_16x16x32_bf16 v[248:251], a[44:47], a[108:111], v[248:251]\n"
"ds_read_b128 v[232:235], v226 offset:35840\n"
"v_cvt_pk_bf16_f32 v66, v68, v69\n"
"v_mfma_f32_16x16x32_bf16 v[252:255], a[44:47], a[124:127], -4.0\n"
"v_cvt_pk_bf16_f32 v67, v70, v71\n"
"v_mul_f32 v96, v96, v208\n"
"v_mfma_f32_16x16x32_bf16 v[252:255], a[40:43], a[120:123], v[252:255]\n"
"v_mul_f32 v97, v97, v208\n"
"v_mul_f32 v98, v98, v208\n"
"v_mfma_f32_16x16x32_bf16 v[252:255], a[36:39], a[116:119], v[252:255]\n"
"v_mul_f32 v99, v99, v208\n"
"v_mul_f32 v100, v100, v208\n"
"v_mfma_f32_16x16x32_bf16 v[252:255], a[32:35], a[112:115], v[252:255]\n"
"v_exp_f32 v248, v248\n"
"v_mfma_f32_16x16x32_bf16 v[76:79], a[72:75], v[168:171], v[76:79]\n"
"v_exp_f32 v249, v249\n"
"v_mfma_f32_16x16x32_bf16 v[76:79], a[76:79], v[172:175], v[76:79]\n"
"v_cvt_pk_bf16_f32 v182, v248, v249\n"
"v_mul_f32 v101, v101, v208\n"
"v_mfma_f32_16x16x32_bf16 v[72:75], a[68:71], v[172:175], v[72:75]\n"
"v_exp_f32 v250, v250\n"
"v_mfma_f32_16x16x32_bf16 v[72:75], a[64:67], v[168:171], v[72:75]\n"
"s_waitcnt lgkmcnt(1)\n"
"buffer_store_dwordx4 v[228:231], v223, s[56:59], s70 offen offset:0 sc1 nt\n"
"v_exp_f32 v251, v251\n"
"s_nop 0\n"
"v_cvt_pk_bf16_f32 v183, v250, v251\n"
"v_pk_mul_f32 v[102:103], v[102:103], v[208:209] op_sel_hi:[1,0]\n"
"v_cvt_pk_bf16_f32 v96, v96, v97\n"
"v_cvt_pk_bf16_f32 v97, v98, v99\n"
"v_mfma_f32_16x16x32_bf16 v[108:111], a[88:91], v[168:171], v[108:111]\n"
"v_exp_f32 v252, v252\n"
"v_mfma_f32_16x16x32_bf16 v[108:111], a[92:95], v[172:175], v[108:111]\n"
"s_waitcnt lgkmcnt(0)\n"
"buffer_store_dwordx4 v[232:235], v223, s[56:59], s70 offen offset:2048 sc1 nt\n"
"v_exp_f32 v253, v253\n"
"s_nop 0\n"
"v_cvt_pk_bf16_f32 v190, v252, v253\n"
"v_mfma_f32_16x16x32_bf16 v[104:107], a[84:87], v[172:175], v[104:107]\n"
"v_exp_f32 v254, v254\n"
"v_mfma_f32_16x16x32_bf16 v[104:107], a[80:83], v[168:171], v[104:107]\n"
"v_exp_f32 v255, v255\n"
"s_nop 0\n"
"v_cvt_pk_bf16_f32 v191, v254, v255\n"
"v_mfma_f32_16x16x32_bf16 v[16:19], a[160:163], v[144:147], v[16:19]\n"
"v_cvt_pk_bf16_f32 v98, v100, v101\n"
"v_cvt_pk_bf16_f32 v99, v102, v103\n"
"v_mfma_f32_16x16x32_bf16 v[16:19], a[164:167], v[148:151], v[16:19]\n"
"ds_write_b128 v224, v[64:67] offset:32768\n"
"v_mfma_f32_16x16x32_bf16 v[20:23], a[172:175], v[148:151], v[20:23]\n"
"v_mul_f32 v72, v72, v210\n"
"v_mul_f32 v73, v73, v210\n"
"v_mfma_f32_16x16x32_bf16 v[20:23], a[168:171], v[144:147], v[20:23]\n"
"ds_write_b128 v225, v[96:99] offset:32768\n"
"v_mfma_f32_16x16x32_bf16 v[48:51], a[176:179], v[144:147], v[48:51]\n"
"v_mul_f32 v74, v74, v210\n"
"v_mul_f32 v75, v75, v210\n"
"v_mfma_f32_16x16x32_bf16 v[48:51], a[180:183], v[148:151], v[48:51]\n"
"ds_read_b128 v[236:239], v226 offset:32768\n"
"v_mul_f32 v76, v76, v210\n"
"v_mfma_f32_16x16x32_bf16 v[52:55], a[188:191], v[148:151], v[52:55]\n"
"ds_read_b128 v[240:243], v226 offset:33792\n"
"v_mul_f32 v77, v77, v210\n"
"v_mfma_f32_16x16x32_bf16 v[52:55], a[184:187], v[144:147], v[52:55]\n"
"v_mul_f32 v78, v78, v210\n"
"v_mul_f32 v79, v79, v210\n"
"v_mfma_f32_16x16x32_bf16 v[80:83], a[48:51], v[144:147], v[80:83]\n"
"v_cvt_pk_bf16_f32 v72, v72, v73\n"
"v_cvt_pk_bf16_f32 v73, v74, v75\n"
"v_mfma_f32_16x16x32_bf16 v[80:83], a[52:55], v[148:151], v[80:83]\n"
"v_cvt_pk_bf16_f32 v74, v76, v77\n"
"v_cvt_pk_bf16_f32 v75, v78, v79\n"
"v_mfma_f32_16x16x32_bf16 v[84:87], a[60:63], v[148:151], v[84:87]\n"
"v_mul_f32 v104, v104, v210\n"
"v_mul_f32 v105, v105, v210\n"
"v_mfma_f32_16x16x32_bf16 v[84:87], a[56:59], v[144:147], v[84:87]\n"
"v_mul_f32 v106, v106, v210\n"
"v_mul_f32 v107, v107, v210\n"
"v_mfma_f32_16x16x32_bf16 v[112:115], a[144:147], v[144:147], v[112:115]\n"
"v_mul_f32 v108, v108, v210\n"
"v_mul_f32 v109, v109, v210\n"
"v_mfma_f32_16x16x32_bf16 v[112:115], a[148:151], v[148:151], v[112:115]\n"
"s_waitcnt lgkmcnt(1)\n"
"buffer_store_dwordx4 v[236:239], v223, s[56:59], s99 offen offset:128 sc1 nt\n"
"v_mul_f32 v110, v110, v210\n"
"v_mul_f32 v111, v111, v210\n"
"v_cvt_pk_bf16_f32 v104, v104, v105\n"
"v_mfma_f32_16x16x32_bf16 v[116:119], a[156:159], v[148:151], v[116:119]\n"
"v_cvt_pk_bf16_f32 v105, v106, v107\n"
"v_cvt_pk_bf16_f32 v106, v108, v109\n"
"v_mfma_f32_16x16x32_bf16 v[116:119], a[152:155], v[144:147], v[116:119]\n"
"s_waitcnt lgkmcnt(0)\n"
"buffer_store_dwordx4 v[240:243], v223, s[56:59], s99 offen offset:2176 sc1 nt\n"
"v_cvt_pk_bf16_f32 v107, v110, v111\n"
"v_smfmac_f32_16x16x64_bf16 a[200:203], a[208:211], v[176:183], v215\n"
"ds_write_b128 v224, v[72:75] offset:34816\n"
"v_smfmac_f32_16x16x64_bf16 a[204:207], a[208:211], v[184:191], v215\n"
"ds_write_b128 v225, v[104:107] offset:34816\n"
"v_mfma_f32_16x16x32_bf16 v[16:19], a[216:219], v[176:179], v[16:19]\n"
"v_mfma_f32_16x16x32_bf16 v[16:19], a[220:223], v[180:183], v[16:19]\n"
"ds_read_b128 v[244:247], v226 offset:34816\n"
"s_nop 1\n"
"v_accvgpr_read_b32 v212, a200\n"
"v_mfma_f32_16x16x32_bf16 v[20:23], a[228:231], v[180:183], v[20:23]\n"
"ds_read_b128 a[248:251], v226 offset:35840\n"
"v_accvgpr_read_b32 v213, a201\n"
"v_mfma_f32_16x16x32_bf16 v[20:23], a[224:227], v[176:179], v[20:23]\n"
"v_add_f32 v212, v212, v213\n"
"v_accvgpr_read_b32 v214, a204\n"
"v_mfma_f32_16x16x32_bf16 v[48:51], a[232:235], v[176:179], v[48:51]\n"
"v_accvgpr_read_b32 v213, a205\n"
"v_add_f32 v214, v214, v213\n"
"v_mfma_f32_16x16x32_bf16 v[48:51], a[236:239], v[180:183], v[48:51]\n"
"v_max3_u32 v209, v209, v212, v214\n"
"v_min3_u32 v211, v211, v212, v214\n"
"v_mfma_f32_16x16x32_bf16 v[52:55], a[244:247], v[180:183], v[52:55]\n"
"v_rcp_f32 v212, v212\n"
"v_mfma_f32_16x16x32_bf16 v[52:55], a[240:243], v[176:179], v[52:55]\n"
"v_rcp_f32 v214, v214\n"
"v_mfma_f32_16x16x32_bf16 v[28:31], a[168:171], v[152:155], v[28:31]\n"
"v_mul_f32 v16, v16, v212\n"
"v_mul_f32 v17, v17, v212\n"
"v_mfma_f32_16x16x32_bf16 v[28:31], a[172:175], v[156:159], v[28:31]\n"
"s_waitcnt lgkmcnt(1)\n"
"buffer_store_dwordx4 v[244:247], v223, s[56:59], s70 offen offset:128 sc1 nt\n"
"v_mul_f32 v18, v18, v212\n"
"v_mul_f32 v19, v19, v212\n"
"v_mul_f32 v20, v20, v212\n"
"v_mfma_f32_16x16x32_bf16 v[24:27], a[164:167], v[156:159], v[24:27]\n"
"v_mul_f32 v21, v21, v212\n"
"v_mul_f32 v22, v22, v212\n"
"v_mfma_f32_16x16x32_bf16 v[24:27], a[160:163], v[152:155], v[24:27]\n"
"s_waitcnt lgkmcnt(0)\n"
"buffer_store_dwordx4 a[248:251], v223, s[56:59], s70 offen offset:2176 sc1 nt\n"
"v_mul_f32 v23, v23, v212\n"
"v_cvt_pk_bf16_f32 v16, v16, v17\n"
"v_cvt_pk_bf16_f32 v17, v18, v19\n"
"v_mfma_f32_16x16x32_bf16 v[60:63], a[184:187], v[152:155], v[60:63]\n"
"v_cvt_pk_bf16_f32 v18, v20, v21\n"
"v_cvt_pk_bf16_f32 v19, v22, v23\n"
"v_mfma_f32_16x16x32_bf16 v[60:63], a[188:191], v[156:159], v[60:63]\n"
"v_mul_f32 v48, v48, v212\n"
"v_mul_f32 v49, v49, v212\n"
"v_mfma_f32_16x16x32_bf16 v[56:59], a[180:183], v[156:159], v[56:59]\n"
"v_mul_f32 v50, v50, v212\n"
"v_mul_f32 v51, v51, v212\n"
"v_mfma_f32_16x16x32_bf16 v[56:59], a[176:179], v[152:155], v[56:59]\n"
"v_mul_f32 v52, v52, v212\n"
"v_mul_f32 v53, v53, v212\n"
"v_mfma_f32_16x16x32_bf16 v[80:83], a[64:67], v[176:179], v[80:83]\n"
"v_mul_f32 v54, v54, v212\n"
"v_mul_f32 v55, v55, v212\n"
"v_mfma_f32_16x16x32_bf16 v[80:83], a[68:71], v[180:183], v[80:83]\n"
"v_cvt_pk_bf16_f32 v48, v48, v49\n"
"v_cvt_pk_bf16_f32 v49, v50, v51\n"
"v_mfma_f32_16x16x32_bf16 v[84:87], a[76:79], v[180:183], v[84:87]\n"
"v_cvt_pk_bf16_f32 v50, v52, v53\n"
"v_cvt_pk_bf16_f32 v51, v54, v55\n"
"v_mfma_f32_16x16x32_bf16 v[84:87], a[72:75], v[176:179], v[84:87]\n"
"ds_write_b128 v224, v[16:19] offset:32768\n"
"v_mfma_f32_16x16x32_bf16 v[112:115], a[80:83], v[176:179], v[112:115]\n"
"v_mfma_f32_16x16x32_bf16 v[112:115], a[84:87], v[180:183], v[112:115]\n"
"ds_write_b128 v225, v[48:51] offset:32768\n"
"v_mfma_f32_16x16x32_bf16 v[116:119], a[92:95], v[180:183], v[116:119]\n"
"v_mul_f32 v80, v80, v212\n"
"v_mul_f32 v81, v81, v212\n"
"v_mfma_f32_16x16x32_bf16 v[116:119], a[88:91], v[176:179], v[116:119]\n"
"ds_read_b128 a[252:255], v226 offset:32768\n"
"v_mul_f32 v82, v82, v212\n"
"v_mfma_f32_16x16x32_bf16 v[28:31], a[224:227], v[184:187], v[28:31]\n"
".Lattn_pad15:\n"
"s_cbranch_vccnz .Lattn_ret\n"
"ds_read_b128 v[200:203], v226 offset:33792\n"
"v_mul_f32 v83, v83, v212\n"
"v_mfma_f32_16x16x32_bf16 v[28:31], a[228:231], v[188:191], v[28:31]\n"
"v_mul_f32 v84, v84, v212\n"
"v_mul_f32 v85, v85, v212\n"
"v_mfma_f32_16x16x32_bf16 v[24:27], a[220:223], v[188:191], v[24:27]\n"
"v_mul_f32 v86, v86, v212\n"
"v_mul_f32 v87, v87, v212\n"
"v_mfma_f32_16x16x32_bf16 v[24:27], a[216:219], v[184:187], v[24:27]\n"
"v_cvt_pk_bf16_f32 v80, v80, v81\n"
"v_cvt_pk_bf16_f32 v81, v82, v83\n"
"v_mfma_f32_16x16x32_bf16 v[60:63], a[240:243], v[184:187], v[60:63]\n"
"v_cvt_pk_bf16_f32 v82, v84, v85\n"
"v_cvt_pk_bf16_f32 v83, v86, v87\n"
"v_mfma_f32_16x16x32_bf16 v[60:63], a[244:247], v[188:191], v[60:63]\n"
"v_mul_f32 v112, v112, v212\n"
"v_mul_f32 v113, v113, v212\n"
"v_mfma_f32_16x16x32_bf16 v[56:59], a[236:239], v[188:191], v[56:59]\n"
"v_mul_f32 v114, v114, v212\n"
"v_mul_f32 v115, v115, v212\n"
"v_mfma_f32_16x16x32_bf16 v[56:59], a[232:235], v[184:187], v[56:59]\n"
"s_waitcnt lgkmcnt(1)\n"
"buffer_store_dwordx4 a[252:255], v223, s[56:59], s71 offen offset:0 sc1 nt\n"
"v_mul_f32 v116, v116, v212\n"
"v_mul_f32 v117, v117, v212\n"
"v_mul_f32 v118, v118, v212\n"
"v_mfma_f32_16x16x32_bf16 v[92:95], a[56:59], v[152:155], v[92:95]\n"
"v_mul_f32 v119, v119, v212\n"
"v_cvt_pk_bf16_f32 v112, v112, v113\n"
"v_mfma_f32_16x16x32_bf16 v[92:95], a[60:63], v[156:159], v[92:95]\n"
"s_waitcnt lgkmcnt(0)\n"
"buffer_store_dwordx4 v[200:203], v223, s[56:59], s71 offen offset:2048 sc1 nt\n"
"v_cvt_pk_bf16_f32 v113, v114, v115\n"
"v_cvt_pk_bf16_f32 v114, v116, v117\n"
"v_cvt_pk_bf16_f32 v115, v118, v119\n"
"v_mfma_f32_16x16x32_bf16 v[88:91], a[52:55], v[156:159], v[88:91]\n"
"v_mul_f32 v24, v24, v214\n"
"v_mul_f32 v25, v25, v214\n"
"v_mfma_f32_16x16x32_bf16 v[88:91], a[48:51], v[152:155], v[88:91]\n"
"ds_write_b128 v224, v[80:83] offset:34816\n"
"v_mfma_f32_16x16x32_bf16 v[92:95], a[72:75], v[184:187], v[92:95]\n"
"v_mul_f32 v26, v26, v214\n"
"v_mul_f32 v27, v27, v214\n"
"v_mfma_f32_16x16x32_bf16 v[92:95], a[76:79], v[188:191], v[92:95]\n"
"ds_write_b128 v225, v[112:115] offset:34816\n"
"v_mfma_f32_16x16x32_bf16 v[88:91], a[68:71], v[188:191], v[88:91]\n"
"v_mul_f32 v28, v28, v214\n"
"v_mul_f32 v29, v29, v214\n"
"v_mfma_f32_16x16x32_bf16 v[88:91], a[64:67], v[184:187], v[88:91]\n"
"ds_read_b128 v[204:207], v226 offset:34816\n"
"v_mul_f32 v30, v30, v214\n"
"v_mfma_f32_16x16x32_bf16 v[124:127], a[152:155], v[152:155], v[124:127]\n"
"ds_read_b128 v[228:231], v226 offset:35840\n"
"v_mul_f32 v31, v31, v214\n"
"v_mfma_f32_16x16x32_bf16 v[124:127], a[156:159], v[156:159], v[124:127]\n"
"v_cvt_pk_bf16_f32 v24, v24, v25\n"
"v_cvt_pk_bf16_f32 v25, v26, v27\n"
"v_cvt_pk_bf16_f32 v26, v28, v29\n"
"v_cvt_pk_bf16_f32 v27, v30, v31\n"
"v_mul_f32 v56, v56, v214\n"
"v_mul_f32 v57, v57, v214\n"
"v_pk_mul_f32 v[58:59], v[58:59], v[214:215] op_sel_hi:[1,0]\n"
"v_pk_mul_f32 v[60:61], v[60:61], v[214:215] op_sel_hi:[1,0]\n"
"v_pk_mul_f32 v[62:63], v[62:63], v[214:215] op_sel_hi:[1,0]\n"
"v_cvt_pk_bf16_f32 v56, v56, v57\n"
"v_cvt_pk_bf16_f32 v57, v58, v59\n"
"v_mfma_f32_16x16x32_bf16 v[120:123], a[148:151], v[156:159], v[120:123]\n"
"v_cvt_pk_bf16_f32 v58, v60, v61\n"
"v_cvt_pk_bf16_f32 v59, v62, v63\n"
"v_mfma_f32_16x16x32_bf16 v[120:123], a[144:147], v[152:155], v[120:123]\n"
"ds_write_b128 v224, v[24:27] offset:32768\n"
"v_mfma_f32_16x16x32_bf16 v[124:127], a[88:91], v[184:187], v[124:127]\n"
"v_mul_f32 v88, v88, v214\n"
"v_mul_f32 v89, v89, v214\n"
"v_mfma_f32_16x16x32_bf16 v[124:127], a[92:95], v[188:191], v[124:127]\n"
"ds_write_b128 v225, v[56:59] offset:32768\n"
"v_mfma_f32_16x16x32_bf16 v[120:123], a[84:87], v[188:191], v[120:123]\n"
"v_mul_f32 v90, v90, v214\n"
"v_mul_f32 v91, v91, v214\n"
"v_mfma_f32_16x16x32_bf16 v[120:123], a[80:83], v[184:187], v[120:123]\n"
"s_waitcnt lgkmcnt(3)\n"
"buffer_store_dwordx4 v[204:207], v223, s[56:59], s71 offen offset:128 sc1 nt\n"
"v_mul_f32 v92, v92, v214\n"
"v_mul_f32 v93, v93, v214\n"
"v_mul_f32 v94, v94, v214\n"
"s_waitcnt lgkmcnt(2)\n"
"buffer_store_dwordx4 v[228:231], v223, s[56:59], s71 offen offset:2176 sc1 nt\n"
"ds_read_b128 v[232:235], v226 offset:32768\n"
"ds_read_b128 v[236:239], v226 offset:33792\n"
"v_mul_f32 v95, v95, v214\n"
"v_cvt_pk_bf16_f32 v88, v88, v89\n"
"v_cvt_pk_bf16_f32 v89, v90, v91\n"
"v_cvt_pk_bf16_f32 v90, v92, v93\n"
"v_cvt_pk_bf16_f32 v91, v94, v95\n"
"v_pk_mul_f32 v[120:121], v[120:121], v[214:215] op_sel_hi:[1,0]\n"
"v_pk_mul_f32 v[122:123], v[122:123], v[214:215] op_sel_hi:[1,0]\n"
"v_pk_mul_f32 v[124:125], v[124:125], v[214:215] op_sel_hi:[1,0]\n"
"v_pk_mul_f32 v[126:127], v[126:127], v[214:215] op_sel_hi:[1,0]\n"
"v_cvt_pk_bf16_f32 v120, v120, v121\n"
"v_cvt_pk_bf16_f32 v121, v122, v123\n"
"v_cvt_pk_bf16_f32 v122, v124, v125\n"
"v_cvt_pk_bf16_f32 v123, v126, v127\n"
"ds_write_b128 v224, v[88:91] offset:34816\n"
"ds_write_b128 v225, v[120:123] offset:34816\n"
"s_waitcnt lgkmcnt(3)\n"
"buffer_store_dwordx4 v[232:235], v223, s[56:59], s37 offen offset:0 sc1 nt\n"
"s_waitcnt lgkmcnt(2)\n"
"buffer_store_dwordx4 v[236:239], v223, s[56:59], s37 offen offset:2048 sc1 nt\n"
"ds_read_b128 v[240:243], v226 offset:34816\n"
"ds_read_b128 v[244:247], v226 offset:35840\n"
"s_waitcnt lgkmcnt(1)\n"
"buffer_store_dwordx4 v[240:243], v223, s[56:59], s37 offen offset:128 sc1 nt\n"
"s_waitcnt lgkmcnt(0)\n"
"buffer_store_dwordx4 v[244:247], v223, s[56:59], s37 offen offset:2176 sc1 nt\n"
"v_cmp_lt_u32 vcc, 0x53800000, v209\n"
"s_nop 3\n"
"s_mov_b64 s[74:75], vcc\n"
"v_cmp_gt_u32 vcc, 0x2b800000, v211\n"
"s_nop 3\n"
"s_or_b64 s[74:75], s[74:75], vcc\n"
"s_or_b32 %[bad], s74, s75\n"
".Lattn_end:\n"
      : [bad] "=&s"(bad)
      : [ka] "s"(__builtin_amdgcn_kernarg_segment_ptr()), [wg] "s"(wg), [wv] "s"(wv), [lds] "s"(ldsb)
      : "v0","v1","v2","v3","v4","v5","v6","v7","v8","v9","v10","v11","v12","v13","v14","v15","v16","v17","v18","v19","v20","v21","v22","v23","v24","v25","v26","v27","v28","v29","v30","v31","v32","v33","v34","v35","v36","v37","v38","v39","v40","v41","v42","v43","v44","v45","v46","v47","v48","v49","v50","v51","v52","v53","v54","v55","v56","v57","v58","v59","v60","v61","v62","v63","v64","v65","v66","v67","v68","v69","v70","v71","v72","v73","v74","v75","v76","v77","v78","v79","v80","v81","v82","v83","v84","v85","v86","v87","v88","v89","v90","v91","v92","v93","v94","v95","v96","v97","v98","v99","v100","v101","v102","v103","v104","v105","v106","v107","v108","v109","v110","v111","v112","v113","v114","v115","v116","v117","v118","v119","v120","v121","v122","v123","v124","v125","v126","v127","v128","v129","v130","v131","v132","v133","v134","v135","v136","v137","v138","v139","v140","v141","v142","v143","v144","v145","v146","v147","v148","v149","v150","v151","v152","v153","v154","v155","v156","v157","v158","v159","v160","v161","v162","v163","v164","v165","v166","v167","v168","v169","v170","v171","v172","v173","v174","v175","v176","v177","v178","v179","v180","v181","v182","v183","v184","v185","v186","v187","v188","v189","v190","v191","v192","v193","v194","v195","v196","v197","v198","v199","v200","v201","v202","v203","v204","v205","v206","v207","v208","v209","v210","v211","v212","v213","v214","v215","v216","v217","v218","v219","v220","v221","v222","v223","v224","v225","v226","v227","v228","v229","v230","v231","v232","v233","v234","v235","v236","v237","v238","v239","v240","v241","v242","v243","v244","v245","v246","v247","v248","v249","v250","v251","v252","v253","v254","v255","a0","a1","a2","a3","a4","a5","a6","a7","a8","a9","a10","a11","a12","a13","a14","a15","a16","a17","a18","a19","a20","a21","a22","a23","a24","a25","a26","a27","a28","a29","a30","a31","a32","a33","a34","a35","a36","a37","a38","a39","a40","a41","a42","a43","a44","a45","a46","a47","a48","a49","a50","a51","a52","a53","a54","a55","a56","a57","a58","a59","a60","a61","a62","a63","a64","a65","a66","a67","a68","a69","a70","a71","a72","a73","a74","a75","a76","a77","a78","a79","a80","a81","a82","a83","a84","a85","a86","a87","a88","a89","a90","a91","a92","a93","a94","a95","a96","a97","a98","a99","a100","a101","a102","a103","a104","a105","a106","a107","a108","a109","a110","a111","a112","a113","a114","a115","a116","a117","a118","a119","a120","a121","a122","a123","a124","a125","a126","a127","a128","a129","a130","a131","a132","a133","a134","a135","a136","a137","a138","a139","a140","a141","a142","a143","a144","a145","a146","a147","a148","a149","a150","a151","a152","a153","a154","a155","a156","a157","a158","a159","a160","a161","a162","a163","a164","a165","a166","a167","a168","a169","a170","a171","a172","a173","a174","a175","a176","a177","a178","a179","a180","a181","a182","a183","a184","a185","a186","a187","a188","a189","a190","a191","a192","a193","a194","a195","a196","a197","a198","a199","a200","a201","a202","a203","a204","a205","a206","a207","a208","a209","a210","a211","a212","a213","a214","a215","a216","a217","a218","a219","a220","a221","a222","a223","a224","a225","a226","a227","a228","a229","a230","a231","a232","a233","a234","a235","a236","a237","a238","a239","a240","a241","a242","a243","a244","a245","a246","a247","a248","a249","a250","a251","a252","a253","a254","a255","s34","s35","s36","s37","s38","s39","s40","s41","s42","s43","s44","s45","s46","s47","s48","s49","s50","s51","s52","s53","s54","s55","s56","s57","s58","s59","s60","s61","s62","s63","s64","s65","s66","s67","s68","s69","s70","s71","s72","s73","s74","s75","s76","s77","s78","s79","s80","s81","s82","s83","s84","s85","s86","s87","s88","s89","s90","s91","s92","s93","s94","s95","s96","s97","s98","s99","vcc","scc","memory");
  if (__builtin_expect(bad != 0, 0)) {
    asm volatile("s_waitcnt vmcnt(0)" ::: "memory");
    const unsigned lane = __builtin_amdgcn_mbcnt_hi(~0u, __builtin_amdgcn_mbcnt_lo(~0u, 0u));
#pragma unroll 1
    for (int it = 0; it < 24; ++it) {
      const unsigned idx = (wg >> 3) + 32u * (it >> 2), head = (wg & 7u) * 64u + idx / 3u;
      const size_t qoff = (size_t)((wg & 7u) * 192u + idx) * 65536u + wv * 16384u + (it & 3) * 4096u;
      slow_rows(Q + qoff, K + (size_t)head * 131072u, V + (size_t)head * 131072u, O + qoff, lds + 131072 + wv * 8192u, lane);
    }
  }
}

extern "C" void kernel_launch(void* const* d_in, const int* in_sizes, int n_in, void* d_out, int out_size, void* d_ws, size_t ws_size, hipStream_t stream) {
  (void)hipFuncSetAttribute((const void*)gqa_attn_fwd, hipFuncAttributeMaxDynamicSharedMemorySize, 163840);
  hipLaunchKernelGGL(gqa_attn_fwd, dim3(256), dim3(256), 163840, stream, (const char*)d_in[0], (const char*)d_in[1], (const char*)d_in[2], (char*)d_out
  );
}
```
